```python
import torch

def _softmax(x, axis=-1):
    return torch.softmax(x, dim=axis)
def _gelu(x):
    return torch.nn.functional.gelu(x, approximate="tanh")
def _silu(x):
    return torch.nn.functional.silu(x)
def _sigmoid(x):
    return torch.sigmoid(x)
def _layernorm(x, axis=-1, eps=1e-5):
    m = x.mean(dim=axis, keepdim=True)
    v = x.var(dim=axis, keepdim=True, unbiased=False)
    return (x - m) / torch.sqrt(v + eps)
def _scatter_add(tgt, idx, src, axis=0):
    return tgt.clone().index_add_(axis, idx, src)
def _segment_sum(src, seg, n):
    out = torch.zeros((n,) + src.shape[1:], dtype=src.dtype)
    return out.index_add_(0, seg, src)
def _pad(x, pw):
    flat = tuple(v for pair in reversed(pw) for v in pair)
    return torch.nn.functional.pad(x, flat)

kernel_name = 'recurrent_scan__softmax_full_mha__glu_gated_swiglu__prompt'

def setup_inputs():

    _w = (lambda a: (lambda t: t.to(torch.bfloat16) if t.is_floating_point() else t)(torch.as_tensor(a)))
    def _A_setup():
        B, S, D = 1, 2048, 448
        return {
            "x": _w((lambda *s: torch.randn(*s, dtype=torch.bfloat16) * 0.05)(B, S, D)),
            "wqkv": _w((lambda *s: torch.randn(*s, dtype=torch.bfloat16) * 0.2)(D, 3 * D)),
            "wo": _w((lambda *s: torch.randn(*s, dtype=torch.bfloat16) * 0.2)(D, D)),
        }
    _A_in = _A_setup()

    def _B_setup():
        B, S, D, F = 1, 2048, 448, 1792
        return {
            "x": _w((lambda *s: torch.randn(*s, dtype=torch.bfloat16) * 0.05)(B, S, D)),
            "w1": _w((lambda *s: torch.randn(*s, dtype=torch.bfloat16) * 0.1)(D, F) * (2.0 / D) ** 0.5),
            "w3": _w((lambda *s: torch.randn(*s, dtype=torch.bfloat16) * 0.1)(D, F) * (2.0 / D) ** 0.5),
            "w2": _w((lambda *s: torch.randn(*s, dtype=torch.bfloat16) * 0.1)(F, D) * (2.0 / F) ** 0.5),
        }
    _B_in = _B_setup()

    return {
        "A_x": _A_in["x"],
        "A_wqkv": _A_in["wqkv"],
        "A_wo": _A_in["wo"],
        "B_x": _B_in["x"],
        "B_w1": _B_in["w1"],
        "B_w3": _B_in["w3"],
        "B_w2": _B_in["w2"],
    }

def reference(A_x, A_wqkv, A_wo, B_x, B_w1, B_w3, B_w2):
    def _A_ref(x, wqkv, wo):
        B, S, D = x.shape
        H = 16; Dh = D // H
        qkv = (lambda x, *d: x.permute(*d))((x @ wqkv).reshape(B, S, 3, H, Dh), 2, 0, 3, 1, 4)
        q, k, v = qkv[0], qkv[1], qkv[2]
        scale = Dh ** -0.5
        scores = torch.einsum("bhqd,bhkd->bhqk", q, k) * scale
        p = _softmax(scores, axis=-1)
        o = torch.einsum("bhqk,bhkd->bhqd", p, v)
        return (lambda x, *d: x.permute(*d))(o, 0, 2, 1, 3).reshape(B, S, D) @ wo

    def _B_ref(x, w1, w3, w2):
        gate = torch.einsum("bsd,df->bsf", x, w1)
        up = torch.einsum("bsd,df->bsf", x, w3)
        h = _silu(gate) * up
        out = torch.einsum("bsf,fd->bsd", h, w2)
        return out

    x = A_x
    xn = _layernorm(x, axis=-1)
    s = _A_ref(xn, A_wqkv, A_wo)
    g = _silu(_B_ref(xn, B_w1, B_w3, B_w2))
    return x + s * g
```

```cpp
#include <hip/hip_runtime.h>
#include <hip/hip_bf16.h>
typedef unsigned short u16;
__device__ __forceinline__ float bf2f(u16 v){ return __uint_as_float(((unsigned)v)<<16); }
__device__ __forceinline__ u16 f2bf(float f){ __hip_bfloat16 b = __float2bfloat16(f); return *(u16*)&b; }
constexpr int S=2048, D=448, H=16, DH=28, F=1792;
__global__ void k_ln(const u16* x, float* xn){
  int r=blockIdx.x; int t=threadIdx.x;
  float v[7]; float s=0;
  for(int i=0;i<7;i++){ v[i]=bf2f(x[r*D+t+64*i]); s+=v[i]; }
  for(int o=32;o;o>>=1) s+=__shfl_xor(s,o);
  float m=s/D; float q=0;
  for(int i=0;i<7;i++){ float d=v[i]-m; q+=d*d; }
  for(int o=32;o;o>>=1) q+=__shfl_xor(q,o);
  float rs=rsqrtf(q/D+1e-5f);
  for(int i=0;i<7;i++) xn[r*D+t+64*i]=(v[i]-m)*rs;
}
__global__ void k_gemm(const float* A, const u16* W, float* C, int K, int N, int rnd){
  int n=blockIdx.x*64+threadIdx.x; int m0=blockIdx.y*8;
  float acc[8]={0,0,0,0,0,0,0,0};
  for(int k=0;k<K;k++){ float w=bf2f(W[k*N+n]);
    #pragma unroll
    for(int i=0;i<8;i++) acc[i]+=A[(m0+i)*K+k]*w; }
  for(int i=0;i<8;i++) C[(m0+i)*N+n]= rnd? bf2f(f2bf(acc[i])) : acc[i];
}
__global__ void k_h(const float* g, const float* u, float* h){
  int i=blockIdx.x*256+threadIdx.x; float a=g[i]; h[i]=bf2f(f2bf(a/(1.f+__expf(-a))*u[i]));
}
__global__ void k_attn(const float* qkv, float* o){
  int q=blockIdx.x*64+threadIdx.x; int h=blockIdx.y;
  float qv[DH], acc[DH];
  #pragma unroll
  for(int d=0;d<DH;d++){ qv[d]=qkv[q*3*D+h*DH+d]*0.1889822365046136f; acc[d]=0; }
  float m=-1e30f,l=0;
  for(int k=0;k<S;k++){
    float s=0;
    #pragma unroll
    for(int d=0;d<DH;d++) s+=qv[d]*qkv[k*3*D+D+h*DH+d];
    float mn=fmaxf(m,s); float a=__expf(m-mn), p=__expf(s-mn); l=l*a+p; m=mn;
    #pragma unroll
    for(int d=0;d<DH;d++) acc[d]=acc[d]*a+p*qkv[k*3*D+2*D+h*DH+d];
  }
  #pragma unroll
  for(int d=0;d<DH;d++) o[q*D+h*DH+d]=bf2f(f2bf(acc[d]/l));
}
__global__ void k_fin(const u16* x, const float* s, const float* f, u16* out){
  int i=blockIdx.x*256+threadIdx.x; float a=f[i]; out[i]=f2bf(bf2f(x[i])+s[i]*(a/(1.f+__expf(-a))));
}
extern "C" void kernel_launch(void* const* d_in, const int* in_sizes, int n_in, void* d_out, int out_size, void* d_ws, size_t ws_size, hipStream_t stream) {
  const u16* x=(const u16*)d_in[0]; const u16* wqkv=(const u16*)d_in[1]; const u16* wo=(const u16*)d_in[2];
  const u16* w1=(const u16*)d_in[4]; const u16* w3=(const u16*)d_in[5]; const u16* w2=(const u16*)d_in[6];
  float* ws=(float*)d_ws;
  float* xn=ws; float* qkv=xn+S*D; float* g=qkv+S*3*D; float* u=g+S*F; float* h=u+S*F; float* o=h+S*F; float* s=o+S*D; float* f=s+S*D;
  k_ln<<<S,64,0,stream>>>(x,xn);
  k_gemm<<<dim3(3*D/64,S/8),64,0,stream>>>(xn,wqkv,qkv,D,3*D,1);
  k_gemm<<<dim3(F/64,S/8),64,0,stream>>>(xn,w1,g,D,F,0);
  k_gemm<<<dim3(F/64,S/8),64,0,stream>>>(xn,w3,u,D,F,0);
  k_h<<<S*F/256,256,0,stream>>>(g,u,h);
  k_attn<<<dim3(S/64,H),64,0,stream>>>(qkv,o);
  k_gemm<<<dim3(D/64,S/8),64,0,stream>>>(o,wo,s,D,D,0);
  k_gemm<<<dim3(D/64,S/8),64,0,stream>>>(h,w2,f,F,D,0);
  k_fin<<<S*D/256,256,0,stream>>>(x,s,f,(u16*)d_out);
}
```

```cpp
#include <hip/hip_runtime.h>

struct Desc { const void* p; unsigned n; unsigned f; };
struct Args { Desc x, wqkv, w1, w3, w2, wo, out, ws; };

__global__ __launch_bounds__(512, 2) void fused_block(Args a) {
    __shared__ __attribute__((aligned(16))) char lds[163840];
    unsigned tid = threadIdx.x;
    asm volatile(
        "; lds %3\n"
"LS1_%=:\n"
"s_load_dwordx16 s[20:35], %0, 0\n"
"s_load_dwordx16 s[36:51], %0, 64\n"
"s_mov_b32 s64, %1\n"
"v_lshrrev_b32 v1, 6, v0\n"
"s_and_b32 s65, s64, 7\n"
"s_lshr_b32 s66, s64, 3\n"
"v_readfirstlane_b32 s69, v1\n"
"s_nop 0\n"
"s_cmp_lt_u32 s69, 4\n"
"s_cbranch_scc1 LA2_%=\n"
"s_setprio 3\n"
"s_lshl_b32 s65, s65, 2\n"
"s_lshr_b32 s65, 0x54763210, s65\n"
"s_and_b32 s65, s65, 7\n"
"s_and_b32 s87, s65, 1\n"
"s_and_b32 s84, s66, 7\n"
"s_lshl3_add_u32 s67, s87, s84\n"
"s_and_b32 s70, s69, 3\n"
"s_and_b32 s8, s66, 1\n"
"s_lshl1_add_u32 s8, s87, s8\n"
"s_lshl_b32 s8, s8, 1\n"
"s_lshr_b32 s85, s65, 1\n"
"s_add_u32 s4, s8, s85\n"
"s_sub_u32 s85, s4, 7\n"
"s_cselect_b32 s4, s4, s85\n"
"s_lshl_b32 s5, s4, 7\n"
"v_and_b32 v223, 63, v0\n"
"v_bfe_u32 v240, v0, 3, 3\n"
"v_and_b32 v241, 7, v0\n"
"v_bfe_u32 v242, v0, 4, 2\n"
"s_lshl3_add_u32 s84, s67, s70\n"
"s_mul_i32 s84, s84, 14336\n"
"s_lshl_b32 s81, s70, 11\n"
"v_xor_b32 v216, v241, v242\n"
"v_mul_u32_u24 v217, 896, v240\n"
"v_lshl_add_u32 v216, v216, 4, s84\n"
"v_add_u32 v216, v216, v217\n"
"v_xor_b32 v217, 64, v216\n"
"v_add_u32 v217, 7168, v217\n"
"s_lshr_b32 s85, s65, 1\n"
"s_lshr_b32 s86, s66, 3\n"
"s_xor_b32 s86, s86, 3\n"
"s_lshl2_add_u32 s68, s85, s86\n"
"s_lshl_b32 s80, s68, 7\n"
"s_mul_i32 s87, s85, 384\n"
"s_add_u32 s90, s87, s80\n"
"s_add_u32 s87, s90, 384\n"
"s_lshl_b32 s88, s85, 8\n"
"s_addk_i32 s88, 1792\n"
"s_add_u32 s89, s88, 128\n"
"s_cmp_eq_u32 s85, 3\n"
"s_cselect_b32 s89, 1073741824, s89\n"
"s_movk_i32 s84, 3584\n"
"s_cmp_eq_u32 s86, 3\n"
"s_cselect_b32 s76, 1, 0\n"
"s_cselect_b32 s77, 2688, s84\n"
"s_cselect_b32 s78, s88, s87\n"
"s_cselect_b32 s79, s89, s87\n"
"s_cselect_b32 s3, s87, s90\n"
"v_and_b32 v242, 1, v242\n"
"v_lshlrev_b32 v242, 2, v242\n"
"v_xor_b32 v242, v242, v241\n"
"v_lshlrev_b32 v242, 4, v242\n"
"s_movk_i32 s86, 2688\n"
"v_lshl_add_u32 v243, s70, 4, v240\n"
"v_mad_u32_u24 v218, v243, s86, v242\n"
"s_cmp_lt_u32 s68, 14\n"
"s_cselect_b32 s86, s80, 1073741824\n"
"v_add_u32 v218, s86, v218\n"
"s_mul_i32 s6, s4, 172032\n"
"s_waitcnt lgkmcnt(0)\n"
"s_add_u32 m0, s81, 0\n"
"s_nop 0\n"
"buffer_load_dwordx4 v216, s[20:23], s5 offen lds\n"
"s_add_u32 m0, s81, 1024\n"
"s_nop 0\n"
"buffer_load_dwordx4 v217, s[20:23], s5 offen lds\n"
"s_add_u32 m0, s81, 8192\n"
"s_add_u32 s91, s5, 57344\n"
"buffer_load_dwordx4 v216, s[20:23], s91 offen lds\n"
"s_add_u32 m0, s81, 9216\n"
"s_add_u32 s91, s5, 57344\n"
"buffer_load_dwordx4 v217, s[20:23], s91 offen lds\n"
"s_add_u32 m0, s81, 49152\n"
"s_nop 0\n"
"buffer_load_dwordx4 v218, s[24:27], s6 offen lds\n"
"v_lshl_add_u32 v243, s70, 3, v240\n"
"s_lshl_b32 s17, 16, s76\n"
"s_bitcmp1_b32 s70, 0\n"
"s_cselect_b32 s85, s17, 0\n"
"v_mad_u32_u24 v219, v243, s77, v242\n"
"v_xor_b32 v220, s85, v219\n"
"v_add_u32 v219, s78, v220\n"
"v_add_u32 v220, s79, v220\n"
"s_add_u32 m0, s81, 50176\n"
"s_add_u32 s91, s6, 21504\n"
"buffer_load_dwordx4 v218, s[24:27], s91 offen lds\n"
"s_lshl_b32 s12, s77, 5\n"
"s_lshl_b32 s9, s77, 6\n"
"s_mul_i32 s82, s4, s9\n"
"s_cmp_eq_u32 s76, 1\n"
"s_cselect_b64 s[52:53], s[24:25], s[28:29]\n"
"s_cselect_b64 s[54:55], s[26:27], s[30:31]\n"
"s_cselect_b64 s[56:57], s[24:25], s[32:33]\n"
"s_cselect_b64 s[58:59], s[26:27], s[34:35]\n"
"s_mov_b32 s10, s4\n"
"s_mul_i32 s7, s4, 229376\n"
"s_load_dword s71, s[48:49], 0\n"
"s_add_u32 m0, s81, 73728\n"
"s_nop 0\n"
"buffer_load_dwordx4 v219, s[52:55], s82 offen lds\n"
"s_add_u32 m0, s81, 74752\n"
"s_nop 0\n"
"buffer_load_dwordx4 v220, s[56:59], s82 offen lds\n"
"s_add_u32 s82, s82, s12\n"
"s_add_u32 m0, s81, 81920\n"
"s_nop 0\n"
"buffer_load_dwordx4 v219, s[52:55], s82 offen lds\n"
"s_add_u32 m0, s81, 82944\n"
"s_nop 0\n"
"buffer_load_dwordx4 v220, s[56:59], s82 offen lds\n"
"s_add_u32 s4, s4, 1\n"
"s_cmp_eq_u32 s4, 7\n"
"s_cselect_b32 s4, 0, s4\n"
"s_lshl_b32 s5, s4, 7\n"
"s_mul_i32 s6, s4, 172032\n"
"s_mul_i32 s82, s4, s9\n"
"s_add_u32 m0, s81, 16384\n"
"s_nop 0\n"
"buffer_load_dwordx4 v216, s[20:23], s5 offen lds\n"
"v_mov_b32 v226, 162048\n"
"v_mov_b32 v227, 1\n"
"v_mov_b32 v164, 0\n"
"v_lshl_add_u32 v165, v223, 2, v226\n"
"s_mov_b64 exec, 0xfffff\n"
"ds_write_b32 v165, v164\n"
"s_mov_b64 exec, -1\n"
"v_lshrrev_b32 v228, 5, v223\n"
"v_bfe_u32 v229, v223, 4, 1\n"
"v_bfe_u32 v230, v223, 2, 2\n"
"v_and_b32 v231, 3, v223\n"
"v_and_b32 v232, 2, v230\n"
"v_or_b32 v232, v232, v229\n"
"s_lshl_b32 s84, s70, 5\n"
"v_and_b32 v157, 31, v223\n"
"v_add_u32 v235, s84, v157\n"
"s_add_u32 m0, s81, 17408\n"
"s_nop 0\n"
"buffer_load_dwordx4 v217, s[20:23], s5 offen lds\n"
"v_and_b32 v233, 15, v223\n"
"v_lshrrev_b32 v234, 4, v223\n"
"v_add_u32 v209, s84, v233\n"
"v_lshrrev_b32 v208, 1, v233\n"
"v_xor_b32 v208, v208, v234\n"
"v_lshlrev_b32 v208, 4, v208\n"
"v_lshl_or_b32 v208, v209, 7, v208\n"
"v_xor_b32 v209, 64, v208\n"
"s_lshl4_add_u32 s84, s67, s68\n"
"s_lshl2_add_u32 s84, s84, s70\n"
"s_lshl_b32 s84, s84, 1\n"
"s_movk_i32 s86, 896\n"
"v_add_u32 v156, s84, v228\n"
"v_lshlrev_b32 v158, 5, v157\n"
"v_cmp_gt_u32 vcc, 28, v157\n"
"v_mad_u32_u24 v158, v156, s86, v158\n"
"v_mov_b32 v159, 1073741824\n"
"s_add_u32 m0, s81, 57344\n"
"s_nop 0\n"
"buffer_load_dwordx4 v218, s[24:27], s6 offen lds\n"
"v_cndmask_b32 v158, v159, v158, vcc\n"
"v_lshlrev_b32 v156, 4, v156\n"
"v_add_u32 v156, 69632, v156\n"
"s_bfe_u32 s13, s66, 0x20001\n"
"s_lshl_b32 s11, 1, s13\n"
"s_or_b32 s11, s11, 4\n"
"s_cmp_lt_u32 s13, 2\n"
"s_cselect_b32 s11, s11, 0\n"
"s_and_b32 s13, s13, 1\n"
"s_add_u32 s84, s8, s13\n"
"s_cmp_lt_u32 s84, 7\n"
"s_cselect_b32 s11, s11, 0\n"
"s_and_b32 s84, s11, 3\n"
"s_cmp_lt_u32 s70, 2\n"
"s_cselect_b32 s11, s11, s84\n"
"s_cmp_lt_u32 s70, 3\n"
"s_cselect_b32 s11, s11, 0\n"
"s_add_u32 m0, s81, 58368\n"
"s_add_u32 s91, s6, 21504\n"
"buffer_load_dwordx4 v218, s[24:27], s91 offen lds\n"
"s_add_u32 s10, s10, s13\n"
"s_cmp_eq_u32 s10, 7\n"
"s_cselect_b32 s10, 0, s10\n"
"s_min_u32 s14, s70, 2\n"
"s_mul_i32 s15, s68, 5\n"
"s_add_u32 s84, s15, s14\n"
"s_lshl_b32 s84, s84, 12\n"
"s_add_u32 s84, s84, 131072\n"
"v_lshl_add_u32 v197, v223, 4, s84\n"
"s_lshl_b32 s85, s10, 9\n"
"s_add_u32 s84, s84, s85\n"
"v_lshl_add_u32 v201, v229, 8, s84\n"
"v_lshl_add_u32 v201, v241, 5, v201\n"
"v_lshl_add_u32 v201, v228, 4, v201\n"
"s_add_u32 m0, s81, 90112\n"
"s_nop 0\n"
"buffer_load_dwordx4 v219, s[52:55], s82 offen lds\n"
"s_lshl_b32 s84, s13, 13\n"
"s_add_u32 s85, s84, 49152\n"
"s_lshl1_add_u32 s84, s84, 73728\n"
"s_movk_i32 s16, 2048\n"
"s_cmp_eq_u32 s70, 0\n"
"s_cselect_b32 s84, s85, s84\n"
"s_cselect_b32 s16, 1024, s16\n"
"s_cselect_b32 s17, 0, s17\n"
"v_lshl_add_u32 v161, v240, 7, v242\n"
"s_lshr_b32 s85, s70, 1\n"
"s_lshl_b32 s85, s85, 10\n"
"v_xor_b32 v200, s85, v161\n"
"v_add_u32 v200, s84, v200\n"
"v_and_b32 v165, 1, v223\n"
"v_lshlrev_b32 v165, 4, v165\n"
"v_mov_b32 v166, 0x3f80\n"
"v_lshlrev_b32 v166, v165, v166\n"
"v_bfe_u32 v164, v223, 1, 3\n"
"s_add_u32 m0, s81, 91136\n"
"s_nop 0\n"
"buffer_load_dwordx4 v220, s[56:59], s82 offen lds\n"
"s_add_u32 s82, s82, s12\n"
"v_cmp_eq_u32 vcc, 0, v164\n"
"s_nop 1\n"
"v_cndmask_b32 v172, 0, v166, vcc\n"
"v_cmp_eq_u32 vcc, 1, v164\n"
"s_nop 1\n"
"v_cndmask_b32 v173, 0, v166, vcc\n"
"v_cmp_eq_u32 vcc, 2, v164\n"
"s_nop 1\n"
"v_cndmask_b32 v174, 0, v166, vcc\n"
"v_cmp_eq_u32 vcc, 3, v164\n"
"s_nop 1\n"
"v_cndmask_b32 v175, 0, v166, vcc\n"
"v_add_u32 v164, v164, v165\n"
"v_lshl_add_u32 v164, v229, 5, v164\n"
"v_lshl_add_u32 v164, v228, 3, v164\n"
"s_lshl_b32 s84, s14, 8\n"
"s_add_u32 s84, s84, 160768\n"
"v_lshl_add_u32 v199, v164, 2, s84\n"
"s_add_u32 m0, s81, 24576\n"
"s_add_u32 s91, s5, 57344\n"
"buffer_load_dwordx4 v216, s[20:23], s91 offen lds\n"
"s_lshl_b32 s84, s67, 11\n"
"s_add_u32 s84, s84, 69632\n"
"v_lshl_add_u32 v196, v235, 4, s84\n"
"v_lshlrev_b32 v198, 2, v235\n"
"v_add_u32 v198, 159744, v198\n"
"s_movk_i32 s86, 3584\n"
"v_mad_u32_u24 v221, v243, s86, v242\n"
"v_add_u32 v221, s3, v221\n"
"s_and_b32 s86, s70, 1\n"
"s_lshl_b32 s86, s86, 4\n"
"v_xor_b32 v221, s86, v221\n"
"s_add_u32 m0, s81, 25600\n"
"s_add_u32 s91, s5, 57344\n"
"buffer_load_dwordx4 v217, s[20:23], s91 offen lds\n"
"s_add_u32 m0, s81, 98304\n"
"s_nop 0\n"
"buffer_load_dwordx4 v219, s[52:55], s82 offen lds\n"
"s_add_u32 m0, s81, 99328\n"
"s_nop 0\n"
"buffer_load_dwordx4 v220, s[56:59], s82 offen lds\n"
"s_add_u32 s4, s4, 1\n"
"s_cmp_eq_u32 s4, 7\n"
"s_cselect_b32 s4, 0, s4\n"
"s_lshl_b32 s5, s4, 7\n"
"s_mul_i32 s6, s4, 172032\n"
"s_mul_i32 s82, s4, s9\n"
"s_waitcnt lgkmcnt(0)\n"
"s_mul_i32 s71, s71, 1664525\n"
"s_add_u32 s71, s71, 1013904223\n"
"s_waitcnt vmcnt(10) lgkmcnt(0)\n"
"s_barrier\n"
"s_bitcmp1_b32 s11, 0\n"
"s_cbranch_scc0 Lj3_%=\n"
"ds_read_b128 v[120:123], v200\n"
"v_add_u32 v200, s16, v200\n"
"v_xor_b32 v200, s17, v200\n"
"ds_read_b128 v[124:127], v200\n"
"v_add_u32 v200, s16, v200\n"
"v_xor_b32 v200, s17, v200\n"
"ds_read_b128 v[128:131], v200\n"
"v_add_u32 v200, s16, v200\n"
"v_xor_b32 v200, s17, v200\n"
"ds_read_b128 v[132:135], v200\n"
"v_add_u32 v200, s16, v200\n"
"v_xor_b32 v200, s17, v200\n"
"ds_read_b128 v[136:139], v200\n"
"v_add_u32 v200, s16, v200\n"
"v_xor_b32 v200, s17, v200\n"
"ds_read_b128 v[140:143], v200\n"
"v_add_u32 v200, s16, v200\n"
"v_xor_b32 v200, s17, v200\n"
"ds_read_b128 v[144:147], v200\n"
"v_add_u32 v200, s16, v200\n"
"v_xor_b32 v200, s17, v200\n"
"ds_read_b128 v[148:151], v200\n"
"v_add_u32 v200, s16, v200\n"
"v_xor_b32 v200, s17, v200\n"
"Lj3_%=:\n"
"s_add_u32 m0, s81, 32768\n"
"s_nop 0\n"
"buffer_load_dwordx4 v216, s[20:23], s5 offen lds\n"
"ds_read_b128 v[0:3], v208 offset:0\n"
"ds_read_b128 v[4:7], v208 offset:2048\n"
"ds_read_b128 v[8:11], v209 offset:0\n"
"ds_read_b128 v[12:15], v209 offset:2048\n"
"s_and_b32 s84, s11, 3\n"
"s_cselect_b32 s18, 0x00ff00ff, 0\n"
"s_add_u32 m0, s81, 33792\n"
"s_nop 0\n"
"buffer_load_dwordx4 v217, s[20:23], s5 offen lds\n"
"s_add_u32 m0, s81, 65536\n"
"s_nop 0\n"
"buffer_load_dwordx4 v218, s[24:27], s6 offen lds\n"
"s_add_u32 m0, s81, 66560\n"
"s_add_u32 s91, s6, 21504\n"
"buffer_load_dwordx4 v218, s[24:27], s91 offen lds\n"
"s_add_u32 m0, s81, 106496\n"
"s_nop 0\n"
"buffer_load_dwordx4 v219, s[52:55], s82 offen lds\n"
"s_add_u32 m0, s81, 107520\n"
"s_nop 0\n"
"buffer_load_dwordx4 v220, s[56:59], s82 offen lds\n"
"s_add_u32 s82, s82, s12\n"
"s_add_u32 m0, s81, 40960\n"
"s_add_u32 s91, s5, 57344\n"
"buffer_load_dwordx4 v216, s[20:23], s91 offen lds\n"
"s_add_u32 m0, s81, 41984\n"
"s_add_u32 s91, s5, 57344\n"
"buffer_load_dwordx4 v217, s[20:23], s91 offen lds\n"
"s_add_u32 m0, s81, 114688\n"
"s_nop 0\n"
"buffer_load_dwordx4 v219, s[52:55], s82 offen lds\n"
"s_add_u32 m0, s81, 115712\n"
"s_nop 0\n"
"buffer_load_dwordx4 v220, s[56:59], s82 offen lds\n"
"s_add_u32 s4, s4, 1\n"
"s_cmp_eq_u32 s4, 7\n"
"s_cselect_b32 s4, 0, s4\n"
"s_lshl_b32 s5, s4, 7\n"
"s_mul_i32 s6, s4, 172032\n"
"s_mul_i32 s82, s4, s9\n"
"buffer_load_dwordx4 v[112:115], v158, s[20:23], 0 offen\n"
"buffer_load_dwordx4 v[116:119], v158, s[20:23], 0 offen offset:16\n"
"s_waitcnt vmcnt(12) lgkmcnt(0)\n"
"s_barrier\n"
"s_bitcmp1_b32 s11, 1\n"
"s_cbranch_scc0 Lj4_%=\n"
"ds_read_b128 v[120:123], v200\n"
"v_add_u32 v200, s16, v200\n"
"v_xor_b32 v200, s17, v200\n"
"ds_read_b128 v[124:127], v200\n"
"v_add_u32 v200, s16, v200\n"
"v_xor_b32 v200, s17, v200\n"
"ds_read_b128 v[128:131], v200\n"
"v_add_u32 v200, s16, v200\n"
"v_xor_b32 v200, s17, v200\n"
"ds_read_b128 v[132:135], v200\n"
"v_add_u32 v200, s16, v200\n"
"v_xor_b32 v200, s17, v200\n"
"ds_read_b128 v[136:139], v200\n"
"v_add_u32 v200, s16, v200\n"
"v_xor_b32 v200, s17, v200\n"
"ds_read_b128 v[140:143], v200\n"
"v_add_u32 v200, s16, v200\n"
"v_xor_b32 v200, s17, v200\n"
"ds_read_b128 v[144:147], v200\n"
"v_add_u32 v200, s16, v200\n"
"v_xor_b32 v200, s17, v200\n"
"ds_read_b128 v[148:151], v200\n"
"v_add_u32 v200, s16, v200\n"
"v_xor_b32 v200, s17, v200\n"
"Lj4_%=:\n"
"s_add_u32 m0, s81, 0\n"
"s_nop 0\n"
"buffer_load_dwordx4 v216, s[20:23], s5 offen lds\n"
"ds_read_b128 v[16:19], v208 offset:16384\n"
"ds_read_b128 v[20:23], v208 offset:18432\n"
"ds_read_b128 v[24:27], v209 offset:16384\n"
"ds_read_b128 v[28:31], v209 offset:18432\n"
"s_add_u32 m0, s81, 1024\n"
"s_nop 0\n"
"buffer_load_dwordx4 v217, s[20:23], s5 offen lds\n"
"s_add_u32 m0, s81, 49152\n"
"s_nop 0\n"
"buffer_load_dwordx4 v218, s[24:27], s6 offen lds\n"
"s_add_u32 m0, s81, 50176\n"
"s_add_u32 s91, s6, 21504\n"
"buffer_load_dwordx4 v218, s[24:27], s91 offen lds\n"
"s_add_u32 m0, s81, 73728\n"
"s_nop 0\n"
"buffer_load_dwordx4 v219, s[52:55], s82 offen lds\n"
"v_mov_b32 v164, 0\n"
"v_mov_b32 v165, 0\n"
"v_mov_b32 v166, 0\n"
"v_mov_b32 v167, 0\n"
"s_waitcnt vmcnt(5)\n"
"v_lshlrev_b32 v168, 16, v112\n"
"v_and_b32 v169, 0xffff0000, v112\n"
"v_add_f32 v164, v164, v168\n"
"v_add_f32 v165, v165, v169\n"
"v_fmac_f32 v166, v168, v168\n"
"v_fmac_f32 v167, v169, v169\n"
"s_add_u32 m0, s81, 74752\n"
"s_nop 0\n"
"buffer_load_dwordx4 v220, s[56:59], s82 offen lds\n"
"s_add_u32 s82, s82, s12\n"
"v_lshlrev_b32 v168, 16, v113\n"
"v_and_b32 v169, 0xffff0000, v113\n"
"v_add_f32 v164, v164, v168\n"
"v_add_f32 v165, v165, v169\n"
"v_fmac_f32 v166, v168, v168\n"
"v_fmac_f32 v167, v169, v169\n"
"v_lshlrev_b32 v168, 16, v114\n"
"v_and_b32 v169, 0xffff0000, v114\n"
"s_add_u32 m0, s81, 8192\n"
"s_add_u32 s91, s5, 57344\n"
"buffer_load_dwordx4 v216, s[20:23], s91 offen lds\n"
"v_add_f32 v164, v164, v168\n"
"v_add_f32 v165, v165, v169\n"
"v_fmac_f32 v166, v168, v168\n"
"v_fmac_f32 v167, v169, v169\n"
"v_lshlrev_b32 v168, 16, v115\n"
"v_and_b32 v169, 0xffff0000, v115\n"
"v_add_f32 v164, v164, v168\n"
"v_add_f32 v165, v165, v169\n"
"v_fmac_f32 v166, v168, v168\n"
"v_fmac_f32 v167, v169, v169\n"
"s_add_u32 m0, s81, 9216\n"
"s_add_u32 s91, s5, 57344\n"
"buffer_load_dwordx4 v217, s[20:23], s91 offen lds\n"
"v_lshlrev_b32 v168, 16, v116\n"
"v_and_b32 v169, 0xffff0000, v116\n"
"v_add_f32 v164, v164, v168\n"
"v_add_f32 v165, v165, v169\n"
"v_fmac_f32 v166, v168, v168\n"
"v_fmac_f32 v167, v169, v169\n"
"v_lshlrev_b32 v168, 16, v117\n"
"v_and_b32 v169, 0xffff0000, v117\n"
"s_add_u32 m0, s81, 81920\n"
"s_nop 0\n"
"buffer_load_dwordx4 v219, s[52:55], s82 offen lds\n"
"v_add_f32 v164, v164, v168\n"
"v_add_f32 v165, v165, v169\n"
"v_fmac_f32 v166, v168, v168\n"
"v_fmac_f32 v167, v169, v169\n"
"v_lshlrev_b32 v168, 16, v118\n"
"v_and_b32 v169, 0xffff0000, v118\n"
"v_add_f32 v164, v164, v168\n"
"v_add_f32 v165, v165, v169\n"
"v_fmac_f32 v166, v168, v168\n"
"v_fmac_f32 v167, v169, v169\n"
"s_add_u32 m0, s81, 82944\n"
"s_nop 0\n"
"buffer_load_dwordx4 v220, s[56:59], s82 offen lds\n"
"s_add_u32 s4, s4, 1\n"
"s_cmp_eq_u32 s4, 7\n"
"s_cselect_b32 s4, 0, s4\n"
"s_lshl_b32 s5, s4, 7\n"
"s_mul_i32 s6, s4, 172032\n"
"s_mul_i32 s82, s4, s9\n"
"v_lshlrev_b32 v168, 16, v119\n"
"v_and_b32 v169, 0xffff0000, v119\n"
"v_add_f32 v164, v164, v168\n"
"v_add_f32 v165, v165, v169\n"
"v_fmac_f32 v166, v168, v168\n"
"v_fmac_f32 v167, v169, v169\n"
"s_waitcnt vmcnt(12) lgkmcnt(0)\n"
"s_barrier\n"
"s_add_u32 m0, s81, 16384\n"
"s_nop 0\n"
"buffer_load_dwordx4 v216, s[20:23], s5 offen lds\n"
"ds_read_b128 v[32:35], v208 offset:32768\n"
"ds_read_b128 v[36:39], v208 offset:34816\n"
"ds_read_b128 v[40:43], v209 offset:32768\n"
"ds_read_b128 v[44:47], v209 offset:34816\n"
"s_add_u32 m0, s81, 17408\n"
"s_nop 0\n"
"buffer_load_dwordx4 v217, s[20:23], s5 offen lds\n"
"v_add_f32 v164, v164, v165\n"
"v_add_f32 v166, v166, v167\n"
"v_mfma_f32_16x16x32_bf16 v[152:155], v[120:123], v[172:175], 0\n"
"v_add_f32_dpp v164, v164, v164 row_shr:1 row_mask:0xf bank_mask:0xf bound_ctrl:1\n"
"s_add_u32 m0, s81, 57344\n"
"s_nop 0\n"
"buffer_load_dwordx4 v218, s[24:27], s6 offen lds\n"
"v_add_f32_dpp v166, v166, v166 row_shr:1 row_mask:0xf bank_mask:0xf bound_ctrl:1\n"
"v_mfma_f32_16x16x32_bf16 v[152:155], v[124:127], v[172:175], v[152:155]\n"
"v_add_f32_dpp v164, v164, v164 row_shr:2 row_mask:0xf bank_mask:0xf bound_ctrl:1\n"
"v_add_f32_dpp v166, v166, v166 row_shr:2 row_mask:0xf bank_mask:0xf bound_ctrl:1\n"
"s_add_u32 m0, s81, 58368\n"
"s_add_u32 s91, s6, 21504\n"
"buffer_load_dwordx4 v218, s[24:27], s91 offen lds\n"
"v_mfma_f32_16x16x32_bf16 v[152:155], v[128:131], v[172:175], v[152:155]\n"
"v_add_f32_dpp v164, v164, v164 row_shr:4 row_mask:0xf bank_mask:0xf bound_ctrl:1\n"
"v_add_f32_dpp v166, v166, v166 row_shr:4 row_mask:0xf bank_mask:0xf bound_ctrl:1\n"
"v_mfma_f32_16x16x32_bf16 v[152:155], v[132:135], v[172:175], v[152:155]\n"
"s_add_u32 m0, s81, 90112\n"
"s_nop 0\n"
"buffer_load_dwordx4 v219, s[52:55], s82 offen lds\n"
"v_add_f32_dpp v164, v164, v164 row_shr:8 row_mask:0xf bank_mask:0xf bound_ctrl:1\n"
"v_add_f32_dpp v166, v166, v166 row_shr:8 row_mask:0xf bank_mask:0xf bound_ctrl:1\n"
"v_mfma_f32_16x16x32_bf16 v[152:155], v[136:139], v[172:175], v[152:155]\n"
"v_add_f32_dpp v164, v164, v164 row_bcast:15 row_mask:0xa bank_mask:0xf\n"
"s_add_u32 m0, s81, 91136\n"
"s_nop 0\n"
"buffer_load_dwordx4 v220, s[56:59], s82 offen lds\n"
"s_add_u32 s82, s82, s12\n"
"v_add_f32_dpp v166, v166, v166 row_bcast:15 row_mask:0xa bank_mask:0xf\n"
"v_mfma_f32_16x16x32_bf16 v[152:155], v[140:143], v[172:175], v[152:155]\n"
"v_mul_f32 v164, 0x3b124925, v164\n"
"v_mul_f32 v166, 0x3b124925, v166\n"
"s_add_u32 m0, s81, 24576\n"
"s_add_u32 s91, s5, 57344\n"
"buffer_load_dwordx4 v216, s[20:23], s91 offen lds\n"
"v_mfma_f32_16x16x32_bf16 v[152:155], v[144:147], v[172:175], v[152:155]\n"
"v_fma_f32 v166, -v164, v164, v166\n"
"v_max_f32 v166, 0, v166\n"
"v_mfma_f32_16x16x32_bf16 v[152:155], v[148:151], v[172:175], v[152:155]\n"
"s_add_u32 m0, s81, 25600\n"
"s_add_u32 s91, s5, 57344\n"
"buffer_load_dwordx4 v217, s[20:23], s91 offen lds\n"
"v_add_f32 v166, 0x3727c5ac, v166\n"
"v_rsq_f32 v166, v166\n"
"v_mov_b32 v165, s71\n"
"v_mov_b32 v167, s71\n"
"s_add_u32 m0, s81, 98304\n"
"s_nop 0\n"
"buffer_load_dwordx4 v219, s[52:55], s82 offen lds\n"
"s_mov_b32 exec_lo, 0x80000000\n"
"s_mov_b32 exec_hi, 0x80000000\n"
"buffer_store_dwordx2 v[164:165], v156, s[48:51], 0 offen sc1\n"
"buffer_store_dwordx2 v[166:167], v156, s[48:51], 0 offen offset:8 sc1\n"
"s_mov_b64 exec, -1\n"
"s_add_u32 m0, s81, 99328\n"
"s_nop 0\n"
"buffer_load_dwordx4 v220, s[56:59], s82 offen lds\n"
"s_add_u32 s4, s4, 1\n"
"s_cmp_eq_u32 s4, 7\n"
"s_cselect_b32 s4, 0, s4\n"
"s_lshl_b32 s5, s4, 7\n"
"s_mul_i32 s6, s4, 172032\n"
"s_mul_i32 s82, s4, s9\n"
"v_mov_b32 v121, s71\n"
"v_mov_b32 v123, s71\n"
"v_permlane32_swap_b32 v152, v154\n"
"v_permlane32_swap_b32 v153, v155\n"
"v_add_f32 v120, v152, v154\n"
"v_add_f32 v122, v153, v155\n"
"s_mov_b32 exec_lo, s18\n"
"s_mov_b32 exec_hi, s18\n"
"buffer_store_dwordx2 v[120:121], v201, s[48:51], 0 offen sc1\n"
"buffer_store_dwordx2 v[122:123], v201, s[48:51], 0 offen offset:8 sc1\n"
"s_mov_b64 exec, -1\n"
"s_waitcnt vmcnt(14) lgkmcnt(0)\n"
"s_barrier\n"
"s_add_u32 m0, s81, 32768\n"
"s_nop 0\n"
"buffer_load_dwordx4 v216, s[20:23], s5 offen lds\n"
"ds_read_b128 v[48:51], v208 offset:0\n"
"ds_read_b128 v[52:55], v208 offset:2048\n"
"ds_read_b128 v[56:59], v209 offset:0\n"
"ds_read_b128 v[60:63], v209 offset:2048\n"
"s_lshl2_add_u32 s84, s67, s70\n"
"s_lshl_b32 s19, s68, 6\n"
"s_add_u32 s19, s19, s84\n"
"s_lshl_b32 s19, s19, 12\n"
"s_add_u32 s19, s19, 1051072\n"
"s_add_u32 m0, s81, 33792\n"
"s_nop 0\n"
"buffer_load_dwordx4 v217, s[20:23], s5 offen lds\n"
"v_cmp_eq_u32 vcc, 0, v231\n"
"v_mov_b32 v113, 0x3f803f80\n"
"s_nop 0\n"
"v_cndmask_b32 v112, 0, v113, vcc\n"
"v_mov_b32 v113, v112\n"
"v_mov_b32 v114, v112\n"
"v_mov_b32 v115, v112\n"
"v_lshlrev_b32 v116, 4, v231\n"
"v_lshl_or_b32 v116, v230, 9, v116\n"
"s_add_u32 m0, s81, 65536\n"
"s_nop 0\n"
"buffer_load_dwordx4 v218, s[24:27], s6 offen lds\n"
"v_and_b32 v212, 2, v230\n"
"v_lshlrev_b32 v212, 5, v212\n"
"v_lshl_or_b32 v212, v230, 7, v212\n"
"v_lshl_or_b32 v212, v234, 11, v212\n"
"v_lshl_or_b32 v212, v229, 4, v212\n"
"v_and_b32 v213, 2, v223\n"
"v_lshl_or_b32 v212, v213, 4, v212\n"
"v_and_b32 v213, 1, v223\n"
"v_lshl_or_b32 v212, v213, 3, v212\n"
"s_add_u32 m0, s81, 66560\n"
"s_add_u32 s91, s6, 21504\n"
"buffer_load_dwordx4 v218, s[24:27], s91 offen lds\n"
"s_and_b32 s84, s70, 2\n"
"s_add_u32 s84, s84, s70\n"
"s_mul_i32 s13, s84, 16\n"
"v_xor_b32 v212, s13, v212\n"
"v_add_u32 v212, 122880, v212\n"
"v_xor_b32 v213, 16, v212\n"
"v_xor_b32 v214, 64, v212\n"
"v_xor_b32 v215, 80, v212\n"
"s_add_u32 m0, s81, 106496\n"
"s_nop 0\n"
"buffer_load_dwordx4 v219, s[52:55], s82 offen lds\n"
"v_mov_b32 v188, 0\n"
"v_mov_b32 v189, s71\n"
"v_mov_b32 v190, 0\n"
"v_mov_b32 v191, s71\n"
"s_add_u32 m0, s81, 107520\n"
"s_nop 0\n"
"buffer_load_dwordx4 v220, s[56:59], s82 offen lds\n"
"s_add_u32 s82, s82, s12\n"
"s_add_u32 m0, s81, 40960\n"
"s_add_u32 s91, s5, 57344\n"
"buffer_load_dwordx4 v216, s[20:23], s91 offen lds\n"
"s_add_u32 m0, s81, 41984\n"
"s_add_u32 s91, s5, 57344\n"
"buffer_load_dwordx4 v217, s[20:23], s91 offen lds\n"
"s_add_u32 m0, s81, 114688\n"
"s_nop 0\n"
"buffer_load_dwordx4 v219, s[52:55], s82 offen lds\n"
"s_add_u32 m0, s81, 115712\n"
"s_nop 0\n"
"buffer_load_dwordx4 v220, s[56:59], s82 offen lds\n"
"s_add_u32 s4, s4, 1\n"
"s_cmp_eq_u32 s4, 7\n"
"s_cselect_b32 s4, 0, s4\n"
"s_lshl_b32 s5, s4, 7\n"
"s_mul_i32 s6, s4, 172032\n"
"s_mul_i32 s82, s4, s9\n"
"s_mov_b64 exec, 0xffff\n"
"buffer_store_dwordx4 v[112:115], v116, s[48:51], s19 offen sc1\n"
"s_mov_b64 exec, -1\n"
"s_waitcnt vmcnt(13) lgkmcnt(0)\n"
"s_barrier\n"
"s_add_u32 m0, s81, 0\n"
"s_nop 0\n"
"buffer_load_dwordx4 v216, s[20:23], s5 offen lds\n"
"ds_read_b128 v[64:67], v208 offset:16384\n"
"ds_read_b128 v[68:71], v208 offset:18432\n"
"ds_read_b128 v[72:75], v209 offset:16384\n"
"ds_read_b128 v[76:79], v209 offset:18432\n"
"s_and_b32 s86, s64, 7\n"
"s_and_b32 s84, s66, 1\n"
"s_lshl1_add_u32 s72, s86, s84\n"
"s_lshr_b32 s73, s66, 1\n"
"s_bitcmp1_b32 s64, 0\n"
"s_cselect_b32 s75, 28, 0\n"
"s_cselect_b32 s83, 28, 28\n"
"s_add_u32 s75, s75, s66\n"
"s_mul_i32 s84, s75, 37\n"
"s_lshr_b32 s84, s84, 8\n"
"s_mul_i32 s85, s84, 7\n"
"s_sub_u32 s75, s75, s85\n"
"s_lshr_b32 s86, s86, 1\n"
"s_lshl3_add_u32 s74, s86, s84\n"
"s_add_u32 m0, s81, 1024\n"
"s_nop 0\n"
"buffer_load_dwordx4 v217, s[20:23], s5 offen lds\n"
"s_lshl2_add_u32 s84, s64, s70\n"
"s_mul_i32 s85, s84, 1664\n"
"s_lshl_b32 s84, s84, 9\n"
"v_lshl_add_u32 v201, v223, 7, s85\n"
"v_lshl_add_u32 v200, v223, 7, s84\n"
"s_add_u32 m0, s81, 49152\n"
"s_nop 0\n"
"buffer_load_dwordx4 v218, s[24:27], s6 offen lds\n"
"s_add_u32 m0, s81, 50176\n"
"s_add_u32 s91, s6, 21504\n"
"buffer_load_dwordx4 v218, s[24:27], s91 offen lds\n"
"s_add_u32 m0, s81, 73728\n"
"s_nop 0\n"
"buffer_load_dwordx4 v219, s[52:55], s82 offen lds\n"
"s_add_u32 m0, s81, 74752\n"
"s_nop 0\n"
"buffer_load_dwordx4 v220, s[56:59], s82 offen lds\n"
"s_add_u32 s82, s82, s12\n"
"s_add_u32 m0, s81, 8192\n"
"s_add_u32 s91, s5, 57344\n"
"buffer_load_dwordx4 v216, s[20:23], s91 offen lds\n"
"s_add_u32 m0, s81, 9216\n"
"s_add_u32 s91, s5, 57344\n"
"buffer_load_dwordx4 v217, s[20:23], s91 offen lds\n"
"s_add_u32 m0, s81, 81920\n"
"s_nop 0\n"
"buffer_load_dwordx4 v219, s[52:55], s82 offen lds\n"
"s_add_u32 m0, s81, 82944\n"
"s_nop 0\n"
"buffer_load_dwordx4 v220, s[56:59], s82 offen lds\n"
"s_add_u32 m0, s81, 122880\n"
"s_nop 0\n"
"buffer_load_dwordx4 v221, s[28:31], s7 offen lds\n"
"s_add_u32 m0, s81, 123904\n"
"s_nop 0\n"
"buffer_load_dwordx4 v221, s[32:35], s7 offen lds\n"
"s_add_u32 s7, s7, 114688\n"
"s_add_u32 m0, s81, 131072\n"
"s_nop 0\n"
"buffer_load_dwordx4 v221, s[28:31], s7 offen lds\n"
"s_add_u32 m0, s81, 132096\n"
"s_nop 0\n"
"buffer_load_dwordx4 v221, s[32:35], s7 offen lds\n"
"s_add_u32 s7, s7, 114688\n"
"s_cmp_eq_u32 s7, 1605632\n"
"s_cselect_b32 s7, 0, s7\n"
"s_add_u32 m0, s81, 139264\n"
"s_nop 0\n"
"buffer_load_dwordx4 v221, s[28:31], s7 offen lds\n"
"s_add_u32 m0, s81, 140288\n"
"s_nop 0\n"
"buffer_load_dwordx4 v221, s[32:35], s7 offen lds\n"
"s_add_u32 s7, s7, 114688\n"
"s_add_u32 m0, s81, 147456\n"
"s_nop 0\n"
"buffer_load_dwordx4 v221, s[28:31], s7 offen lds\n"
"s_add_u32 m0, s81, 148480\n"
"s_nop 0\n"
"buffer_load_dwordx4 v221, s[32:35], s7 offen lds\n"
"s_add_u32 s7, s7, 114688\n"
"s_cmp_eq_u32 s7, 1605632\n"
"s_cselect_b32 s7, 0, s7\n"
"s_waitcnt vmcnt(19) lgkmcnt(0)\n"
"s_barrier\n"
"buffer_load_dwordx4 v[192:195], v196, s[48:51], 0 offen offset:0 sc1\n"
"ds_read_b128 v[80:83], v208 offset:32768\n"
"ds_read_b128 v[84:87], v208 offset:34816\n"
"ds_read_b128 v[88:91], v209 offset:32768\n"
"ds_read_b128 v[92:95], v209 offset:34816\n"
"v_cmp_eq_u32 vcc, 0, v233\n"
"v_mov_b32 v165, 0x3f803f80\n"
"v_and_b32 v164, 8, v233\n"
"v_add_u32 v164, v164, v233\n"
"v_cndmask_b32 v216, 0, v165, vcc\n"
"s_lshl1_add_u32 s14, s13, 161536\n"
"v_lshl_add_u32 v220, v164, 2, s14\n"
"v_mov_b32 v217, v216\n"
"v_mov_b32 v218, v216\n"
"v_mov_b32 v219, v216\n"
"buffer_load_dwordx4 v[176:179], v197, s[48:51], 0 offen offset:0 sc1\n"
"buffer_load_dwordx4 v[180:183], v197, s[48:51], 0 offen offset:1024 sc1\n"
"buffer_load_dwordx4 v[184:187], v197, s[48:51], 0 offen offset:2048 sc1\n"
"s_mov_b32 exec_hi, 0\n"
"buffer_load_dwordx4 v[188:191], v197, s[48:51], 0 offen offset:3072 sc1\n"
"s_mov_b32 exec_hi, -1\n"
"s_lshl2_add_u32 s84, s67, s70\n"
"s_mul_i32 s84, s84, 28\n"
"s_lshr_b32 s85, s3, 7\n"
"s_add_u32 s84, s84, s85\n"
"s_and_b32 s85, s70, 2\n"
"s_lshl2_add_u32 s84, s84, s85\n"
"s_lshl_b32 s84, s84, 10\n"
"s_add_u32 s8, s84, 9437184\n"
"s_lshl2_add_u32 s84, s67, s70\n"
"s_lshl4_add_u32 s84, s84, s68\n"
"s_lshl_b32 s84, s84, 11\n"
"s_add_u32 s9, s84, 16777216\n"
"s_lshr_b32 s84, s70, 1\n"
"s_lshl1_add_u32 s84, s67, s84\n"
"s_mul_i32 s84, s84, 224\n"
"s_lshr_b32 s85, s78, 5\n"
"s_add_u32 s84, s84, s85\n"
"s_add_u32 m0, s81, 16384\n"
"s_nop 0\n"
"buffer_load_dwordx4 v221, s[28:31], s7 offen lds\n"
"s_and_b32 s85, s70, 1\n"
"s_lshl1_add_u32 s84, s85, s84\n"
"s_lshl_b32 s84, s84, 10\n"
"s_add_u32 s10, s84, 9437184\n"
"s_add_u32 s11, s10, 114688\n"
"s_lshl4_add_u32 s89, s67, s68\n"
"s_lshl_b32 s89, s89, 7\n"
"s_add_u32 s89, s89, 36864\n"
"s_lshr_b32 s84, s74, 1\n"
"s_lshl2_add_u32 s84, s84, s70\n"
"s_lshl_b32 s84, s84, 9\n"
"s_add_u32 s84, s84, 36864\n"
"v_lshl_add_u32 v251, v223, 7, s84\n"
"s_lshl4_add_u32 s15, s73, s72\n"
"s_lshl_b32 s15, s15, 7\n"
"s_add_u32 s15, s15, 36864\n"
"v_writelane_b32 v251, s15, 4\n"
"s_add_u32 m0, s81, 17408\n"
"s_nop 0\n"
"buffer_load_dwordx4 v221, s[32:35], s7 offen lds\n"
"s_add_u32 s7, s7, 114688\n"
"v_mov_b32 v243, v226\n"
"v_mov_b32 v244, 1\n"
"v_lshlrev_b32 v245, 4, v223\n"
"s_lshl_b32 s85, s75, 7\n"
"s_cmp_lt_u32 s66, s83\n"
"s_cselect_b32 s85, s85, 1073741824\n"
"s_mul_i32 s84, s70, 7168\n"
"s_add_u32 s84, s84, s85\n"
"s_movk_i32 s86, 896\n"
"v_and_b32 v252, 6, v240\n"
"v_xor_b32 v252, v252, v241\n"
"v_lshl_add_u32 v252, v252, 4, s84\n"
"v_mad_u32_u24 v253, v240, s86, v252\n"
"s_mul_i32 s84, s70, 401408\n"
"s_add_u32 s84, s84, s85\n"
"s_add_u32 m0, s81, 24576\n"
"s_nop 0\n"
"buffer_load_dwordx4 v221, s[28:31], s7 offen lds\n"
"v_mad_u32_u24 v241, v240, s86, v242\n"
"v_add_u32 v241, s84, v241\n"
"v_mov_b32 v254, 1073741824\n"
"s_mul_i32 s82, s70, 12288\n"
"s_add_u32 s82, s82, 107520\n"
"s_lshl_b32 s6, s70, 10\n"
"s_mov_b32 s52, 0\n"
"s_and_b32 s84, s70, 1\n"
"s_lshl_b32 s84, s84, 6\n"
"v_lshlrev_b32 v248, 5, v232\n"
"v_xor_b32 v248, s84, v248\n"
"v_lshl_or_b32 v248, v231, 3, v248\n"
"v_lshl_or_b32 v248, v230, 7, v248\n"
"v_lshl_or_b32 v248, v228, 9, v248\n"
"s_add_u32 m0, s81, 25600\n"
"s_nop 0\n"
"buffer_load_dwordx4 v221, s[32:35], s7 offen lds\n"
"s_add_u32 s7, s7, 114688\n"
"s_cmp_eq_u32 s7, 1605632\n"
"s_cselect_b32 s7, 0, s7\n"
"v_add_u32 v248, s82, v248\n"
"v_xor_b32 v249, 64, v248\n"
"s_lshr_b32 s87, s70, 1\n"
"s_mul_i32 s88, s87, 8704\n"
"s_lshl1_add_u32 s88, s84, s88\n"
"s_movk_i32 s86, 272\n"
"v_lshl_add_u32 v250, v228, 4, s88\n"
"v_and_b32 v252, 31, v223\n"
"v_mad_u32_u24 v250, v252, s86, v250\n"
"s_lshl1_add_u32 s85, s74, s87\n"
"s_xor_b32 s86, s85, 1\n"
"s_lshl2_add_u32 s88, s85, s70\n"
"s_mul_i32 s88, s88, 28672\n"
"s_add_u32 s88, s88, 9437184\n"
"v_add_u32 v246, s88, v245\n"
"s_add_u32 m0, s81, 57344\n"
"s_nop 0\n"
"buffer_load_dwordx4 v221, s[28:31], s7 offen lds\n"
"s_lshl2_add_u32 s88, s86, s70\n"
"s_mul_i32 s88, s88, 28672\n"
"s_add_u32 s88, s88, 9437184\n"
"v_add_u32 v247, s88, v245\n"
"s_and_b32 s86, s74, 3\n"
"s_mul_i32 s84, s86, 6\n"
"s_mul_i32 s85, s75, 7\n"
"s_lshr_b32 s85, s85, 1\n"
"s_add_u32 s84, s84, s85\n"
"s_sub_u32 s85, s84, 24\n"
"s_cmp_lt_u32 s84, 24\n"
"s_cselect_b32 s84, s84, s85\n"
"s_mul_i32 s4, s84, 14336\n"
"s_mul_i32 s5, s84, 1024\n"
"s_add_u32 m0, s81, 58368\n"
"s_nop 0\n"
"buffer_load_dwordx4 v221, s[32:35], s7 offen lds\n"
"s_add_u32 s7, s7, 114688\n"
"s_mul_i32 s12, s86, 7\n"
"s_lshr_b32 s12, s12, 1\n"
"s_waitcnt vmcnt(19) lgkmcnt(0)\n"
"s_barrier\n"
"s_add_u32 m0, s81, 90112\n"
"s_nop 0\n"
"buffer_load_dwordx4 v221, s[28:31], s7 offen lds\n"
"ds_read_b128 v[96:99], v208 offset:0\n"
"ds_read_b128 v[100:103], v208 offset:2048\n"
"ds_read_b128 v[104:107], v209 offset:0\n"
"ds_read_b128 v[108:111], v209 offset:2048\n"
"s_waitcnt vmcnt(7)\n"
"v_min3_u32 v164, v177, v179, s71\n"
"v_max3_u32 v165, v177, v179, s71\n"
"v_min3_u32 v164, v181, v183, v164\n"
"s_add_u32 m0, s81, 91136\n"
"s_nop 0\n"
"buffer_load_dwordx4 v221, s[32:35], s7 offen lds\n"
"s_add_u32 s7, s7, 114688\n"
"s_cmp_eq_u32 s7, 1605632\n"
"s_cselect_b32 s7, 0, s7\n"
"v_max3_u32 v165, v181, v183, v165\n"
"v_min3_u32 v164, v185, v187, v164\n"
"v_max3_u32 v165, v185, v187, v165\n"
"v_min3_u32 v164, v189, v191, v164\n"
"v_max3_u32 v165, v189, v191, v165\n"
"v_min3_u32 v164, v193, v195, v164\n"
"v_max3_u32 v165, v193, v195, v165\n"
"v_cmp_ne_u32 vcc, v164, v165\n"
"s_nop 0\n"
"s_cbranch_vccnz Lfs5_%=\n"
"Lfb6_%=:\n"
"v_add_f32 v176, v176, v180\n"
"v_add_f32 v184, v184, v188\n"
"v_add_f32 v178, v178, v182\n"
"v_add_f32 v186, v186, v190\n"
"v_add_f32 v176, v176, v184\n"
"v_add_f32 v178, v178, v186\n"
"v_sub_f32 v224, 0, v192\n"
"v_mov_b32 v225, v194\n"
"ds_write2st64_b32 v198, v224, v225 offset1:2\n"
"v_permlane32_swap_b32 v176, v178\n"
"v_add_f32 v176, v176, v178\n"
"ds_write_b32 v199, v176\n"
"s_mov_b64 exec, 1\n"
"ds_add_u32 v226, v227 offset:60\n"
"s_mov_b64 exec, -1\n"
"s_add_u32 m0, s81, 98304\n"
"s_nop 0\n"
"buffer_load_dwordx4 v221, s[28:31], s7 offen lds\n"
"s_add_u32 m0, s81, 99328\n"
"s_nop 0\n"
"buffer_load_dwordx4 v221, s[32:35], s7 offen lds\n"
"s_add_u32 s7, s7, 114688\n"
"s_add_u32 m0, s81, 32768\n"
"s_nop 0\n"
"buffer_load_dwordx4 v221, s[28:31], s7 offen lds\n"
"s_add_u32 m0, s81, 33792\n"
"s_nop 0\n"
"buffer_load_dwordx4 v221, s[32:35], s7 offen lds\n"
"s_add_u32 s7, s7, 114688\n"
"s_cmp_eq_u32 s7, 1605632\n"
"s_cselect_b32 s7, 0, s7\n"
"s_add_u32 m0, s81, 40960\n"
"s_nop 0\n"
"buffer_load_dwordx4 v221, s[28:31], s7 offen lds\n"
"s_add_u32 m0, s81, 41984\n"
"s_nop 0\n"
"buffer_load_dwordx4 v221, s[32:35], s7 offen lds\n"
"s_add_u32 s7, s7, 114688\n"
"s_add_u32 m0, s81, 65536\n"
"s_nop 0\n"
"buffer_load_dwordx4 v221, s[28:31], s7 offen lds\n"
"s_add_u32 m0, s81, 66560\n"
"s_nop 0\n"
"buffer_load_dwordx4 v221, s[32:35], s7 offen lds\n"
"s_add_u32 s7, s7, 114688\n"
"s_cmp_eq_u32 s7, 1605632\n"
"s_cselect_b32 s7, 0, s7\n"
"s_add_u32 m0, s81, 106496\n"
"s_nop 0\n"
"buffer_load_dwordx4 v221, s[28:31], s7 offen lds\n"
"s_add_u32 m0, s81, 107520\n"
"s_nop 0\n"
"buffer_load_dwordx4 v221, s[32:35], s7 offen lds\n"
"s_add_u32 s7, s7, 114688\n"
"s_add_u32 m0, s81, 114688\n"
"s_nop 0\n"
"buffer_load_dwordx4 v221, s[28:31], s7 offen lds\n"
"s_add_u32 m0, s81, 115712\n"
"s_nop 0\n"
"buffer_load_dwordx4 v221, s[32:35], s7 offen lds\n"
"s_add_u32 s7, s7, 114688\n"
"s_cmp_eq_u32 s7, 1605632\n"
"s_cselect_b32 s7, 0, s7\n"
"s_nop 0\n"
"s_cmp_eq_u32 s76, 1\n"
"s_cselect_b32 s87, 4, 4\n"
"s_setprio 1\n"
"s_mov_b64 exec, 1\n"
"ds_add_u32 v226, v227 offset:48\n"
"s_mov_b64 exec, -1\n"
"ds_read_b32 v222, v226 offset:48\n"
"s_waitcnt lgkmcnt(0)\n"
"v_cmp_gt_u32 vcc, s87, v222\n"
"s_nop 0\n"
"s_cbranch_vccnz Lcs8_%=\n"
"Lcb9_%=:\n"
"ds_read_b64_tr_b16 v[176:177], v212 offset:0\n"
"ds_read_b64_tr_b16 v[178:179], v212 offset:512\n"
"ds_read_b64_tr_b16 v[180:181], v213 offset:0\n"
"ds_read_b64_tr_b16 v[182:183], v213 offset:512\n"
"ds_read_b64_tr_b16 v[184:185], v212 offset:1024\n"
"ds_read_b64_tr_b16 v[186:187], v212 offset:1536\n"
"ds_read_b64_tr_b16 v[188:189], v213 offset:1024\n"
"ds_read_b64_tr_b16 v[190:191], v213 offset:1536\n"
"ds_read_b64_tr_b16 v[192:193], v214 offset:0\n"
"ds_read_b64_tr_b16 v[194:195], v214 offset:512\n"
"ds_read_b64_tr_b16 v[196:197], v215 offset:0\n"
"ds_read_b64_tr_b16 v[198:199], v215 offset:512\n"
"ds_read_b64_tr_b16 v[200:201], v214 offset:1024\n"
"ds_read_b64_tr_b16 v[202:203], v214 offset:1536\n"
"s_waitcnt lgkmcnt(10)\n"
"v_mfma_f32_16x16x32_bf16 v[112:115], v[176:179], v[0:3], 0\n"
"ds_read_b64_tr_b16 v[204:205], v215 offset:1024\n"
"v_mfma_f32_16x16x32_bf16 v[208:211], v[216:219], v[176:179], 0\n"
"v_mfma_f32_16x16x32_bf16 v[144:147], v[176:179], v[4:7], 0\n"
"ds_read_b64_tr_b16 v[206:207], v215 offset:1536\n"
"v_mfma_f32_16x16x32_bf16 v[116:119], v[180:183], v[0:3], 0\n"
"ds_read_b64_tr_b16 v[176:177], v212 offset:8192\n"
"v_mfma_f32_16x16x32_bf16 v[148:151], v[180:183], v[4:7], 0\n"
"ds_read_b64_tr_b16 v[178:179], v212 offset:8704\n"
"s_waitcnt lgkmcnt(10)\n"
"v_mfma_f32_16x16x32_bf16 v[120:123], v[184:187], v[0:3], 0\n"
"ds_read_b64_tr_b16 v[180:181], v213 offset:8192\n"
"v_mfma_f32_16x16x32_bf16 v[236:239], v[216:219], v[184:187], 0\n"
"v_mfma_f32_16x16x32_bf16 v[152:155], v[184:187], v[4:7], 0\n"
"ds_read_b64_tr_b16 v[182:183], v213 offset:8704\n"
"v_mfma_f32_16x16x32_bf16 v[124:127], v[188:191], v[0:3], 0\n"
"ds_read_b64_tr_b16 v[184:185], v212 offset:9216\n"
"v_mfma_f32_16x16x32_bf16 v[156:159], v[188:191], v[4:7], 0\n"
"ds_read_b64_tr_b16 v[186:187], v212 offset:9728\n"
"s_waitcnt lgkmcnt(10)\n"
"v_mfma_f32_16x16x32_bf16 v[128:131], v[192:195], v[0:3], 0\n"
"ds_read_b64_tr_b16 v[188:189], v213 offset:9216\n"
"v_mfma_f32_16x16x32_bf16 v[160:163], v[192:195], v[4:7], 0\n"
"ds_read_b64_tr_b16 v[190:191], v213 offset:9728\n"
"v_mfma_f32_16x16x32_bf16 v[132:135], v[196:199], v[0:3], 0\n"
"ds_read_b64_tr_b16 v[192:193], v214 offset:8192\n"
"v_mfma_f32_16x16x32_bf16 v[164:167], v[196:199], v[4:7], 0\n"
"ds_read_b64_tr_b16 v[194:195], v214 offset:8704\n"
"s_waitcnt lgkmcnt(12)\n"
"v_mfma_f32_16x16x32_bf16 v[136:139], v[200:203], v[0:3], 0\n"
"ds_read_b64_tr_b16 v[196:197], v215 offset:8192\n"
"v_mfma_f32_16x16x32_bf16 v[168:171], v[200:203], v[4:7], 0\n"
"ds_read_b64_tr_b16 v[198:199], v215 offset:8704\n"
"s_waitcnt lgkmcnt(12)\n"
"v_mfma_f32_16x16x32_bf16 v[140:143], v[204:207], v[0:3], 0\n"
"ds_read_b64_tr_b16 v[200:201], v214 offset:9216\n"
"v_mfma_f32_16x16x32_bf16 v[172:175], v[204:207], v[4:7], 0\n"
"ds_read_b64_tr_b16 v[202:203], v214 offset:9728\n"
"s_waitcnt lgkmcnt(10)\n"
"v_mfma_f32_16x16x32_bf16 v[112:115], v[176:179], v[8:11], v[112:115]\n"
"ds_read_b64_tr_b16 v[204:205], v215 offset:9216\n"
"v_mfma_f32_16x16x32_bf16 v[208:211], v[216:219], v[176:179], v[208:211]\n"
"v_mfma_f32_16x16x32_bf16 v[144:147], v[176:179], v[12:15], v[144:147]\n"
"ds_read_b64_tr_b16 v[206:207], v215 offset:9728\n"
"v_mfma_f32_16x16x32_bf16 v[116:119], v[180:183], v[8:11], v[116:119]\n"
"ds_read_b64_tr_b16 v[176:177], v212 offset:16384\n"
"v_mfma_f32_16x16x32_bf16 v[148:151], v[180:183], v[12:15], v[148:151]\n"
"ds_read_b64_tr_b16 v[178:179], v212 offset:16896\n"
"s_waitcnt lgkmcnt(10)\n"
"v_mfma_f32_16x16x32_bf16 v[120:123], v[184:187], v[8:11], v[120:123]\n"
"ds_read_b64_tr_b16 v[180:181], v213 offset:16384\n"
"v_mfma_f32_16x16x32_bf16 v[236:239], v[216:219], v[184:187], v[236:239]\n"
"v_mfma_f32_16x16x32_bf16 v[152:155], v[184:187], v[12:15], v[152:155]\n"
"ds_read_b64_tr_b16 v[182:183], v213 offset:16896\n"
"v_mfma_f32_16x16x32_bf16 v[124:127], v[188:191], v[8:11], v[124:127]\n"
"ds_read_b64_tr_b16 v[184:185], v212 offset:17408\n"
"v_mfma_f32_16x16x32_bf16 v[156:159], v[188:191], v[12:15], v[156:159]\n"
"ds_read_b64_tr_b16 v[186:187], v212 offset:17920\n"
"s_waitcnt lgkmcnt(10)\n"
"v_mfma_f32_16x16x32_bf16 v[128:131], v[192:195], v[8:11], v[128:131]\n"
"ds_read_b64_tr_b16 v[188:189], v213 offset:17408\n"
"v_mfma_f32_16x16x32_bf16 v[160:163], v[192:195], v[12:15], v[160:163]\n"
"ds_read_b64_tr_b16 v[190:191], v213 offset:17920\n"
"v_mfma_f32_16x16x32_bf16 v[132:135], v[196:199], v[8:11], v[132:135]\n"
"ds_read_b64_tr_b16 v[192:193], v214 offset:16384\n"
"v_mfma_f32_16x16x32_bf16 v[164:167], v[196:199], v[12:15], v[164:167]\n"
"ds_read_b64_tr_b16 v[194:195], v214 offset:16896\n"
"s_waitcnt lgkmcnt(12)\n"
"v_mfma_f32_16x16x32_bf16 v[136:139], v[200:203], v[8:11], v[136:139]\n"
"ds_read_b64_tr_b16 v[196:197], v215 offset:16384\n"
"v_mfma_f32_16x16x32_bf16 v[168:171], v[200:203], v[12:15], v[168:171]\n"
"ds_read_b64_tr_b16 v[198:199], v215 offset:16896\n"
"s_waitcnt lgkmcnt(12)\n"
"v_mfma_f32_16x16x32_bf16 v[140:143], v[204:207], v[8:11], v[140:143]\n"
"ds_read_b64_tr_b16 v[200:201], v214 offset:17408\n"
"v_mfma_f32_16x16x32_bf16 v[172:175], v[204:207], v[12:15], v[172:175]\n"
"ds_read_b64_tr_b16 v[202:203], v214 offset:17920\n"
"s_waitcnt lgkmcnt(10)\n"
"v_mfma_f32_16x16x32_bf16 v[112:115], v[176:179], v[16:19], v[112:115]\n"
"ds_read_b64_tr_b16 v[204:205], v215 offset:17408\n"
"v_mfma_f32_16x16x32_bf16 v[208:211], v[216:219], v[176:179], v[208:211]\n"
"v_mfma_f32_16x16x32_bf16 v[144:147], v[176:179], v[20:23], v[144:147]\n"
"ds_read_b64_tr_b16 v[206:207], v215 offset:17920\n"
"v_mfma_f32_16x16x32_bf16 v[116:119], v[180:183], v[16:19], v[116:119]\n"
"ds_read_b64_tr_b16 v[176:177], v212 offset:24576\n"
"v_mfma_f32_16x16x32_bf16 v[148:151], v[180:183], v[20:23], v[148:151]\n"
"ds_read_b64_tr_b16 v[178:179], v212 offset:25088\n"
"s_waitcnt lgkmcnt(10)\n"
"v_mfma_f32_16x16x32_bf16 v[120:123], v[184:187], v[16:19], v[120:123]\n"
"ds_read_b64_tr_b16 v[180:181], v213 offset:24576\n"
"v_mfma_f32_16x16x32_bf16 v[236:239], v[216:219], v[184:187], v[236:239]\n"
"v_mfma_f32_16x16x32_bf16 v[152:155], v[184:187], v[20:23], v[152:155]\n"
"ds_read_b64_tr_b16 v[182:183], v213 offset:25088\n"
"v_mfma_f32_16x16x32_bf16 v[124:127], v[188:191], v[16:19], v[124:127]\n"
"ds_read_b64_tr_b16 v[184:185], v212 offset:25600\n"
"v_mfma_f32_16x16x32_bf16 v[156:159], v[188:191], v[20:23], v[156:159]\n"
"ds_read_b64_tr_b16 v[186:187], v212 offset:26112\n"
"s_waitcnt vmcnt(14)\n"
"s_mov_b64 exec, 1\n"
"ds_add_u32 v226, v227 offset:0\n"
"s_mov_b64 exec, -1\n"
"s_waitcnt lgkmcnt(11)\n"
"v_mfma_f32_16x16x32_bf16 v[128:131], v[192:195], v[16:19], v[128:131]\n"
"ds_read_b64_tr_b16 v[188:189], v213 offset:25600\n"
"v_mfma_f32_16x16x32_bf16 v[160:163], v[192:195], v[20:23], v[160:163]\n"
"ds_read_b64_tr_b16 v[190:191], v213 offset:26112\n"
"v_mfma_f32_16x16x32_bf16 v[132:135], v[196:199], v[16:19], v[132:135]\n"
"ds_read_b64_tr_b16 v[192:193], v214 offset:24576\n"
"v_mfma_f32_16x16x32_bf16 v[164:167], v[196:199], v[20:23], v[164:167]\n"
"ds_read_b64_tr_b16 v[194:195], v214 offset:25088\n"
"s_waitcnt lgkmcnt(13)\n"
"v_mfma_f32_16x16x32_bf16 v[136:139], v[200:203], v[16:19], v[136:139]\n"
"ds_read_b64_tr_b16 v[196:197], v215 offset:24576\n"
"v_mfma_f32_16x16x32_bf16 v[168:171], v[200:203], v[20:23], v[168:171]\n"
"ds_read_b64_tr_b16 v[198:199], v215 offset:25088\n"
"ds_read_b32 v222, v226 offset:0\n"
"s_waitcnt lgkmcnt(14)\n"
"v_mfma_f32_16x16x32_bf16 v[140:143], v[204:207], v[16:19], v[140:143]\n"
"ds_read_b64_tr_b16 v[200:201], v214 offset:25600\n"
"v_mfma_f32_16x16x32_bf16 v[172:175], v[204:207], v[20:23], v[172:175]\n"
"ds_read_b64_tr_b16 v[202:203], v214 offset:26112\n"
"s_waitcnt lgkmcnt(12)\n"
"v_mfma_f32_16x16x32_bf16 v[112:115], v[176:179], v[24:27], v[112:115]\n"
"ds_read_b64_tr_b16 v[204:205], v215 offset:25600\n"
"v_mfma_f32_16x16x32_bf16 v[208:211], v[216:219], v[176:179], v[208:211]\n"
"v_mfma_f32_16x16x32_bf16 v[144:147], v[176:179], v[28:31], v[144:147]\n"
"ds_read_b64_tr_b16 v[206:207], v215 offset:26112\n"
"s_waitcnt lgkmcnt(4)\n"
"v_cmp_gt_u32 vcc, 4, v222\n"
"s_nop 0\n"
"s_cbranch_vccnz Lcs11_%=\n"
"Lcb12_%=:\n"
"v_mfma_f32_16x16x32_bf16 v[116:119], v[180:183], v[24:27], v[116:119]\n"
"v_add_u32 v212, 4294860800, v212\n"
"v_add_u32 v213, 4294860800, v213\n"
"v_add_u32 v214, 4294860800, v214\n"
"v_add_u32 v215, 4294860800, v215\n"
"ds_read_b64_tr_b16 v[176:177], v212 offset:0\n"
"v_mfma_f32_16x16x32_bf16 v[148:151], v[180:183], v[28:31], v[148:151]\n"
"ds_read_b64_tr_b16 v[178:179], v212 offset:512\n"
"v_mfma_f32_16x16x32_bf16 v[120:123], v[184:187], v[24:27], v[120:123]\n"
"ds_read_b64_tr_b16 v[180:181], v213 offset:0\n"
"v_mfma_f32_16x16x32_bf16 v[236:239], v[216:219], v[184:187], v[236:239]\n"
"v_mfma_f32_16x16x32_bf16 v[152:155], v[184:187], v[28:31], v[152:155]\n"
"ds_read_b64_tr_b16 v[182:183], v213 offset:512\n"
"v_mfma_f32_16x16x32_bf16 v[124:127], v[188:191], v[24:27], v[124:127]\n"
"ds_read_b64_tr_b16 v[184:185], v212 offset:1024\n"
"v_mfma_f32_16x16x32_bf16 v[156:159], v[188:191], v[28:31], v[156:159]\n"
"ds_read_b64_tr_b16 v[186:187], v212 offset:1536\n"
"v_mfma_f32_16x16x32_bf16 v[128:131], v[192:195], v[24:27], v[128:131]\n"
"ds_read_b64_tr_b16 v[188:189], v213 offset:1024\n"
"v_mfma_f32_16x16x32_bf16 v[160:163], v[192:195], v[28:31], v[160:163]\n"
"ds_read_b64_tr_b16 v[190:191], v213 offset:1536\n"
"v_mfma_f32_16x16x32_bf16 v[132:135], v[196:199], v[24:27], v[132:135]\n"
"ds_read_b64_tr_b16 v[192:193], v214 offset:0\n"
"v_mfma_f32_16x16x32_bf16 v[164:167], v[196:199], v[28:31], v[164:167]\n"
"ds_read_b64_tr_b16 v[194:195], v214 offset:512\n"
"s_waitcnt lgkmcnt(12)\n"
"v_mfma_f32_16x16x32_bf16 v[136:139], v[200:203], v[24:27], v[136:139]\n"
"ds_read_b64_tr_b16 v[196:197], v215 offset:0\n"
"v_mfma_f32_16x16x32_bf16 v[168:171], v[200:203], v[28:31], v[168:171]\n"
"ds_read_b64_tr_b16 v[198:199], v215 offset:512\n"
"s_waitcnt lgkmcnt(12)\n"
"v_mfma_f32_16x16x32_bf16 v[140:143], v[204:207], v[24:27], v[140:143]\n"
"ds_read_b64_tr_b16 v[200:201], v214 offset:1024\n"
"v_mfma_f32_16x16x32_bf16 v[172:175], v[204:207], v[28:31], v[172:175]\n"
"ds_read_b64_tr_b16 v[202:203], v214 offset:1536\n"
"s_waitcnt lgkmcnt(10)\n"
"v_mfma_f32_16x16x32_bf16 v[112:115], v[176:179], v[32:35], v[112:115]\n"
"ds_read_b64_tr_b16 v[204:205], v215 offset:1024\n"
"v_mfma_f32_16x16x32_bf16 v[208:211], v[216:219], v[176:179], v[208:211]\n"
"v_mfma_f32_16x16x32_bf16 v[144:147], v[176:179], v[36:39], v[144:147]\n"
"ds_read_b64_tr_b16 v[206:207], v215 offset:1536\n"
"v_mfma_f32_16x16x32_bf16 v[116:119], v[180:183], v[32:35], v[116:119]\n"
"ds_read_b64_tr_b16 v[176:177], v212 offset:8192\n"
"v_mfma_f32_16x16x32_bf16 v[148:151], v[180:183], v[36:39], v[148:151]\n"
"ds_read_b64_tr_b16 v[178:179], v212 offset:8704\n"
"s_waitcnt lgkmcnt(10)\n"
"v_mfma_f32_16x16x32_bf16 v[120:123], v[184:187], v[32:35], v[120:123]\n"
"ds_read_b64_tr_b16 v[180:181], v213 offset:8192\n"
"v_mfma_f32_16x16x32_bf16 v[236:239], v[216:219], v[184:187], v[236:239]\n"
"v_mfma_f32_16x16x32_bf16 v[152:155], v[184:187], v[36:39], v[152:155]\n"
"ds_read_b64_tr_b16 v[182:183], v213 offset:8704\n"
"v_mfma_f32_16x16x32_bf16 v[124:127], v[188:191], v[32:35], v[124:127]\n"
"ds_read_b64_tr_b16 v[184:185], v212 offset:9216\n"
"v_mfma_f32_16x16x32_bf16 v[156:159], v[188:191], v[36:39], v[156:159]\n"
"ds_read_b64_tr_b16 v[186:187], v212 offset:9728\n"
"s_waitcnt lgkmcnt(10)\n"
"v_mfma_f32_16x16x32_bf16 v[128:131], v[192:195], v[32:35], v[128:131]\n"
"ds_read_b64_tr_b16 v[188:189], v213 offset:9216\n"
"v_mfma_f32_16x16x32_bf16 v[160:163], v[192:195], v[36:39], v[160:163]\n"
"ds_read_b64_tr_b16 v[190:191], v213 offset:9728\n"
"v_mfma_f32_16x16x32_bf16 v[132:135], v[196:199], v[32:35], v[132:135]\n"
"ds_read_b64_tr_b16 v[192:193], v214 offset:8192\n"
"v_mfma_f32_16x16x32_bf16 v[164:167], v[196:199], v[36:39], v[164:167]\n"
"ds_read_b64_tr_b16 v[194:195], v214 offset:8704\n"
"s_waitcnt lgkmcnt(12)\n"
"v_mfma_f32_16x16x32_bf16 v[136:139], v[200:203], v[32:35], v[136:139]\n"
"ds_read_b64_tr_b16 v[196:197], v215 offset:8192\n"
"v_mfma_f32_16x16x32_bf16 v[168:171], v[200:203], v[36:39], v[168:171]\n"
"ds_read_b64_tr_b16 v[198:199], v215 offset:8704\n"
"s_waitcnt lgkmcnt(12)\n"
"v_mfma_f32_16x16x32_bf16 v[140:143], v[204:207], v[32:35], v[140:143]\n"
"ds_read_b64_tr_b16 v[200:201], v214 offset:9216\n"
"v_mfma_f32_16x16x32_bf16 v[172:175], v[204:207], v[36:39], v[172:175]\n"
"ds_read_b64_tr_b16 v[202:203], v214 offset:9728\n"
"s_waitcnt lgkmcnt(10)\n"
"v_mfma_f32_16x16x32_bf16 v[112:115], v[176:179], v[40:43], v[112:115]\n"
"ds_read_b64_tr_b16 v[204:205], v215 offset:9216\n"
"v_mfma_f32_16x16x32_bf16 v[208:211], v[216:219], v[176:179], v[208:211]\n"
"v_mfma_f32_16x16x32_bf16 v[144:147], v[176:179], v[44:47], v[144:147]\n"
"ds_read_b64_tr_b16 v[206:207], v215 offset:9728\n"
"v_mfma_f32_16x16x32_bf16 v[116:119], v[180:183], v[40:43], v[116:119]\n"
"ds_read_b64_tr_b16 v[176:177], v212 offset:40960\n"
"v_mfma_f32_16x16x32_bf16 v[148:151], v[180:183], v[44:47], v[148:151]\n"
"ds_read_b64_tr_b16 v[178:179], v212 offset:41472\n"
"s_waitcnt lgkmcnt(10)\n"
"v_mfma_f32_16x16x32_bf16 v[120:123], v[184:187], v[40:43], v[120:123]\n"
"ds_read_b64_tr_b16 v[180:181], v213 offset:40960\n"
"v_mfma_f32_16x16x32_bf16 v[236:239], v[216:219], v[184:187], v[236:239]\n"
"v_mfma_f32_16x16x32_bf16 v[152:155], v[184:187], v[44:47], v[152:155]\n"
"ds_read_b64_tr_b16 v[182:183], v213 offset:41472\n"
"v_mfma_f32_16x16x32_bf16 v[124:127], v[188:191], v[40:43], v[124:127]\n"
"ds_read_b64_tr_b16 v[184:185], v212 offset:41984\n"
"v_mfma_f32_16x16x32_bf16 v[156:159], v[188:191], v[44:47], v[156:159]\n"
"ds_read_b64_tr_b16 v[186:187], v212 offset:42496\n"
"s_waitcnt vmcnt(8)\n"
"s_mov_b64 exec, 1\n"
"ds_add_u32 v226, v227 offset:4\n"
"s_mov_b64 exec, -1\n"
"s_waitcnt lgkmcnt(11)\n"
"v_mfma_f32_16x16x32_bf16 v[128:131], v[192:195], v[40:43], v[128:131]\n"
"ds_read_b64_tr_b16 v[188:189], v213 offset:41984\n"
"v_mfma_f32_16x16x32_bf16 v[160:163], v[192:195], v[44:47], v[160:163]\n"
"ds_read_b64_tr_b16 v[190:191], v213 offset:42496\n"
"v_mfma_f32_16x16x32_bf16 v[132:135], v[196:199], v[40:43], v[132:135]\n"
"ds_read_b64_tr_b16 v[192:193], v214 offset:40960\n"
"v_mfma_f32_16x16x32_bf16 v[164:167], v[196:199], v[44:47], v[164:167]\n"
"ds_read_b64_tr_b16 v[194:195], v214 offset:41472\n"
"s_waitcnt lgkmcnt(13)\n"
"v_mfma_f32_16x16x32_bf16 v[136:139], v[200:203], v[40:43], v[136:139]\n"
"ds_read_b64_tr_b16 v[196:197], v215 offset:40960\n"
"v_mfma_f32_16x16x32_bf16 v[168:171], v[200:203], v[44:47], v[168:171]\n"
"ds_read_b64_tr_b16 v[198:199], v215 offset:41472\n"
"ds_read_b32 v222, v226 offset:4\n"
"s_waitcnt lgkmcnt(14)\n"
"v_mfma_f32_16x16x32_bf16 v[140:143], v[204:207], v[40:43], v[140:143]\n"
"ds_read_b64_tr_b16 v[200:201], v214 offset:41984\n"
"v_mfma_f32_16x16x32_bf16 v[172:175], v[204:207], v[44:47], v[172:175]\n"
"ds_read_b64_tr_b16 v[202:203], v214 offset:42496\n"
"s_waitcnt lgkmcnt(12)\n"
"v_mfma_f32_16x16x32_bf16 v[112:115], v[176:179], v[48:51], v[112:115]\n"
"ds_read_b64_tr_b16 v[204:205], v215 offset:41984\n"
"v_mfma_f32_16x16x32_bf16 v[208:211], v[216:219], v[176:179], v[208:211]\n"
"v_mfma_f32_16x16x32_bf16 v[144:147], v[176:179], v[52:55], v[144:147]\n"
"ds_read_b64_tr_b16 v[206:207], v215 offset:42496\n"
"s_waitcnt lgkmcnt(4)\n"
"v_cmp_gt_u32 vcc, 4, v222\n"
"s_nop 0\n"
"s_cbranch_vccnz Lcs14_%=\n"
"Lcb15_%=:\n"
"v_mfma_f32_16x16x32_bf16 v[116:119], v[180:183], v[48:51], v[116:119]\n"
"v_add_u32 v212, 73728, v212\n"
"v_add_u32 v213, 73728, v213\n"
"v_add_u32 v214, 73728, v214\n"
"v_add_u32 v215, 73728, v215\n"
"ds_read_b64_tr_b16 v[176:177], v212 offset:0\n"
"v_mfma_f32_16x16x32_bf16 v[148:151], v[180:183], v[52:55], v[148:151]\n"
"ds_read_b64_tr_b16 v[178:179], v212 offset:512\n"
"s_mov_b32 s81, s82\n"
"s_lshl_b32 s84, s70, 12\n"
"v_add_u32 v3, s84, v245\n"
"s_cmp_eq_u32 s76, 1\n"
"s_cselect_b64 s[16:17], 0, -1\n"
"v_mfma_f32_16x16x32_bf16 v[120:123], v[184:187], v[48:51], v[120:123]\n"
"ds_read_b64_tr_b16 v[180:181], v213 offset:0\n"
"v_mfma_f32_16x16x32_bf16 v[236:239], v[216:219], v[184:187], v[236:239]\n"
"v_mfma_f32_16x16x32_bf16 v[152:155], v[184:187], v[52:55], v[152:155]\n"
"ds_read_b64_tr_b16 v[182:183], v213 offset:512\n"
"v_lshl_add_u32 v4, v228, 6, s14\n"
"v_lshl_add_u32 v4, v229, 4, v4\n"
"v_mfma_f32_16x16x32_bf16 v[124:127], v[188:191], v[48:51], v[124:127]\n"
"ds_read_b64_tr_b16 v[184:185], v212 offset:1024\n"
"v_mfma_f32_16x16x32_bf16 v[156:159], v[188:191], v[52:55], v[156:159]\n"
"ds_read_b64_tr_b16 v[186:187], v212 offset:1536\n"
"v_xor_b32 v5, 32, v4\n"
"v_mfma_f32_16x16x32_bf16 v[128:131], v[192:195], v[48:51], v[128:131]\n"
"ds_read_b64_tr_b16 v[188:189], v213 offset:1024\n"
"v_mfma_f32_16x16x32_bf16 v[160:163], v[192:195], v[52:55], v[160:163]\n"
"ds_read_b64_tr_b16 v[190:191], v213 offset:1536\n"
"v_xor_b32 v6, 128, v4\n"
"v_mfma_f32_16x16x32_bf16 v[132:135], v[196:199], v[48:51], v[132:135]\n"
"ds_read_b64_tr_b16 v[192:193], v214 offset:0\n"
"v_mfma_f32_16x16x32_bf16 v[164:167], v[196:199], v[52:55], v[164:167]\n"
"ds_read_b64_tr_b16 v[194:195], v214 offset:512\n"
"v_xor_b32 v7, 160, v4\n"
"s_waitcnt lgkmcnt(12)\n"
"v_mfma_f32_16x16x32_bf16 v[136:139], v[200:203], v[48:51], v[136:139]\n"
"ds_read_b64_tr_b16 v[196:197], v215 offset:0\n"
"v_mfma_f32_16x16x32_bf16 v[168:171], v[200:203], v[52:55], v[168:171]\n"
"ds_read_b64_tr_b16 v[198:199], v215 offset:512\n"
"s_lshl_b32 s84, s70, 7\n"
"s_add_u32 s84, s84, 159744\n"
"v_lshl_add_u32 v2, v233, 2, s84\n"
"s_bitcmp1_b32 s70, 0\n"
"s_cselect_b64 s[18:19], -1, 0\n"
"s_waitcnt lgkmcnt(12)\n"
"v_mfma_f32_16x16x32_bf16 v[140:143], v[204:207], v[48:51], v[140:143]\n"
"ds_read_b64_tr_b16 v[200:201], v214 offset:1024\n"
"v_mfma_f32_16x16x32_bf16 v[172:175], v[204:207], v[52:55], v[172:175]\n"
"ds_read_b64_tr_b16 v[202:203], v214 offset:1536\n"
"s_waitcnt lgkmcnt(10)\n"
"v_mfma_f32_16x16x32_bf16 v[112:115], v[176:179], v[56:59], v[112:115]\n"
"ds_read_b64_tr_b16 v[204:205], v215 offset:1024\n"
"v_mfma_f32_16x16x32_bf16 v[208:211], v[216:219], v[176:179], v[208:211]\n"
"v_mfma_f32_16x16x32_bf16 v[144:147], v[176:179], v[60:63], v[144:147]\n"
"ds_read_b64_tr_b16 v[206:207], v215 offset:1536\n"
"v_mfma_f32_16x16x32_bf16 v[116:119], v[180:183], v[56:59], v[116:119]\n"
"ds_read_b64_tr_b16 v[176:177], v212 offset:8192\n"
"v_mfma_f32_16x16x32_bf16 v[148:151], v[180:183], v[60:63], v[148:151]\n"
"ds_read_b64_tr_b16 v[178:179], v212 offset:8704\n"
"s_waitcnt lgkmcnt(10)\n"
"v_mfma_f32_16x16x32_bf16 v[120:123], v[184:187], v[56:59], v[120:123]\n"
"ds_read_b64_tr_b16 v[180:181], v213 offset:8192\n"
"v_mfma_f32_16x16x32_bf16 v[236:239], v[216:219], v[184:187], v[236:239]\n"
"v_mfma_f32_16x16x32_bf16 v[152:155], v[184:187], v[60:63], v[152:155]\n"
"ds_read_b64_tr_b16 v[182:183], v213 offset:8704\n"
"v_mfma_f32_16x16x32_bf16 v[124:127], v[188:191], v[56:59], v[124:127]\n"
"ds_read_b64_tr_b16 v[184:185], v212 offset:9216\n"
"v_mfma_f32_16x16x32_bf16 v[156:159], v[188:191], v[60:63], v[156:159]\n"
"ds_read_b64_tr_b16 v[186:187], v212 offset:9728\n"
"s_waitcnt lgkmcnt(10)\n"
"v_mfma_f32_16x16x32_bf16 v[128:131], v[192:195], v[56:59], v[128:131]\n"
"ds_read_b64_tr_b16 v[188:189], v213 offset:9216\n"
"v_mfma_f32_16x16x32_bf16 v[160:163], v[192:195], v[60:63], v[160:163]\n"
"ds_read_b64_tr_b16 v[190:191], v213 offset:9728\n"
"v_mfma_f32_16x16x32_bf16 v[132:135], v[196:199], v[56:59], v[132:135]\n"
"ds_read_b64_tr_b16 v[192:193], v214 offset:8192\n"
"v_mfma_f32_16x16x32_bf16 v[164:167], v[196:199], v[60:63], v[164:167]\n"
"ds_read_b64_tr_b16 v[194:195], v214 offset:8704\n"
"s_waitcnt lgkmcnt(12)\n"
"v_mfma_f32_16x16x32_bf16 v[136:139], v[200:203], v[56:59], v[136:139]\n"
"ds_read_b64_tr_b16 v[196:197], v215 offset:8192\n"
"v_mfma_f32_16x16x32_bf16 v[168:171], v[200:203], v[60:63], v[168:171]\n"
"ds_read_b64_tr_b16 v[198:199], v215 offset:8704\n"
"s_waitcnt lgkmcnt(12)\n"
"v_mfma_f32_16x16x32_bf16 v[140:143], v[204:207], v[56:59], v[140:143]\n"
"ds_read_b64_tr_b16 v[200:201], v214 offset:9216\n"
"v_mfma_f32_16x16x32_bf16 v[172:175], v[204:207], v[60:63], v[172:175]\n"
"ds_read_b64_tr_b16 v[202:203], v214 offset:9728\n"
"s_waitcnt lgkmcnt(10)\n"
"v_mfma_f32_16x16x32_bf16 v[112:115], v[176:179], v[64:67], v[112:115]\n"
"ds_read_b64_tr_b16 v[204:205], v215 offset:9216\n"
"v_mfma_f32_16x16x32_bf16 v[208:211], v[216:219], v[176:179], v[208:211]\n"
"v_mfma_f32_16x16x32_bf16 v[144:147], v[176:179], v[68:71], v[144:147]\n"
"ds_read_b64_tr_b16 v[206:207], v215 offset:9728\n"
"v_mfma_f32_16x16x32_bf16 v[116:119], v[180:183], v[64:67], v[116:119]\n"
"v_add_u32 v212, 4294909952, v212\n"
"v_add_u32 v213, 4294909952, v213\n"
"v_add_u32 v214, 4294909952, v214\n"
"v_add_u32 v215, 4294909952, v215\n"
"ds_read_b64_tr_b16 v[176:177], v212 offset:0\n"
"v_mfma_f32_16x16x32_bf16 v[148:151], v[180:183], v[68:71], v[148:151]\n"
"ds_read_b64_tr_b16 v[178:179], v212 offset:512\n"
"s_waitcnt lgkmcnt(10)\n"
"v_mfma_f32_16x16x32_bf16 v[120:123], v[184:187], v[64:67], v[120:123]\n"
"ds_read_b64_tr_b16 v[180:181], v213 offset:0\n"
"v_mfma_f32_16x16x32_bf16 v[236:239], v[216:219], v[184:187], v[236:239]\n"
"v_mfma_f32_16x16x32_bf16 v[152:155], v[184:187], v[68:71], v[152:155]\n"
"ds_read_b64_tr_b16 v[182:183], v213 offset:512\n"
"v_mfma_f32_16x16x32_bf16 v[124:127], v[188:191], v[64:67], v[124:127]\n"
"ds_read_b64_tr_b16 v[184:185], v212 offset:1024\n"
"v_mfma_f32_16x16x32_bf16 v[156:159], v[188:191], v[68:71], v[156:159]\n"
"ds_read_b64_tr_b16 v[186:187], v212 offset:1536\n"
"s_waitcnt vmcnt(0)\n"
"s_mov_b64 exec, 1\n"
"ds_add_u32 v226, v227 offset:8\n"
"s_mov_b64 exec, -1\n"
"s_waitcnt lgkmcnt(11)\n"
"v_mfma_f32_16x16x32_bf16 v[128:131], v[192:195], v[64:67], v[128:131]\n"
"ds_read_b64_tr_b16 v[188:189], v213 offset:1024\n"
"v_mfma_f32_16x16x32_bf16 v[160:163], v[192:195], v[68:71], v[160:163]\n"
"ds_read_b64_tr_b16 v[190:191], v213 offset:1536\n"
"v_mfma_f32_16x16x32_bf16 v[132:135], v[196:199], v[64:67], v[132:135]\n"
"ds_read_b64_tr_b16 v[192:193], v214 offset:0\n"
"v_mfma_f32_16x16x32_bf16 v[164:167], v[196:199], v[68:71], v[164:167]\n"
"ds_read_b64_tr_b16 v[194:195], v214 offset:512\n"
"s_waitcnt lgkmcnt(13)\n"
"v_mfma_f32_16x16x32_bf16 v[136:139], v[200:203], v[64:67], v[136:139]\n"
"ds_read_b64_tr_b16 v[196:197], v215 offset:0\n"
"v_mfma_f32_16x16x32_bf16 v[168:171], v[200:203], v[68:71], v[168:171]\n"
"ds_read_b64_tr_b16 v[198:199], v215 offset:512\n"
"ds_read_b32 v222, v226 offset:8\n"
"s_waitcnt lgkmcnt(14)\n"
"v_mfma_f32_16x16x32_bf16 v[140:143], v[204:207], v[64:67], v[140:143]\n"
"ds_read_b64_tr_b16 v[200:201], v214 offset:1024\n"
"v_mfma_f32_16x16x32_bf16 v[172:175], v[204:207], v[68:71], v[172:175]\n"
"ds_read_b64_tr_b16 v[202:203], v214 offset:1536\n"
"s_waitcnt lgkmcnt(12)\n"
"v_mfma_f32_16x16x32_bf16 v[112:115], v[176:179], v[72:75], v[112:115]\n"
"ds_read_b64_tr_b16 v[204:205], v215 offset:1024\n"
"v_mfma_f32_16x16x32_bf16 v[208:211], v[216:219], v[176:179], v[208:211]\n"
"v_mfma_f32_16x16x32_bf16 v[144:147], v[176:179], v[76:79], v[144:147]\n"
"ds_read_b64_tr_b16 v[206:207], v215 offset:1536\n"
"s_waitcnt lgkmcnt(4)\n"
"v_cmp_gt_u32 vcc, 4, v222\n"
"s_nop 0\n"
"s_cbranch_vccnz Lcs17_%=\n"
"Lcb18_%=:\n"
"v_mfma_f32_16x16x32_bf16 v[116:119], v[180:183], v[72:75], v[116:119]\n"
"ds_read_b64_tr_b16 v[176:177], v212 offset:8192\n"
"v_mfma_f32_16x16x32_bf16 v[148:151], v[180:183], v[76:79], v[148:151]\n"
"ds_read_b64_tr_b16 v[178:179], v212 offset:8704\n"
"v_mfma_f32_16x16x32_bf16 v[120:123], v[184:187], v[72:75], v[120:123]\n"
"ds_read_b64_tr_b16 v[180:181], v213 offset:8192\n"
"v_mfma_f32_16x16x32_bf16 v[236:239], v[216:219], v[184:187], v[236:239]\n"
"v_mfma_f32_16x16x32_bf16 v[152:155], v[184:187], v[76:79], v[152:155]\n"
"ds_read_b64_tr_b16 v[182:183], v213 offset:8704\n"
"v_mfma_f32_16x16x32_bf16 v[124:127], v[188:191], v[72:75], v[124:127]\n"
"ds_read_b64_tr_b16 v[184:185], v212 offset:9216\n"
"v_mfma_f32_16x16x32_bf16 v[156:159], v[188:191], v[76:79], v[156:159]\n"
"ds_read_b64_tr_b16 v[186:187], v212 offset:9728\n"
"v_mfma_f32_16x16x32_bf16 v[128:131], v[192:195], v[72:75], v[128:131]\n"
"ds_read_b64_tr_b16 v[188:189], v213 offset:9216\n"
"v_mfma_f32_16x16x32_bf16 v[160:163], v[192:195], v[76:79], v[160:163]\n"
"ds_read_b64_tr_b16 v[190:191], v213 offset:9728\n"
"v_mfma_f32_16x16x32_bf16 v[132:135], v[196:199], v[72:75], v[132:135]\n"
"ds_read_b64_tr_b16 v[192:193], v214 offset:8192\n"
"v_mfma_f32_16x16x32_bf16 v[164:167], v[196:199], v[76:79], v[164:167]\n"
"ds_read_b64_tr_b16 v[194:195], v214 offset:8704\n"
"s_waitcnt lgkmcnt(12)\n"
"v_mfma_f32_16x16x32_bf16 v[136:139], v[200:203], v[72:75], v[136:139]\n"
"ds_read_b64_tr_b16 v[196:197], v215 offset:8192\n"
"v_mfma_f32_16x16x32_bf16 v[168:171], v[200:203], v[76:79], v[168:171]\n"
"ds_read_b64_tr_b16 v[198:199], v215 offset:8704\n"
"s_waitcnt lgkmcnt(12)\n"
"v_mfma_f32_16x16x32_bf16 v[140:143], v[204:207], v[72:75], v[140:143]\n"
"ds_read_b64_tr_b16 v[200:201], v214 offset:9216\n"
"v_mfma_f32_16x16x32_bf16 v[172:175], v[204:207], v[76:79], v[172:175]\n"
"ds_read_b64_tr_b16 v[202:203], v214 offset:9728\n"
"s_waitcnt lgkmcnt(10)\n"
"v_mfma_f32_16x16x32_bf16 v[112:115], v[176:179], v[80:83], v[112:115]\n"
"ds_read_b64_tr_b16 v[204:205], v215 offset:9216\n"
"v_mfma_f32_16x16x32_bf16 v[208:211], v[216:219], v[176:179], v[208:211]\n"
"v_mfma_f32_16x16x32_bf16 v[144:147], v[176:179], v[84:87], v[144:147]\n"
"ds_read_b64_tr_b16 v[206:207], v215 offset:9728\n"
"v_mfma_f32_16x16x32_bf16 v[116:119], v[180:183], v[80:83], v[116:119]\n"
"ds_read_b64_tr_b16 v[176:177], v212 offset:32768\n"
"v_mfma_f32_16x16x32_bf16 v[148:151], v[180:183], v[84:87], v[148:151]\n"
"ds_read_b64_tr_b16 v[178:179], v212 offset:33280\n"
"s_waitcnt lgkmcnt(10)\n"
"v_mfma_f32_16x16x32_bf16 v[120:123], v[184:187], v[80:83], v[120:123]\n"
"ds_read_b64_tr_b16 v[180:181], v213 offset:32768\n"
"v_mfma_f32_16x16x32_bf16 v[236:239], v[216:219], v[184:187], v[236:239]\n"
"v_mfma_f32_16x16x32_bf16 v[152:155], v[184:187], v[84:87], v[152:155]\n"
"ds_read_b64_tr_b16 v[182:183], v213 offset:33280\n"
"ds_read_b32 v222, v226 offset:72\n"
"v_mfma_f32_16x16x32_bf16 v[124:127], v[188:191], v[80:83], v[124:127]\n"
"ds_read_b64_tr_b16 v[184:185], v212 offset:33792\n"
"v_mfma_f32_16x16x32_bf16 v[156:159], v[188:191], v[84:87], v[156:159]\n"
"ds_read_b64_tr_b16 v[186:187], v212 offset:34304\n"
"s_waitcnt lgkmcnt(11)\n"
"v_mfma_f32_16x16x32_bf16 v[128:131], v[192:195], v[80:83], v[128:131]\n"
"ds_read_b64_tr_b16 v[188:189], v213 offset:33792\n"
"v_mfma_f32_16x16x32_bf16 v[160:163], v[192:195], v[84:87], v[160:163]\n"
"ds_read_b64_tr_b16 v[190:191], v213 offset:34304\n"
"v_mfma_f32_16x16x32_bf16 v[132:135], v[196:199], v[80:83], v[132:135]\n"
"ds_read_b64_tr_b16 v[192:193], v214 offset:32768\n"
"v_mfma_f32_16x16x32_bf16 v[164:167], v[196:199], v[84:87], v[164:167]\n"
"ds_read_b64_tr_b16 v[194:195], v214 offset:33280\n"
"s_waitcnt lgkmcnt(13)\n"
"v_mfma_f32_16x16x32_bf16 v[136:139], v[200:203], v[80:83], v[136:139]\n"
"ds_read_b64_tr_b16 v[196:197], v215 offset:32768\n"
"v_mfma_f32_16x16x32_bf16 v[168:171], v[200:203], v[84:87], v[168:171]\n"
"ds_read_b64_tr_b16 v[198:199], v215 offset:33280\n"
"s_waitcnt lgkmcnt(8)\n"
"v_cmp_gt_u32 vcc, 4, v222\n"
"s_nop 0\n"
"s_cbranch_vccnz Lcs20_%=\n"
"Lcb21_%=:\n"
"v_mfma_f32_16x16x32_bf16 v[140:143], v[204:207], v[80:83], v[140:143]\n"
"ds_read_b64_tr_b16 v[200:201], v214 offset:33792\n"
"v_mfma_f32_16x16x32_bf16 v[172:175], v[204:207], v[84:87], v[172:175]\n"
"ds_read_b64_tr_b16 v[202:203], v214 offset:34304\n"
"v_mfma_f32_16x16x32_bf16 v[112:115], v[176:179], v[88:91], v[112:115]\n"
"ds_read_b64_tr_b16 v[204:205], v215 offset:33792\n"
"v_mfma_f32_16x16x32_bf16 v[208:211], v[216:219], v[176:179], v[208:211]\n"
"v_mfma_f32_16x16x32_bf16 v[144:147], v[176:179], v[92:95], v[144:147]\n"
"ds_read_b64_tr_b16 v[206:207], v215 offset:34304\n"
"v_mfma_f32_16x16x32_bf16 v[116:119], v[180:183], v[88:91], v[116:119]\n"
"v_add_u32 v212, 73728, v212\n"
"v_add_u32 v213, 73728, v213\n"
"v_add_u32 v214, 73728, v214\n"
"v_add_u32 v215, 73728, v215\n"
"ds_read_b64_tr_b16 v[176:177], v212 offset:0\n"
"v_mfma_f32_16x16x32_bf16 v[148:151], v[180:183], v[92:95], v[148:151]\n"
"ds_read_b64_tr_b16 v[178:179], v212 offset:512\n"
"ds_read2_b32 v[64:65], v2 offset1:16\n"
"ds_read2_b32 v[68:69], v2 offset0:128 offset1:144\n"
"s_waitcnt lgkmcnt(12)\n"
"v_mfma_f32_16x16x32_bf16 v[120:123], v[184:187], v[88:91], v[120:123]\n"
"ds_read_b64_tr_b16 v[180:181], v213 offset:0\n"
"v_mfma_f32_16x16x32_bf16 v[236:239], v[216:219], v[184:187], v[236:239]\n"
"v_mfma_f32_16x16x32_bf16 v[152:155], v[184:187], v[92:95], v[152:155]\n"
"ds_read_b64_tr_b16 v[182:183], v213 offset:512\n"
"v_mfma_f32_16x16x32_bf16 v[124:127], v[188:191], v[88:91], v[124:127]\n"
"ds_read_b64_tr_b16 v[184:185], v212 offset:1024\n"
"v_mfma_f32_16x16x32_bf16 v[156:159], v[188:191], v[92:95], v[156:159]\n"
"ds_read_b64_tr_b16 v[186:187], v212 offset:1536\n"
"s_waitcnt lgkmcnt(12)\n"
"v_mfma_f32_16x16x32_bf16 v[128:131], v[192:195], v[88:91], v[128:131]\n"
"ds_read_b64_tr_b16 v[188:189], v213 offset:1024\n"
"v_mfma_f32_16x16x32_bf16 v[160:163], v[192:195], v[92:95], v[160:163]\n"
"ds_read_b64_tr_b16 v[190:191], v213 offset:1536\n"
"v_mfma_f32_16x16x32_bf16 v[132:135], v[196:199], v[88:91], v[132:135]\n"
"ds_read_b64_tr_b16 v[192:193], v214 offset:0\n"
"v_mfma_f32_16x16x32_bf16 v[164:167], v[196:199], v[92:95], v[164:167]\n"
"ds_read_b64_tr_b16 v[194:195], v214 offset:512\n"
"s_waitcnt lgkmcnt(14)\n"
"v_mfma_f32_16x16x32_bf16 v[136:139], v[200:203], v[88:91], v[136:139]\n"
"ds_read_b64_tr_b16 v[196:197], v215 offset:0\n"
"v_mfma_f32_16x16x32_bf16 v[168:171], v[200:203], v[92:95], v[168:171]\n"
"ds_read_b64_tr_b16 v[198:199], v215 offset:512\n"
"s_waitcnt lgkmcnt(14)\n"
"v_mfma_f32_16x16x32_bf16 v[140:143], v[204:207], v[88:91], v[140:143]\n"
"ds_read_b64_tr_b16 v[200:201], v214 offset:1024\n"
"v_mfma_f32_16x16x32_bf16 v[172:175], v[204:207], v[92:95], v[172:175]\n"
"ds_read_b64_tr_b16 v[202:203], v214 offset:1536\n"
"s_waitcnt lgkmcnt(10)\n"
"v_mfma_f32_16x16x32_bf16 v[112:115], v[176:179], v[96:99], v[112:115]\n"
"ds_read_b64_tr_b16 v[204:205], v215 offset:1024\n"
"v_mfma_f32_16x16x32_bf16 v[208:211], v[216:219], v[176:179], v[208:211]\n"
"v_mfma_f32_16x16x32_bf16 v[144:147], v[176:179], v[100:103], v[144:147]\n"
"ds_read_b64_tr_b16 v[206:207], v215 offset:1536\n"
"v_mfma_f32_16x16x32_bf16 v[116:119], v[180:183], v[96:99], v[116:119]\n"
"ds_read_b64_tr_b16 v[176:177], v212 offset:8192\n"
"v_mfma_f32_16x16x32_bf16 v[148:151], v[180:183], v[100:103], v[148:151]\n"
"ds_read_b64_tr_b16 v[178:179], v212 offset:8704\n"
"v_mul_f32 v66, 0xbfb8aa3b, v68\n"
"v_mul_f32 v68, v68, v68\n"
"v_mul_f32 v67, 0xbfb8aa3b, v69\n"
"v_mul_f32 v69, v69, v69\n"
"s_waitcnt lgkmcnt(10)\n"
"v_mfma_f32_16x16x32_bf16 v[120:123], v[184:187], v[96:99], v[120:123]\n"
"ds_read_b64_tr_b16 v[180:181], v213 offset:8192\n"
"v_mfma_f32_16x16x32_bf16 v[236:239], v[216:219], v[184:187], v[236:239]\n"
"v_mfma_f32_16x16x32_bf16 v[152:155], v[184:187], v[100:103], v[152:155]\n"
"ds_read_b64_tr_b16 v[182:183], v213 offset:8704\n"
"v_mfma_f32_16x16x32_bf16 v[124:127], v[188:191], v[96:99], v[124:127]\n"
"ds_read_b64_tr_b16 v[184:185], v212 offset:9216\n"
"v_mfma_f32_16x16x32_bf16 v[156:159], v[188:191], v[100:103], v[156:159]\n"
"ds_read_b64_tr_b16 v[186:187], v212 offset:9728\n"
"v_rcp_f32 v68, v68\n"
"v_rcp_f32 v69, v69\n"
"v_lshlrev_b32 v70, 4, v233\n"
"v_lshl_or_b32 v70, v234, 9, v70\n"
"s_waitcnt lgkmcnt(10)\n"
"v_mfma_f32_16x16x32_bf16 v[128:131], v[192:195], v[96:99], v[128:131]\n"
"ds_read_b64_tr_b16 v[188:189], v213 offset:9216\n"
"v_mfma_f32_16x16x32_bf16 v[160:163], v[192:195], v[100:103], v[160:163]\n"
"ds_read_b64_tr_b16 v[190:191], v213 offset:9728\n"
"v_mfma_f32_16x16x32_bf16 v[132:135], v[196:199], v[96:99], v[132:135]\n"
"ds_read_b64_tr_b16 v[192:193], v214 offset:8192\n"
"v_mfma_f32_16x16x32_bf16 v[164:167], v[196:199], v[100:103], v[164:167]\n"
"ds_read_b64_tr_b16 v[194:195], v214 offset:8704\n"
"s_waitcnt lgkmcnt(12)\n"
"v_mfma_f32_16x16x32_bf16 v[136:139], v[200:203], v[96:99], v[136:139]\n"
"ds_read_b64_tr_b16 v[196:197], v215 offset:8192\n"
"v_mfma_f32_16x16x32_bf16 v[168:171], v[200:203], v[100:103], v[168:171]\n"
"ds_read_b64_tr_b16 v[198:199], v215 offset:8704\n"
"s_waitcnt lgkmcnt(12)\n"
"v_mfma_f32_16x16x32_bf16 v[140:143], v[204:207], v[96:99], v[140:143]\n"
"ds_read_b64_tr_b16 v[200:201], v214 offset:9216\n"
"v_mfma_f32_16x16x32_bf16 v[172:175], v[204:207], v[100:103], v[172:175]\n"
"ds_read_b64_tr_b16 v[202:203], v214 offset:9728\n"
"s_waitcnt lgkmcnt(10)\n"
"v_mfma_f32_16x16x32_bf16 v[112:115], v[176:179], v[104:107], v[112:115]\n"
"ds_read_b64_tr_b16 v[204:205], v215 offset:9216\n"
"v_mfma_f32_16x16x32_bf16 v[208:211], v[216:219], v[176:179], v[208:211]\n"
"v_mfma_f32_16x16x32_bf16 v[144:147], v[176:179], v[108:111], v[144:147]\n"
"ds_read_b64_tr_b16 v[206:207], v215 offset:9728\n"
"v_mfma_f32_16x16x32_bf16 v[116:119], v[180:183], v[104:107], v[116:119]\n"
"v_mfma_f32_16x16x32_bf16 v[148:151], v[180:183], v[108:111], v[148:151]\n"
"s_waitcnt lgkmcnt(8)\n"
"v_mfma_f32_16x16x32_bf16 v[120:123], v[184:187], v[104:107], v[120:123]\n"
"v_mfma_f32_16x16x32_bf16 v[236:239], v[216:219], v[184:187], v[236:239]\n"
"v_mfma_f32_16x16x32_bf16 v[152:155], v[184:187], v[108:111], v[152:155]\n"
"v_mfma_f32_16x16x32_bf16 v[124:127], v[188:191], v[104:107], v[124:127]\n"
"v_mfma_f32_16x16x32_bf16 v[156:159], v[188:191], v[108:111], v[156:159]\n"
"s_waitcnt lgkmcnt(4)\n"
"v_mfma_f32_16x16x32_bf16 v[128:131], v[192:195], v[104:107], v[128:131]\n"
"v_mfma_f32_16x16x32_bf16 v[160:163], v[192:195], v[108:111], v[160:163]\n"
"s_mov_b64 exec, 0xffff\n"
"s_nop 0\n"
"ds_write2st64_b32 v220, v208, v236 offset1:1\n"
"s_mov_b64 exec, -1\n"
"s_mov_b64 exec, 1\n"
"ds_add_u32 v226, v227 offset:44\n"
"s_mov_b64 exec, -1\n"
"v_mfma_f32_16x16x32_bf16 v[132:135], v[196:199], v[104:107], v[132:135]\n"
"v_mfma_f32_16x16x32_bf16 v[164:167], v[196:199], v[108:111], v[164:167]\n"
"s_waitcnt lgkmcnt(4)\n"
"v_mfma_f32_16x16x32_bf16 v[136:139], v[200:203], v[104:107], v[136:139]\n"
"v_mfma_f32_16x16x32_bf16 v[168:171], v[200:203], v[108:111], v[168:171]\n"
"s_waitcnt lgkmcnt(2)\n"
"v_mfma_f32_16x16x32_bf16 v[140:143], v[204:207], v[104:107], v[140:143]\n"
"v_mfma_f32_16x16x32_bf16 v[172:175], v[204:207], v[108:111], v[172:175]\n"
"s_mov_b64 exec, 1\n"
"ds_add_u32 v226, v227 offset:20\n"
"s_mov_b64 exec, -1\n"
"ds_read_b32 v222, v226 offset:44\n"
"s_waitcnt lgkmcnt(0)\n"
"v_cmp_gt_u32 vcc, 4, v222\n"
"s_nop 0\n"
"s_cbranch_vccnz Lcs23_%=\n"
"Lcb24_%=:\n"
"ds_read_b128 v[32:35], v4 offset:0\n"
"ds_read_b128 v[36:39], v5 offset:0\n"
"ds_read_b128 v[40:43], v4 offset:256\n"
"ds_read_b128 v[44:47], v5 offset:256\n"
"ds_read_b128 v[48:51], v6 offset:0\n"
"ds_read_b128 v[52:55], v7 offset:0\n"
"ds_read_b128 v[56:59], v6 offset:256\n"
"ds_read_b128 v[60:63], v7 offset:256\n"
"v_mov_b32 v72, -1\n"
"v_mov_b32 v73, -1\n"
"v_mov_b32 v74, -1\n"
"v_mov_b32 v75, -1\n"
"s_waitcnt lgkmcnt(0)\n"
"v_fmac_f32 v112, v64, v32\n"
"v_fmac_f32 v113, v64, v33\n"
"v_fmac_f32 v114, v64, v34\n"
"v_fmac_f32 v115, v64, v35\n"
"v_fmac_f32 v116, v64, v36\n"
"v_fmac_f32 v117, v64, v37\n"
"v_fmac_f32 v118, v64, v38\n"
"v_fmac_f32 v119, v64, v39\n"
"v_fmac_f32 v120, v64, v40\n"
"v_fmac_f32 v121, v64, v41\n"
"v_fmac_f32 v122, v64, v42\n"
"v_fmac_f32 v123, v64, v43\n"
"v_fmac_f32 v124, v64, v44\n"
"v_fmac_f32 v125, v64, v45\n"
"v_fmac_f32 v126, v64, v46\n"
"v_fmac_f32 v127, v64, v47\n"
"v_mul_f32 v8, v66, v112\n"
"v_mul_f32 v9, v66, v113\n"
"v_mul_f32 v10, v66, v114\n"
"v_mul_f32 v11, v66, v115\n"
"v_mul_f32 v12, v66, v116\n"
"v_mul_f32 v13, v66, v117\n"
"v_mul_f32 v14, v66, v118\n"
"v_mul_f32 v15, v66, v119\n"
"v_exp_f32 v8, v8\n"
"v_exp_f32 v9, v9\n"
"v_exp_f32 v10, v10\n"
"v_exp_f32 v11, v11\n"
"v_exp_f32 v12, v12\n"
"v_exp_f32 v13, v13\n"
"v_exp_f32 v14, v14\n"
"v_exp_f32 v15, v15\n"
"v_mul_f32 v112, v112, v120\n"
"v_mul_f32 v113, v113, v121\n"
"v_mul_f32 v114, v114, v122\n"
"v_mul_f32 v115, v115, v123\n"
"v_mul_f32 v116, v116, v124\n"
"v_mul_f32 v117, v117, v125\n"
"v_mul_f32 v118, v118, v126\n"
"v_mul_f32 v119, v119, v127\n"
"v_fma_f32 v8, v8, v68, v68\n"
"v_fma_f32 v9, v9, v68, v68\n"
"v_fma_f32 v10, v10, v68, v68\n"
"v_fma_f32 v11, v11, v68, v68\n"
"v_fma_f32 v12, v12, v68, v68\n"
"v_fma_f32 v13, v13, v68, v68\n"
"v_fma_f32 v14, v14, v68, v68\n"
"v_fma_f32 v15, v15, v68, v68\n"
"v_rcp_f32 v8, v8\n"
"v_rcp_f32 v9, v9\n"
"v_rcp_f32 v10, v10\n"
"v_rcp_f32 v11, v11\n"
"v_rcp_f32 v12, v12\n"
"v_rcp_f32 v13, v13\n"
"v_rcp_f32 v14, v14\n"
"v_rcp_f32 v15, v15\n"
"v_mul_f32 v112, v112, v8\n"
"v_mul_f32 v113, v113, v9\n"
"v_mul_f32 v114, v114, v10\n"
"v_mul_f32 v115, v115, v11\n"
"v_mul_f32 v116, v116, v12\n"
"v_mul_f32 v117, v117, v13\n"
"v_mul_f32 v118, v118, v14\n"
"v_mul_f32 v119, v119, v15\n"
"v_cvt_pk_bf16_f32 v16, v112, v113\n"
"v_cvt_pk_bf16_f32 v17, v114, v115\n"
"v_cvt_pk_bf16_f32 v18, v116, v117\n"
"v_cvt_pk_bf16_f32 v19, v118, v119\n"
"s_mov_b64 exec, s[18:19]\n"
"v_swap_b32 v16, v18\n"
"v_swap_b32 v17, v19\n"
"s_mov_b64 exec, -1\n"
"ds_read_b32 v252, v243 offset:20\n"
"s_waitcnt lgkmcnt(0)\n"
"v_cmp_gt_u32 vcc, 4, v252\n"
"s_nop 0\n"
"s_cbranch_vccnz Lcs26_%=\n"
"Lcb27_%=:\n"
"s_add_u32 m0, s81, 0\n"
"s_nop 0\n"
"buffer_load_dwordx4 v241, s[36:39], s4 offen lds\n"
"s_waitcnt vmcnt(5)\n"
"v_fmac_f32 v128, v64, v48\n"
"v_fmac_f32 v129, v64, v49\n"
"v_fmac_f32 v130, v64, v50\n"
"v_fmac_f32 v131, v64, v51\n"
"v_fmac_f32 v132, v64, v52\n"
"v_fmac_f32 v133, v64, v53\n"
"v_fmac_f32 v134, v64, v54\n"
"v_fmac_f32 v135, v64, v55\n"
"s_add_u32 m0, s81, 1024\n"
"s_add_u32 s91, s4, 7168\n"
"buffer_load_dwordx4 v241, s[36:39], s91 offen lds\n"
"s_waitcnt vmcnt(5)\n"
"v_fmac_f32 v136, v64, v56\n"
"v_fmac_f32 v137, v64, v57\n"
"v_fmac_f32 v138, v64, v58\n"
"v_fmac_f32 v139, v64, v59\n"
"v_fmac_f32 v140, v64, v60\n"
"v_fmac_f32 v141, v64, v61\n"
"v_fmac_f32 v142, v64, v62\n"
"v_fmac_f32 v143, v64, v63\n"
"s_add_u32 s4, s4, 14336\n"
"s_cmp_eq_u32 s4, 344064\n"
"s_cselect_b32 s4, 0, s4\n"
"v_mul_f32 v8, v66, v128\n"
"v_mul_f32 v9, v66, v129\n"
"v_mul_f32 v10, v66, v130\n"
"v_mul_f32 v11, v66, v131\n"
"v_mul_f32 v12, v66, v132\n"
"v_mul_f32 v13, v66, v133\n"
"v_mul_f32 v14, v66, v134\n"
"v_mul_f32 v15, v66, v135\n"
"s_add_u32 m0, s81, 2048\n"
"s_nop 0\n"
"buffer_load_dwordx4 v241, s[36:39], s4 offen lds\n"
"s_waitcnt vmcnt(5)\n"
"v_exp_f32 v8, v8\n"
"v_exp_f32 v9, v9\n"
"v_exp_f32 v10, v10\n"
"v_exp_f32 v11, v11\n"
"v_exp_f32 v12, v12\n"
"v_exp_f32 v13, v13\n"
"v_exp_f32 v14, v14\n"
"v_exp_f32 v15, v15\n"
"s_add_u32 m0, s81, 3072\n"
"s_add_u32 s91, s4, 7168\n"
"buffer_load_dwordx4 v241, s[36:39], s91 offen lds\n"
"s_waitcnt vmcnt(5)\n"
"v_mul_f32 v128, v128, v136\n"
"v_mul_f32 v129, v129, v137\n"
"v_mul_f32 v130, v130, v138\n"
"v_mul_f32 v131, v131, v139\n"
"v_mul_f32 v132, v132, v140\n"
"v_mul_f32 v133, v133, v141\n"
"v_mul_f32 v134, v134, v142\n"
"v_mul_f32 v135, v135, v143\n"
"s_add_u32 s4, s4, 14336\n"
"s_cmp_eq_u32 s4, 344064\n"
"s_cselect_b32 s4, 0, s4\n"
"v_fma_f32 v8, v8, v68, v68\n"
"v_fma_f32 v9, v9, v68, v68\n"
"v_fma_f32 v10, v10, v68, v68\n"
"v_fma_f32 v11, v11, v68, v68\n"
"v_fma_f32 v12, v12, v68, v68\n"
"v_fma_f32 v13, v13, v68, v68\n"
"v_fma_f32 v14, v14, v68, v68\n"
"v_fma_f32 v15, v15, v68, v68\n"
"s_add_u32 m0, s81, 4096\n"
"s_nop 0\n"
"buffer_load_dwordx4 v241, s[36:39], s4 offen lds\n"
"s_waitcnt vmcnt(5)\n"
"v_rcp_f32 v8, v8\n"
"v_rcp_f32 v9, v9\n"
"v_rcp_f32 v10, v10\n"
"v_rcp_f32 v11, v11\n"
"v_rcp_f32 v12, v12\n"
"v_rcp_f32 v13, v13\n"
"v_rcp_f32 v14, v14\n"
"v_rcp_f32 v15, v15\n"
"s_add_u32 m0, s81, 5120\n"
"s_add_u32 s91, s4, 7168\n"
"buffer_load_dwordx4 v241, s[36:39], s91 offen lds\n"
"s_waitcnt vmcnt(5)\n"
"v_mul_f32 v128, v128, v8\n"
"v_mul_f32 v129, v129, v9\n"
"v_mul_f32 v130, v130, v10\n"
"v_mul_f32 v131, v131, v11\n"
"v_mul_f32 v132, v132, v12\n"
"v_mul_f32 v133, v133, v13\n"
"v_mul_f32 v134, v134, v14\n"
"v_mul_f32 v135, v135, v15\n"
"s_add_u32 s4, s4, 14336\n"
"s_cmp_eq_u32 s4, 344064\n"
"s_cselect_b32 s4, 0, s4\n"
"v_cvt_pk_bf16_f32 v20, v128, v129\n"
"v_cvt_pk_bf16_f32 v21, v130, v131\n"
"v_cvt_pk_bf16_f32 v22, v132, v133\n"
"v_cvt_pk_bf16_f32 v23, v134, v135\n"
"s_mov_b64 exec, s[18:19]\n"
"v_swap_b32 v20, v22\n"
"v_swap_b32 v21, v23\n"
"s_mov_b64 exec, -1\n"
"s_add_u32 m0, s81, 6144\n"
"s_nop 0\n"
"buffer_load_dwordx4 v241, s[36:39], s4 offen lds\n"
"s_waitcnt vmcnt(5)\n"
"v_fmac_f32 v144, v65, v32\n"
"v_fmac_f32 v145, v65, v33\n"
"v_fmac_f32 v146, v65, v34\n"
"v_fmac_f32 v147, v65, v35\n"
"v_fmac_f32 v148, v65, v36\n"
"v_fmac_f32 v149, v65, v37\n"
"v_fmac_f32 v150, v65, v38\n"
"v_fmac_f32 v151, v65, v39\n"
"s_add_u32 m0, s81, 7168\n"
"s_add_u32 s91, s4, 7168\n"
"buffer_load_dwordx4 v241, s[36:39], s91 offen lds\n"
"s_waitcnt vmcnt(5)\n"
"v_fmac_f32 v152, v65, v40\n"
"v_fmac_f32 v153, v65, v41\n"
"v_fmac_f32 v154, v65, v42\n"
"v_fmac_f32 v155, v65, v43\n"
"v_fmac_f32 v156, v65, v44\n"
"v_fmac_f32 v157, v65, v45\n"
"v_fmac_f32 v158, v65, v46\n"
"v_fmac_f32 v159, v65, v47\n"
"s_add_u32 s4, s4, 14336\n"
"s_cmp_eq_u32 s4, 344064\n"
"s_cselect_b32 s4, 0, s4\n"
"v_mul_f32 v8, v67, v144\n"
"v_mul_f32 v9, v67, v145\n"
"v_mul_f32 v10, v67, v146\n"
"v_mul_f32 v11, v67, v147\n"
"v_mul_f32 v12, v67, v148\n"
"v_mul_f32 v13, v67, v149\n"
"v_mul_f32 v14, v67, v150\n"
"v_mul_f32 v15, v67, v151\n"
"s_add_u32 m0, s81, 8192\n"
"s_nop 0\n"
"buffer_load_dwordx4 v241, s[36:39], s4 offen lds\n"
"s_waitcnt vmcnt(5)\n"
"v_exp_f32 v8, v8\n"
"v_exp_f32 v9, v9\n"
"v_exp_f32 v10, v10\n"
"v_exp_f32 v11, v11\n"
"v_exp_f32 v12, v12\n"
"v_exp_f32 v13, v13\n"
"v_exp_f32 v14, v14\n"
"v_exp_f32 v15, v15\n"
"s_add_u32 m0, s81, 9216\n"
"s_add_u32 s91, s4, 7168\n"
"buffer_load_dwordx4 v241, s[36:39], s91 offen lds\n"
"s_waitcnt vmcnt(5)\n"
"v_mul_f32 v144, v144, v152\n"
"v_mul_f32 v145, v145, v153\n"
"v_mul_f32 v146, v146, v154\n"
"v_mul_f32 v147, v147, v155\n"
"v_mul_f32 v148, v148, v156\n"
"v_mul_f32 v149, v149, v157\n"
"v_mul_f32 v150, v150, v158\n"
"v_mul_f32 v151, v151, v159\n"
"s_add_u32 s4, s4, 14336\n"
"s_cmp_eq_u32 s4, 344064\n"
"s_cselect_b32 s4, 0, s4\n"
"v_fma_f32 v8, v8, v69, v69\n"
"v_fma_f32 v9, v9, v69, v69\n"
"v_fma_f32 v10, v10, v69, v69\n"
"v_fma_f32 v11, v11, v69, v69\n"
"v_fma_f32 v12, v12, v69, v69\n"
"v_fma_f32 v13, v13, v69, v69\n"
"v_fma_f32 v14, v14, v69, v69\n"
"v_fma_f32 v15, v15, v69, v69\n"
"s_add_u32 m0, s81, 10240\n"
"s_nop 0\n"
"buffer_load_dwordx4 v241, s[36:39], s4 offen lds\n"
"s_waitcnt vmcnt(5)\n"
"v_rcp_f32 v8, v8\n"
"v_rcp_f32 v9, v9\n"
"v_rcp_f32 v10, v10\n"
"v_rcp_f32 v11, v11\n"
"v_rcp_f32 v12, v12\n"
"v_rcp_f32 v13, v13\n"
"v_rcp_f32 v14, v14\n"
"v_rcp_f32 v15, v15\n"
"s_add_u32 m0, s81, 11264\n"
"s_add_u32 s91, s4, 7168\n"
"buffer_load_dwordx4 v241, s[36:39], s91 offen lds\n"
"s_waitcnt vmcnt(5)\n"
"v_mul_f32 v144, v144, v8\n"
"v_mul_f32 v145, v145, v9\n"
"v_mul_f32 v146, v146, v10\n"
"v_mul_f32 v147, v147, v11\n"
"v_mul_f32 v148, v148, v12\n"
"v_mul_f32 v149, v149, v13\n"
"v_mul_f32 v150, v150, v14\n"
"v_mul_f32 v151, v151, v15\n"
"s_add_u32 s4, s4, 14336\n"
"s_cmp_eq_u32 s4, 344064\n"
"s_cselect_b32 s4, 0, s4\n"
"v_cvt_pk_bf16_f32 v24, v144, v145\n"
"v_cvt_pk_bf16_f32 v25, v146, v147\n"
"v_cvt_pk_bf16_f32 v26, v148, v149\n"
"v_cvt_pk_bf16_f32 v27, v150, v151\n"
"s_mov_b64 exec, s[18:19]\n"
"v_swap_b32 v24, v26\n"
"v_swap_b32 v25, v27\n"
"s_mov_b64 exec, -1\n"
"v_fmac_f32 v160, v65, v48\n"
"v_fmac_f32 v161, v65, v49\n"
"v_fmac_f32 v162, v65, v50\n"
"v_fmac_f32 v163, v65, v51\n"
"v_fmac_f32 v164, v65, v52\n"
"v_fmac_f32 v165, v65, v53\n"
"v_fmac_f32 v166, v65, v54\n"
"v_fmac_f32 v167, v65, v55\n"
"v_fmac_f32 v168, v65, v56\n"
"v_fmac_f32 v169, v65, v57\n"
"v_fmac_f32 v170, v65, v58\n"
"v_fmac_f32 v171, v65, v59\n"
"v_fmac_f32 v172, v65, v60\n"
"v_fmac_f32 v173, v65, v61\n"
"v_fmac_f32 v174, v65, v62\n"
"v_fmac_f32 v175, v65, v63\n"
"v_mul_f32 v8, v67, v160\n"
"v_mul_f32 v9, v67, v161\n"
"v_mul_f32 v10, v67, v162\n"
"v_mul_f32 v11, v67, v163\n"
"v_mul_f32 v12, v67, v164\n"
"v_mul_f32 v13, v67, v165\n"
"v_mul_f32 v14, v67, v166\n"
"v_mul_f32 v15, v67, v167\n"
"v_exp_f32 v8, v8\n"
"v_exp_f32 v9, v9\n"
"v_exp_f32 v10, v10\n"
"v_exp_f32 v11, v11\n"
"v_exp_f32 v12, v12\n"
"v_exp_f32 v13, v13\n"
"v_exp_f32 v14, v14\n"
"v_exp_f32 v15, v15\n"
"v_mul_f32 v160, v160, v168\n"
"v_mul_f32 v161, v161, v169\n"
"v_mul_f32 v162, v162, v170\n"
"v_mul_f32 v163, v163, v171\n"
"v_mul_f32 v164, v164, v172\n"
"v_mul_f32 v165, v165, v173\n"
"v_mul_f32 v166, v166, v174\n"
"v_mul_f32 v167, v167, v175\n"
"v_fma_f32 v8, v8, v69, v69\n"
"v_fma_f32 v9, v9, v69, v69\n"
"v_fma_f32 v10, v10, v69, v69\n"
"v_fma_f32 v11, v11, v69, v69\n"
"v_fma_f32 v12, v12, v69, v69\n"
"v_fma_f32 v13, v13, v69, v69\n"
"v_fma_f32 v14, v14, v69, v69\n"
"v_fma_f32 v15, v15, v69, v69\n"
"v_rcp_f32 v8, v8\n"
"v_rcp_f32 v9, v9\n"
"v_rcp_f32 v10, v10\n"
"v_rcp_f32 v11, v11\n"
"v_rcp_f32 v12, v12\n"
"v_rcp_f32 v13, v13\n"
"v_rcp_f32 v14, v14\n"
"v_rcp_f32 v15, v15\n"
"v_mul_f32 v160, v160, v8\n"
"v_mul_f32 v161, v161, v9\n"
"v_mul_f32 v162, v162, v10\n"
"v_mul_f32 v163, v163, v11\n"
"v_mul_f32 v164, v164, v12\n"
"v_mul_f32 v165, v165, v13\n"
"v_mul_f32 v166, v166, v14\n"
"v_mul_f32 v167, v167, v15\n"
"v_cvt_pk_bf16_f32 v28, v160, v161\n"
"v_cvt_pk_bf16_f32 v29, v162, v163\n"
"v_cvt_pk_bf16_f32 v30, v164, v165\n"
"v_cvt_pk_bf16_f32 v31, v166, v167\n"
"s_mov_b64 exec, s[18:19]\n"
"v_swap_b32 v28, v30\n"
"v_swap_b32 v29, v31\n"
"s_mov_b64 exec, -1\n"
"ds_read_b32 v252, v243 offset:56\n"
"s_waitcnt lgkmcnt(0)\n"
"v_cmp_gt_u32 vcc, 4, v252\n"
"s_nop 0\n"
"s_cbranch_vccnz Lcs29_%=\n"
"Lcb30_%=:\n"
"ds_read_b128 v[96:99], v3 offset:0\n"
"ds_read_b128 v[100:103], v3 offset:1024\n"
"ds_read_b128 v[104:107], v3 offset:2048\n"
"ds_read_b128 v[108:111], v3 offset:3072\n"
"s_waitcnt lgkmcnt(0)\n"
"s_mov_b64 exec, s[16:17]\n"
"buffer_store_dwordx4 v[96:99], v245, s[48:51], s10 offen offset:0 sc1\n"
"s_mov_b64 exec, -1\n"
"s_waitcnt vmcnt(3)\n"
"s_mov_b64 exec, s[16:17]\n"
"buffer_store_dwordx4 v[100:103], v245, s[48:51], s10 offen offset:1024 sc1\n"
"s_mov_b64 exec, -1\n"
"s_waitcnt vmcnt(3)\n"
"s_mov_b64 exec, s[16:17]\n"
"buffer_store_dwordx4 v[104:107], v245, s[48:51], s11 offen offset:0 sc1\n"
"s_mov_b64 exec, -1\n"
"s_waitcnt vmcnt(3)\n"
"s_mov_b64 exec, s[16:17]\n"
"buffer_store_dwordx4 v[108:111], v245, s[48:51], s11 offen offset:1024 sc1\n"
"s_mov_b64 exec, -1\n"
"s_waitcnt vmcnt(3)\n"
"buffer_store_dwordx4 v[72:75], v245, s[48:51], s9 offen offset:0 sc1\n"
"s_waitcnt vmcnt(3)\n"
"buffer_store_dwordx4 v[72:75], v245, s[48:51], s9 offen offset:1024 sc1\n"
"s_waitcnt vmcnt(3)\n"
"s_xor_b32 s91, s8, 0\n"
"buffer_store_dwordx4 v[16:19], v70, s[48:51], s91 offen offset:0 sc1\n"
"s_waitcnt vmcnt(3)\n"
"s_xor_b32 s91, s8, 2048\n"
"buffer_store_dwordx4 v[20:23], v70, s[48:51], s91 offen offset:0 sc1\n"
"s_waitcnt vmcnt(3)\n"
"s_xor_b32 s91, s8, 0\n"
"buffer_store_dwordx4 v[24:27], v70, s[48:51], s91 offen offset:256 sc1\n"
"s_waitcnt vmcnt(3)\n"
"s_xor_b32 s91, s8, 2048\n"
"buffer_store_dwordx4 v[28:31], v70, s[48:51], s91 offen offset:256 sc1\n"
"s_waitcnt vmcnt(3)\n"
"s_lshl_b32 s13, s12, 12\n"
"s_add_u32 s13, s13, s6\n"
"s_mul_i32 s14, s12, 28672\n"
"s_add_u32 m0, s13, 49152\n"
"s_nop 0\n"
"buffer_load_dwordx4 v253, s[40:43], s14 offen lds\n"
"s_waitcnt vmcnt(5)\n"
"s_add_u32 s12, s12, 1\n"
"s_cmp_eq_u32 s12, 14\n"
"s_cselect_b32 s12, 0, s12\n"
"s_lshl_b32 s13, s12, 12\n"
"s_add_u32 s13, s13, s6\n"
"s_mul_i32 s14, s12, 28672\n"
"s_add_u32 m0, s13, 49152\n"
"s_nop 0\n"
"buffer_load_dwordx4 v253, s[40:43], s14 offen lds\n"
"s_waitcnt vmcnt(5)\n"
"s_add_u32 s12, s12, 1\n"
"s_cmp_eq_u32 s12, 14\n"
"s_cselect_b32 s12, 0, s12\n"
"s_lshl_b32 s13, s12, 12\n"
"s_add_u32 s13, s13, s6\n"
"s_mul_i32 s14, s12, 28672\n"
"s_add_u32 m0, s13, 49152\n"
"s_nop 0\n"
"buffer_load_dwordx4 v253, s[40:43], s14 offen lds\n"
"s_waitcnt vmcnt(5)\n"
"s_add_u32 s12, s12, 1\n"
"s_cmp_eq_u32 s12, 14\n"
"s_cselect_b32 s12, 0, s12\n"
"s_lshl_b32 s13, s12, 12\n"
"s_add_u32 s13, s13, s6\n"
"s_mul_i32 s14, s12, 28672\n"
"s_add_u32 m0, s13, 49152\n"
"s_nop 0\n"
"buffer_load_dwordx4 v253, s[40:43], s14 offen lds\n"
"s_waitcnt vmcnt(5)\n"
"s_add_u32 s12, s12, 1\n"
"s_cmp_eq_u32 s12, 14\n"
"s_cselect_b32 s12, 0, s12\n"
"s_lshl_b32 s13, s12, 12\n"
"s_add_u32 s13, s13, s6\n"
"s_mul_i32 s14, s12, 28672\n"
"s_add_u32 m0, s13, 49152\n"
"s_nop 0\n"
"buffer_load_dwordx4 v253, s[40:43], s14 offen lds\n"
"s_waitcnt vmcnt(5)\n"
"s_add_u32 s12, s12, 1\n"
"s_cmp_eq_u32 s12, 14\n"
"s_cselect_b32 s12, 0, s12\n"
"s_lshl_b32 s13, s12, 12\n"
"s_add_u32 s13, s13, s6\n"
"s_mul_i32 s14, s12, 28672\n"
"s_add_u32 m0, s13, 49152\n"
"s_nop 0\n"
"buffer_load_dwordx4 v253, s[40:43], s14 offen lds\n"
"s_waitcnt vmcnt(5)\n"
"s_add_u32 s12, s12, 1\n"
"s_cmp_eq_u32 s12, 14\n"
"s_cselect_b32 s12, 0, s12\n"
"s_lshl_b32 s13, s12, 12\n"
"s_add_u32 s13, s13, s6\n"
"s_mul_i32 s14, s12, 28672\n"
"s_add_u32 m0, s13, 49152\n"
"s_nop 0\n"
"buffer_load_dwordx4 v253, s[40:43], s14 offen lds\n"
"s_waitcnt vmcnt(5)\n"
"s_add_u32 s12, s12, 1\n"
"s_cmp_eq_u32 s12, 14\n"
"s_cselect_b32 s12, 0, s12\n"
"s_waitcnt vmcnt(7)\n"
"s_mov_b64 exec, 1\n"
"ds_add_rtn_u32 v252, v243, v244 offset:16\n"
"s_waitcnt lgkmcnt(0)\n"
"v_cmp_ne_u32 vcc, 3, v252\n"
"s_nop 0\n"
"s_cbranch_vccnz Llf32_%=\n"
"v_mov_b32 v252, s71\n"
"buffer_store_dword v252, off, s[48:51], s89 sc1\n"
"Llf32_%=:\n"
"s_mov_b64 exec, -1\n"
"s_cmp_lt_u32 s66, s83\n"
"s_cbranch_scc1 LBD33_%=\n"
"v_mov_b32 v251, s15\n"
"s_mov_b32 s90, 400000\n"
"Lpl34_%=:\n"
"s_mov_b64 exec, 0x1\n"
"buffer_load_dword v252, v251, s[48:51], 0 offen sc1\n"
"s_waitcnt vmcnt(0)\n"
"v_cmp_ne_u32 vcc, s71, v252\n"
"s_mov_b64 exec, -1\n"
"s_sub_u32 s90, s90, 1\n"
"s_cbranch_scc1 Lpd35_%=\n"
"s_cbranch_vccz Lpd35_%=\n"
"s_sleep 16\n"
"s_branch Lpl34_%=\n"
"Lpd35_%=:\n"
"s_mov_b64 exec, 1\n"
"ds_add_u32 v243, v244 offset:40\n"
"s_mov_b64 exec, -1\n"
"s_mov_b64 exec, 1\n"
"ds_add_u32 v243, v244 offset:64\n"
"s_mov_b64 exec, -1\n"
"s_endpgm\n"
"LBD33_%=:\n"
"ds_read_b64_tr_b16 v[176:177], v248 offset:0\n"
"ds_read_b64_tr_b16 v[178:179], v248 offset:1024\n"
"ds_read_b64_tr_b16 v[180:181], v249 offset:0\n"
"ds_read_b64_tr_b16 v[182:183], v249 offset:1024\n"
"s_lshl_b32 s13, s12, 12\n"
"s_add_u32 s13, s13, s6\n"
"s_mul_i32 s14, s12, 28672\n"
"s_add_u32 m0, s13, 49152\n"
"s_nop 0\n"
"buffer_load_dwordx4 v253, s[40:43], s14 offen lds\n"
"s_waitcnt vmcnt(5)\n"
"s_add_u32 s12, s12, 1\n"
"s_cmp_eq_u32 s12, 14\n"
"s_cselect_b32 s12, 0, s12\n"
"s_waitcnt lgkmcnt(0)\n"
"s_add_u32 m0, s81, 0\n"
"s_nop 0\n"
"buffer_load_dwordx4 v241, s[36:39], s4 offen lds\n"
"s_waitcnt vmcnt(5)\n"
"s_add_u32 m0, s81, 1024\n"
"s_add_u32 s91, s4, 7168\n"
"buffer_load_dwordx4 v241, s[36:39], s91 offen lds\n"
"s_waitcnt vmcnt(5)\n"
"s_add_u32 s4, s4, 14336\n"
"s_cmp_eq_u32 s4, 344064\n"
"s_cselect_b32 s4, 0, s4\n"
"ds_read_b64_tr_b16 v[184:185], v248 offset:2048\n"
"ds_read_b64_tr_b16 v[186:187], v248 offset:3072\n"
"ds_read_b64_tr_b16 v[188:189], v249 offset:2048\n"
"ds_read_b64_tr_b16 v[190:191], v249 offset:3072\n"
"s_lshl_b32 s13, s12, 12\n"
"s_add_u32 s13, s13, s6\n"
"s_mul_i32 s14, s12, 28672\n"
"s_add_u32 m0, s13, 49152\n"
"s_nop 0\n"
"buffer_load_dwordx4 v253, s[40:43], s14 offen lds\n"
"s_waitcnt vmcnt(5)\n"
"s_add_u32 s12, s12, 1\n"
"s_cmp_eq_u32 s12, 14\n"
"s_cselect_b32 s12, 0, s12\n"
"s_waitcnt lgkmcnt(0)\n"
"s_add_u32 m0, s81, 2048\n"
"s_nop 0\n"
"buffer_load_dwordx4 v241, s[36:39], s4 offen lds\n"
"s_waitcnt vmcnt(5)\n"
"s_add_u32 m0, s81, 3072\n"
"s_add_u32 s91, s4, 7168\n"
"buffer_load_dwordx4 v241, s[36:39], s91 offen lds\n"
"s_waitcnt vmcnt(5)\n"
"s_add_u32 s4, s4, 14336\n"
"s_cmp_eq_u32 s4, 344064\n"
"s_cselect_b32 s4, 0, s4\n"
"ds_read_b64_tr_b16 v[192:193], v248 offset:4096\n"
"ds_read_b64_tr_b16 v[194:195], v248 offset:5120\n"
"ds_read_b64_tr_b16 v[196:197], v249 offset:4096\n"
"ds_read_b64_tr_b16 v[198:199], v249 offset:5120\n"
"s_lshl_b32 s13, s12, 12\n"
"s_add_u32 s13, s13, s6\n"
"s_mul_i32 s14, s12, 28672\n"
"s_add_u32 m0, s13, 49152\n"
"s_nop 0\n"
"buffer_load_dwordx4 v253, s[40:43], s14 offen lds\n"
"s_waitcnt vmcnt(5)\n"
"s_add_u32 s12, s12, 1\n"
"s_cmp_eq_u32 s12, 14\n"
"s_cselect_b32 s12, 0, s12\n"
"s_waitcnt lgkmcnt(0)\n"
"s_add_u32 m0, s81, 4096\n"
"s_nop 0\n"
"buffer_load_dwordx4 v241, s[36:39], s4 offen lds\n"
"s_waitcnt vmcnt(5)\n"
"s_add_u32 m0, s81, 5120\n"
"s_add_u32 s91, s4, 7168\n"
"buffer_load_dwordx4 v241, s[36:39], s91 offen lds\n"
"s_waitcnt vmcnt(5)\n"
"s_add_u32 s4, s4, 14336\n"
"s_cmp_eq_u32 s4, 344064\n"
"s_cselect_b32 s4, 0, s4\n"
"ds_read_b64_tr_b16 v[200:201], v248 offset:6144\n"
"ds_read_b64_tr_b16 v[202:203], v248 offset:7168\n"
"ds_read_b64_tr_b16 v[204:205], v249 offset:6144\n"
"ds_read_b64_tr_b16 v[206:207], v249 offset:7168\n"
"s_lshl_b32 s13, s12, 12\n"
"s_add_u32 s13, s13, s6\n"
"s_mul_i32 s14, s12, 28672\n"
"s_add_u32 m0, s13, 49152\n"
"s_nop 0\n"
"buffer_load_dwordx4 v253, s[40:43], s14 offen lds\n"
"s_waitcnt vmcnt(5)\n"
"s_add_u32 s12, s12, 1\n"
"s_cmp_eq_u32 s12, 14\n"
"s_cselect_b32 s12, 0, s12\n"
"s_waitcnt lgkmcnt(0)\n"
"s_add_u32 m0, s81, 6144\n"
"s_nop 0\n"
"buffer_load_dwordx4 v241, s[36:39], s4 offen lds\n"
"s_waitcnt vmcnt(5)\n"
"s_add_u32 m0, s81, 7168\n"
"s_add_u32 s91, s4, 7168\n"
"buffer_load_dwordx4 v241, s[36:39], s91 offen lds\n"
"s_waitcnt vmcnt(5)\n"
"s_add_u32 s4, s4, 14336\n"
"s_cmp_eq_u32 s4, 344064\n"
"s_cselect_b32 s4, 0, s4\n"
"ds_read_b64_tr_b16 v[208:209], v248 offset:8192\n"
"ds_read_b64_tr_b16 v[210:211], v248 offset:9216\n"
"ds_read_b64_tr_b16 v[212:213], v249 offset:8192\n"
"ds_read_b64_tr_b16 v[214:215], v249 offset:9216\n"
"s_lshl_b32 s13, s12, 12\n"
"s_add_u32 s13, s13, s6\n"
"s_mul_i32 s14, s12, 28672\n"
"s_add_u32 m0, s13, 49152\n"
"s_nop 0\n"
"buffer_load_dwordx4 v253, s[40:43], s14 offen lds\n"
"s_waitcnt vmcnt(5)\n"
"s_add_u32 s12, s12, 1\n"
"s_cmp_eq_u32 s12, 14\n"
"s_cselect_b32 s12, 0, s12\n"
"s_waitcnt lgkmcnt(0)\n"
"s_add_u32 m0, s81, 8192\n"
"s_nop 0\n"
"buffer_load_dwordx4 v241, s[36:39], s4 offen lds\n"
"s_waitcnt vmcnt(5)\n"
"s_add_u32 m0, s81, 9216\n"
"s_add_u32 s91, s4, 7168\n"
"buffer_load_dwordx4 v241, s[36:39], s91 offen lds\n"
"s_waitcnt vmcnt(5)\n"
"s_add_u32 s4, s4, 14336\n"
"s_cmp_eq_u32 s4, 344064\n"
"s_cselect_b32 s4, 0, s4\n"
"ds_read_b64_tr_b16 v[216:217], v248 offset:10240\n"
"ds_read_b64_tr_b16 v[218:219], v248 offset:11264\n"
"ds_read_b64_tr_b16 v[220:221], v249 offset:10240\n"
"ds_read_b64_tr_b16 v[222:223], v249 offset:11264\n"
"s_mov_b64 exec, 0x7\n"
"buffer_load_dword v252, v251, s[48:51], 0 offen sc1\n"
"s_mov_b64 exec, -1\n"
"s_lshl_b32 s13, s12, 12\n"
"s_add_u32 s13, s13, s6\n"
"s_mul_i32 s14, s12, 28672\n"
"s_add_u32 m0, s13, 49152\n"
"s_nop 0\n"
"buffer_load_dwordx4 v253, s[40:43], s14 offen lds\n"
"s_waitcnt vmcnt(5)\n"
"s_add_u32 s12, s12, 1\n"
"s_cmp_eq_u32 s12, 14\n"
"s_cselect_b32 s12, 0, s12\n"
"s_waitcnt lgkmcnt(0)\n"
"s_add_u32 m0, s81, 10240\n"
"s_nop 0\n"
"buffer_load_dwordx4 v241, s[36:39], s4 offen lds\n"
"s_waitcnt vmcnt(5)\n"
"s_add_u32 m0, s81, 11264\n"
"s_add_u32 s91, s4, 7168\n"
"buffer_load_dwordx4 v241, s[36:39], s91 offen lds\n"
"s_waitcnt vmcnt(5)\n"
"s_add_u32 s4, s4, 14336\n"
"s_cmp_eq_u32 s4, 344064\n"
"s_cselect_b32 s4, 0, s4\n"
"s_waitcnt vmcnt(16)\n"
"ds_read_b64_tr_b16 v[224:225], v248 offset:0\n"
"ds_read_b64_tr_b16 v[226:227], v248 offset:1024\n"
"ds_read_b64_tr_b16 v[228:229], v249 offset:0\n"
"ds_read_b64_tr_b16 v[230:231], v249 offset:1024\n"
"s_lshl_b32 s13, s12, 12\n"
"s_add_u32 s13, s13, s6\n"
"s_mul_i32 s14, s12, 28672\n"
"s_add_u32 m0, s13, 49152\n"
"s_nop 0\n"
"buffer_load_dwordx4 v253, s[40:43], s14 offen lds\n"
"s_waitcnt vmcnt(5)\n"
"s_add_u32 s12, s12, 1\n"
"s_cmp_eq_u32 s12, 14\n"
"s_cselect_b32 s12, 0, s12\n"
"s_waitcnt lgkmcnt(0)\n"
"s_add_u32 m0, s81, 0\n"
"s_nop 0\n"
"buffer_load_dwordx4 v241, s[36:39], s4 offen lds\n"
"s_waitcnt vmcnt(5)\n"
"s_add_u32 m0, s81, 1024\n"
"s_add_u32 s91, s4, 7168\n"
"buffer_load_dwordx4 v241, s[36:39], s91 offen lds\n"
"s_waitcnt vmcnt(5)\n"
"s_add_u32 s4, s4, 14336\n"
"s_cmp_eq_u32 s4, 344064\n"
"s_cselect_b32 s4, 0, s4\n"
"s_waitcnt vmcnt(16)\n"
"ds_read_b64_tr_b16 v[232:233], v248 offset:2048\n"
"ds_read_b64_tr_b16 v[234:235], v248 offset:3072\n"
"ds_read_b64_tr_b16 v[236:237], v249 offset:2048\n"
"ds_read_b64_tr_b16 v[238:239], v249 offset:3072\n"
"s_add_u32 m0, s52, 106496\n"
"s_nop 0\n"
"buffer_load_dwordx4 v254, s[40:43], 0 offen lds\n"
"s_waitcnt lgkmcnt(0)\n"
"s_add_u32 m0, s81, 2048\n"
"s_nop 0\n"
"buffer_load_dwordx4 v241, s[36:39], s4 offen lds\n"
"s_waitcnt vmcnt(5)\n"
"s_add_u32 m0, s81, 3072\n"
"s_add_u32 s91, s4, 7168\n"
"buffer_load_dwordx4 v241, s[36:39], s91 offen lds\n"
"s_waitcnt vmcnt(5)\n"
"s_add_u32 s4, s4, 14336\n"
"s_cmp_eq_u32 s4, 344064\n"
"s_cselect_b32 s4, 0, s4\n"
"s_waitcnt vmcnt(9)\n"
"s_mov_b64 exec, 0x7\n"
"v_cmp_ne_u32 vcc, s71, v252\n"
"s_mov_b64 exec, -1\n"
"s_cbranch_vccnz Lns36_%=\n"
"Lnb37_%=:\n"
"buffer_load_dwordx4 v[64:67], v246, s[48:51], s5 offen offset:0 sc1\n"
"s_waitcnt vmcnt(5)\n"
"buffer_load_dwordx4 v[68:71], v247, s[48:51], s5 offen offset:0 sc1\n"
"s_waitcnt vmcnt(5)\n"
"s_add_u32 s5, s5, 1024\n"
"s_cmp_eq_u32 s5, 24576\n"
"s_cselect_b32 s5, 0, s5\n"
"buffer_load_dwordx4 v[72:75], v246, s[48:51], s5 offen offset:0 sc1\n"
"s_waitcnt vmcnt(5)\n"
"buffer_load_dwordx4 v[76:79], v247, s[48:51], s5 offen offset:0 sc1\n"
"s_waitcnt vmcnt(5)\n"
"s_add_u32 s5, s5, 1024\n"
"s_cmp_eq_u32 s5, 24576\n"
"s_cselect_b32 s5, 0, s5\n"
"buffer_load_dwordx4 v[80:83], v246, s[48:51], s5 offen offset:0 sc1\n"
"s_waitcnt vmcnt(5)\n"
"buffer_load_dwordx4 v[84:87], v247, s[48:51], s5 offen offset:0 sc1\n"
"s_waitcnt vmcnt(5)\n"
"s_add_u32 s5, s5, 1024\n"
"s_cmp_eq_u32 s5, 24576\n"
"s_cselect_b32 s5, 0, s5\n"
"buffer_load_dwordx4 v[88:91], v246, s[48:51], s5 offen offset:0 sc1\n"
"s_waitcnt vmcnt(5)\n"
"buffer_load_dwordx4 v[92:95], v247, s[48:51], s5 offen offset:0 sc1\n"
"s_waitcnt vmcnt(5)\n"
"s_add_u32 s5, s5, 1024\n"
"s_cmp_eq_u32 s5, 24576\n"
"s_cselect_b32 s5, 0, s5\n"
"buffer_load_dwordx4 v[96:99], v246, s[48:51], s5 offen offset:0 sc1\n"
"s_waitcnt vmcnt(5)\n"
"buffer_load_dwordx4 v[100:103], v247, s[48:51], s5 offen offset:0 sc1\n"
"s_waitcnt vmcnt(5)\n"
"s_add_u32 s5, s5, 1024\n"
"s_cmp_eq_u32 s5, 24576\n"
"s_cselect_b32 s5, 0, s5\n"
"buffer_load_dwordx4 v[104:107], v246, s[48:51], s5 offen offset:0 sc1\n"
"s_waitcnt vmcnt(5)\n"
"buffer_load_dwordx4 v[108:111], v247, s[48:51], s5 offen offset:0 sc1\n"
"s_waitcnt vmcnt(5)\n"
"s_add_u32 s5, s5, 1024\n"
"s_cmp_eq_u32 s5, 24576\n"
"s_cselect_b32 s5, 0, s5\n"
"buffer_load_dwordx4 v[112:115], v246, s[48:51], s5 offen offset:0 sc1\n"
"s_waitcnt vmcnt(5)\n"
"buffer_load_dwordx4 v[116:119], v247, s[48:51], s5 offen offset:0 sc1\n"
"s_waitcnt vmcnt(5)\n"
"s_add_u32 s5, s5, 1024\n"
"s_cmp_eq_u32 s5, 24576\n"
"s_cselect_b32 s5, 0, s5\n"
"buffer_load_dwordx4 v[120:123], v246, s[48:51], s5 offen offset:0 sc1\n"
"s_waitcnt vmcnt(5)\n"
"buffer_load_dwordx4 v[124:127], v247, s[48:51], s5 offen offset:0 sc1\n"
"s_waitcnt vmcnt(5)\n"
"s_add_u32 s5, s5, 1024\n"
"s_cmp_eq_u32 s5, 24576\n"
"s_cselect_b32 s5, 0, s5\n"
"buffer_load_dwordx4 v[128:131], v246, s[48:51], s5 offen offset:0 sc1\n"
"s_waitcnt vmcnt(5)\n"
"buffer_load_dwordx4 v[132:135], v247, s[48:51], s5 offen offset:0 sc1\n"
"s_waitcnt vmcnt(5)\n"
"s_add_u32 s5, s5, 1024\n"
"s_cmp_eq_u32 s5, 24576\n"
"s_cselect_b32 s5, 0, s5\n"
"buffer_load_dwordx4 v[136:139], v246, s[48:51], s5 offen offset:0 sc1\n"
"s_waitcnt vmcnt(5)\n"
"buffer_load_dwordx4 v[140:143], v247, s[48:51], s5 offen offset:0 sc1\n"
"s_waitcnt vmcnt(5)\n"
"s_add_u32 s5, s5, 1024\n"
"s_cmp_eq_u32 s5, 24576\n"
"s_cselect_b32 s5, 0, s5\n"
"buffer_load_dwordx4 v[144:147], v246, s[48:51], s5 offen offset:0 sc1\n"
"s_waitcnt vmcnt(5)\n"
"buffer_load_dwordx4 v[148:151], v247, s[48:51], s5 offen offset:0 sc1\n"
"s_waitcnt vmcnt(5)\n"
"s_add_u32 s5, s5, 1024\n"
"s_cmp_eq_u32 s5, 24576\n"
"s_cselect_b32 s5, 0, s5\n"
"s_mov_b64 exec, 0x1f\n"
"buffer_load_dword v252, v251, s[48:51], 0 offen sc1\n"
"s_mov_b64 exec, -1\n"
"buffer_load_dwordx4 v[152:155], v246, s[48:51], s5 offen offset:0 sc1\n"
"s_waitcnt vmcnt(5)\n"
"buffer_load_dwordx4 v[156:159], v247, s[48:51], s5 offen offset:0 sc1\n"
"s_waitcnt vmcnt(5)\n"
"s_add_u32 s5, s5, 1024\n"
"s_cmp_eq_u32 s5, 24576\n"
"s_cselect_b32 s5, 0, s5\n"
"buffer_load_dwordx4 v[160:163], v246, s[48:51], s5 offen offset:0 sc1\n"
"s_waitcnt vmcnt(5)\n"
"buffer_load_dwordx4 v[164:167], v247, s[48:51], s5 offen offset:0 sc1\n"
"s_waitcnt vmcnt(5)\n"
"s_add_u32 s5, s5, 1024\n"
"s_cmp_eq_u32 s5, 24576\n"
"s_cselect_b32 s5, 0, s5\n"
"buffer_load_dwordx4 v[168:171], v246, s[48:51], s5 offen offset:0 sc1\n"
"s_waitcnt vmcnt(5)\n"
"buffer_load_dwordx4 v[172:175], v247, s[48:51], s5 offen offset:0 sc1\n"
"s_waitcnt vmcnt(5)\n"
"s_add_u32 s5, s5, 1024\n"
"s_cmp_eq_u32 s5, 24576\n"
"s_cselect_b32 s5, 0, s5\n"
"s_waitcnt vmcnt(6)\n"
"s_mov_b64 exec, 0x1f\n"
"v_cmp_ne_u32 vcc, s71, v252\n"
"s_mov_b64 exec, -1\n"
"s_cbranch_vccnz Lns40_%=\n"
"Lnb41_%=:\n"
"s_mov_b64 exec, 1\n"
"ds_add_u32 v243, v244 offset:40\n"
"s_mov_b64 exec, -1\n"
"v_mfma_f32_32x32x16_bf16 v[32:47], v[176:179], v[68:71], 0\n"
"v_mfma_f32_32x32x16_bf16 v[0:15], v[176:179], v[64:67], 0\n"
"ds_read_b64_tr_b16 v[176:177], v248 offset:4096\n"
"ds_read_b64_tr_b16 v[178:179], v248 offset:5120\n"
"v_mfma_f32_32x32x16_bf16 v[48:63], v[180:183], v[68:71], 0\n"
"v_mfma_f32_32x32x16_bf16 v[16:31], v[180:183], v[64:67], 0\n"
"ds_read_b64_tr_b16 v[180:181], v249 offset:4096\n"
"ds_read_b64_tr_b16 v[182:183], v249 offset:5120\n"
"buffer_load_dwordx4 v[64:67], v246, s[48:51], s5 offen offset:0 sc1\n"
"buffer_load_dwordx4 v[68:71], v247, s[48:51], s5 offen offset:0 sc1\n"
"s_add_u32 s5, s5, 1024\n"
"s_cmp_eq_u32 s5, 24576\n"
"s_cselect_b32 s5, 0, s5\n"
"v_mfma_f32_32x32x16_bf16 v[32:47], v[184:187], v[76:79], v[32:47]\n"
"v_mfma_f32_32x32x16_bf16 v[0:15], v[184:187], v[72:75], v[0:15]\n"
"ds_read_b64_tr_b16 v[184:185], v248 offset:6144\n"
"ds_read_b64_tr_b16 v[186:187], v248 offset:7168\n"
"v_mfma_f32_32x32x16_bf16 v[48:63], v[188:191], v[76:79], v[48:63]\n"
"v_mfma_f32_32x32x16_bf16 v[16:31], v[188:191], v[72:75], v[16:31]\n"
"ds_read_b64_tr_b16 v[188:189], v249 offset:6144\n"
"ds_read_b64_tr_b16 v[190:191], v249 offset:7168\n"
"buffer_load_dwordx4 v[72:75], v246, s[48:51], s5 offen offset:0 sc1\n"
"buffer_load_dwordx4 v[76:79], v247, s[48:51], s5 offen offset:0 sc1\n"
"s_add_u32 s5, s5, 1024\n"
"s_cmp_eq_u32 s5, 24576\n"
"s_cselect_b32 s5, 0, s5\n"
"s_waitcnt lgkmcnt(4)\n"
"s_add_u32 m0, s81, 4096\n"
"s_nop 0\n"
"buffer_load_dwordx4 v241, s[36:39], s4 offen lds\n"
"s_add_u32 m0, s81, 5120\n"
"s_add_u32 s91, s4, 7168\n"
"buffer_load_dwordx4 v241, s[36:39], s91 offen lds\n"
"s_add_u32 s4, s4, 14336\n"
"s_cmp_eq_u32 s4, 344064\n"
"s_cselect_b32 s4, 0, s4\n"
"v_mfma_f32_32x32x16_bf16 v[32:47], v[192:195], v[84:87], v[32:47]\n"
"v_mfma_f32_32x32x16_bf16 v[0:15], v[192:195], v[80:83], v[0:15]\n"
"ds_read_b64_tr_b16 v[192:193], v248 offset:8192\n"
"ds_read_b64_tr_b16 v[194:195], v248 offset:9216\n"
"v_mfma_f32_32x32x16_bf16 v[48:63], v[196:199], v[84:87], v[48:63]\n"
"v_mfma_f32_32x32x16_bf16 v[16:31], v[196:199], v[80:83], v[16:31]\n"
"ds_read_b64_tr_b16 v[196:197], v249 offset:8192\n"
"ds_read_b64_tr_b16 v[198:199], v249 offset:9216\n"
"buffer_load_dwordx4 v[80:83], v246, s[48:51], s5 offen offset:0 sc1\n"
"buffer_load_dwordx4 v[84:87], v247, s[48:51], s5 offen offset:0 sc1\n"
"s_add_u32 s5, s5, 1024\n"
"s_cmp_eq_u32 s5, 24576\n"
"s_cselect_b32 s5, 0, s5\n"
"s_waitcnt lgkmcnt(4)\n"
"s_add_u32 m0, s81, 6144\n"
"s_nop 0\n"
"buffer_load_dwordx4 v241, s[36:39], s4 offen lds\n"
"s_add_u32 m0, s81, 7168\n"
"s_add_u32 s91, s4, 7168\n"
"buffer_load_dwordx4 v241, s[36:39], s91 offen lds\n"
"s_add_u32 s4, s4, 14336\n"
"s_cmp_eq_u32 s4, 344064\n"
"s_cselect_b32 s4, 0, s4\n"
"v_mfma_f32_32x32x16_bf16 v[32:47], v[200:203], v[92:95], v[32:47]\n"
"v_mfma_f32_32x32x16_bf16 v[0:15], v[200:203], v[88:91], v[0:15]\n"
"ds_read_b64_tr_b16 v[200:201], v248 offset:10240\n"
"ds_read_b64_tr_b16 v[202:203], v248 offset:11264\n"
"v_mfma_f32_32x32x16_bf16 v[48:63], v[204:207], v[92:95], v[48:63]\n"
"v_mfma_f32_32x32x16_bf16 v[16:31], v[204:207], v[88:91], v[16:31]\n"
"ds_read_b64_tr_b16 v[204:205], v249 offset:10240\n"
"ds_read_b64_tr_b16 v[206:207], v249 offset:11264\n"
"buffer_load_dwordx4 v[88:91], v246, s[48:51], s5 offen offset:0 sc1\n"
"buffer_load_dwordx4 v[92:95], v247, s[48:51], s5 offen offset:0 sc1\n"
"s_add_u32 s5, s5, 1024\n"
"s_cmp_eq_u32 s5, 24576\n"
"s_cselect_b32 s5, 0, s5\n"
"s_waitcnt lgkmcnt(4)\n"
"s_add_u32 m0, s81, 8192\n"
"s_nop 0\n"
"buffer_load_dwordx4 v241, s[36:39], s4 offen lds\n"
"s_add_u32 m0, s81, 9216\n"
"s_add_u32 s91, s4, 7168\n"
"buffer_load_dwordx4 v241, s[36:39], s91 offen lds\n"
"s_add_u32 s4, s4, 14336\n"
"s_cmp_eq_u32 s4, 344064\n"
"s_cselect_b32 s4, 0, s4\n"
"v_mfma_f32_32x32x16_bf16 v[32:47], v[208:211], v[100:103], v[32:47]\n"
"v_mfma_f32_32x32x16_bf16 v[0:15], v[208:211], v[96:99], v[0:15]\n"
"ds_read_b64_tr_b16 v[208:209], v248 offset:0\n"
"ds_read_b64_tr_b16 v[210:211], v248 offset:1024\n"
"v_mfma_f32_32x32x16_bf16 v[48:63], v[212:215], v[100:103], v[48:63]\n"
"v_mfma_f32_32x32x16_bf16 v[16:31], v[212:215], v[96:99], v[16:31]\n"
"ds_read_b64_tr_b16 v[212:213], v249 offset:0\n"
"ds_read_b64_tr_b16 v[214:215], v249 offset:1024\n"
"buffer_load_dwordx4 v[96:99], v246, s[48:51], s5 offen offset:0 sc1\n"
"buffer_load_dwordx4 v[100:103], v247, s[48:51], s5 offen offset:0 sc1\n"
"s_add_u32 s5, s5, 1024\n"
"s_cmp_eq_u32 s5, 24576\n"
"s_cselect_b32 s5, 0, s5\n"
"s_waitcnt lgkmcnt(4)\n"
"s_add_u32 m0, s81, 10240\n"
"s_nop 0\n"
"buffer_load_dwordx4 v241, s[36:39], s4 offen lds\n"
"s_add_u32 m0, s81, 11264\n"
"s_add_u32 s91, s4, 7168\n"
"buffer_load_dwordx4 v241, s[36:39], s91 offen lds\n"
"s_add_u32 s4, s4, 14336\n"
"s_cmp_eq_u32 s4, 344064\n"
"s_cselect_b32 s4, 0, s4\n"
"v_mfma_f32_32x32x16_bf16 v[32:47], v[216:219], v[108:111], v[32:47]\n"
"v_mfma_f32_32x32x16_bf16 v[0:15], v[216:219], v[104:107], v[0:15]\n"
"ds_read_b64_tr_b16 v[216:217], v248 offset:2048\n"
"ds_read_b64_tr_b16 v[218:219], v248 offset:3072\n"
"v_mfma_f32_32x32x16_bf16 v[48:63], v[220:223], v[108:111], v[48:63]\n"
"v_mfma_f32_32x32x16_bf16 v[16:31], v[220:223], v[104:107], v[16:31]\n"
"ds_read_b64_tr_b16 v[220:221], v249 offset:2048\n"
"ds_read_b64_tr_b16 v[222:223], v249 offset:3072\n"
"buffer_load_dwordx4 v[104:107], v246, s[48:51], s5 offen offset:0 sc1\n"
"buffer_load_dwordx4 v[108:111], v247, s[48:51], s5 offen offset:0 sc1\n"
"s_add_u32 s5, s5, 1024\n"
"s_cmp_eq_u32 s5, 24576\n"
"s_cselect_b32 s5, 0, s5\n"
"s_waitcnt lgkmcnt(4)\n"
"s_add_u32 m0, s81, 0\n"
"s_nop 0\n"
"buffer_load_dwordx4 v241, s[36:39], s4 offen lds\n"
"s_add_u32 m0, s81, 1024\n"
"s_add_u32 s91, s4, 7168\n"
"buffer_load_dwordx4 v241, s[36:39], s91 offen lds\n"
"s_add_u32 s4, s4, 14336\n"
"s_cmp_eq_u32 s4, 344064\n"
"s_cselect_b32 s4, 0, s4\n"
"v_mfma_f32_32x32x16_bf16 v[32:47], v[224:227], v[116:119], v[32:47]\n"
"v_mfma_f32_32x32x16_bf16 v[0:15], v[224:227], v[112:115], v[0:15]\n"
"s_waitcnt vmcnt(16)\n"
"ds_read_b64_tr_b16 v[224:225], v248 offset:4096\n"
"ds_read_b64_tr_b16 v[226:227], v248 offset:5120\n"
"v_mfma_f32_32x32x16_bf16 v[48:63], v[228:231], v[116:119], v[48:63]\n"
"v_mfma_f32_32x32x16_bf16 v[16:31], v[228:231], v[112:115], v[16:31]\n"
"ds_read_b64_tr_b16 v[228:229], v249 offset:4096\n"
"ds_read_b64_tr_b16 v[230:231], v249 offset:5120\n"
"buffer_load_dwordx4 v[112:115], v246, s[48:51], s5 offen offset:0 sc1\n"
"buffer_load_dwordx4 v[116:119], v247, s[48:51], s5 offen offset:0 sc1\n"
"s_add_u32 s5, s5, 1024\n"
"s_cmp_eq_u32 s5, 24576\n"
"s_cselect_b32 s5, 0, s5\n"
"s_waitcnt lgkmcnt(4)\n"
"s_add_u32 m0, s81, 2048\n"
"s_nop 0\n"
"buffer_load_dwordx4 v241, s[36:39], s4 offen lds\n"
"s_add_u32 m0, s81, 3072\n"
"s_add_u32 s91, s4, 7168\n"
"buffer_load_dwordx4 v241, s[36:39], s91 offen lds\n"
"s_add_u32 s4, s4, 14336\n"
"s_cmp_eq_u32 s4, 344064\n"
"s_cselect_b32 s4, 0, s4\n"
"v_mfma_f32_32x32x16_bf16 v[32:47], v[232:235], v[124:127], v[32:47]\n"
"v_mfma_f32_32x32x16_bf16 v[0:15], v[232:235], v[120:123], v[0:15]\n"
"s_waitcnt vmcnt(16)\n"
"ds_read_b64_tr_b16 v[232:233], v248 offset:6144\n"
"ds_read_b64_tr_b16 v[234:235], v248 offset:7168\n"
"v_mfma_f32_32x32x16_bf16 v[48:63], v[236:239], v[124:127], v[48:63]\n"
"v_mfma_f32_32x32x16_bf16 v[16:31], v[236:239], v[120:123], v[16:31]\n"
"ds_read_b64_tr_b16 v[236:237], v249 offset:6144\n"
"ds_read_b64_tr_b16 v[238:239], v249 offset:7168\n"
"buffer_load_dwordx4 v[120:123], v246, s[48:51], s5 offen offset:0 sc1\n"
"buffer_load_dwordx4 v[124:127], v247, s[48:51], s5 offen offset:0 sc1\n"
"s_add_u32 s5, s5, 1024\n"
"s_cmp_eq_u32 s5, 24576\n"
"s_cselect_b32 s5, 0, s5\n"
"s_waitcnt lgkmcnt(4)\n"
"s_add_u32 m0, s81, 4096\n"
"s_nop 0\n"
"buffer_load_dwordx4 v241, s[36:39], s4 offen lds\n"
"s_add_u32 m0, s81, 5120\n"
"s_add_u32 s91, s4, 7168\n"
"buffer_load_dwordx4 v241, s[36:39], s91 offen lds\n"
"s_add_u32 s4, s4, 14336\n"
"s_cmp_eq_u32 s4, 344064\n"
"s_cselect_b32 s4, 0, s4\n"
"v_mfma_f32_32x32x16_bf16 v[32:47], v[176:179], v[132:135], v[32:47]\n"
"v_mfma_f32_32x32x16_bf16 v[0:15], v[176:179], v[128:131], v[0:15]\n"
"s_waitcnt vmcnt(16)\n"
"ds_read_b64_tr_b16 v[176:177], v248 offset:8192\n"
"ds_read_b64_tr_b16 v[178:179], v248 offset:9216\n"
"v_mfma_f32_32x32x16_bf16 v[48:63], v[180:183], v[132:135], v[48:63]\n"
"v_mfma_f32_32x32x16_bf16 v[16:31], v[180:183], v[128:131], v[16:31]\n"
"ds_read_b64_tr_b16 v[180:181], v249 offset:8192\n"
"ds_read_b64_tr_b16 v[182:183], v249 offset:9216\n"
"buffer_load_dwordx4 v[128:131], v246, s[48:51], s5 offen offset:0 sc1\n"
"buffer_load_dwordx4 v[132:135], v247, s[48:51], s5 offen offset:0 sc1\n"
"s_add_u32 s5, s5, 1024\n"
"s_cmp_eq_u32 s5, 24576\n"
"s_cselect_b32 s5, 0, s5\n"
"s_waitcnt lgkmcnt(4)\n"
"s_add_u32 m0, s81, 6144\n"
"s_nop 0\n"
"buffer_load_dwordx4 v241, s[36:39], s4 offen lds\n"
"s_add_u32 m0, s81, 7168\n"
"s_add_u32 s91, s4, 7168\n"
"buffer_load_dwordx4 v241, s[36:39], s91 offen lds\n"
"s_add_u32 s4, s4, 14336\n"
"s_cmp_eq_u32 s4, 344064\n"
"s_cselect_b32 s4, 0, s4\n"
"v_mfma_f32_32x32x16_bf16 v[32:47], v[184:187], v[140:143], v[32:47]\n"
"v_mfma_f32_32x32x16_bf16 v[0:15], v[184:187], v[136:139], v[0:15]\n"
"s_waitcnt vmcnt(16)\n"
"ds_read_b64_tr_b16 v[184:185], v248 offset:10240\n"
"ds_read_b64_tr_b16 v[186:187], v248 offset:11264\n"
"v_mfma_f32_32x32x16_bf16 v[48:63], v[188:191], v[140:143], v[48:63]\n"
"v_mfma_f32_32x32x16_bf16 v[16:31], v[188:191], v[136:139], v[16:31]\n"
"ds_read_b64_tr_b16 v[188:189], v249 offset:10240\n"
"ds_read_b64_tr_b16 v[190:191], v249 offset:11264\n"
"buffer_load_dwordx4 v[136:139], v246, s[48:51], s5 offen offset:0 sc1\n"
"buffer_load_dwordx4 v[140:143], v247, s[48:51], s5 offen offset:0 sc1\n"
"s_movk_i32 s5, 24576\n"
"s_waitcnt lgkmcnt(4)\n"
"s_add_u32 m0, s81, 8192\n"
"s_nop 0\n"
"buffer_load_dwordx4 v241, s[36:39], s4 offen lds\n"
"s_add_u32 m0, s81, 9216\n"
"s_add_u32 s91, s4, 7168\n"
"buffer_load_dwordx4 v241, s[36:39], s91 offen lds\n"
"s_add_u32 s4, s4, 14336\n"
"s_cmp_eq_u32 s4, 344064\n"
"s_cselect_b32 s4, 0, s4\n"
"v_mfma_f32_32x32x16_bf16 v[32:47], v[192:195], v[148:151], v[32:47]\n"
"v_mfma_f32_32x32x16_bf16 v[0:15], v[192:195], v[144:147], v[0:15]\n"
"s_waitcnt vmcnt(16)\n"
"ds_read_b64_tr_b16 v[192:193], v248 offset:0\n"
"ds_read_b64_tr_b16 v[194:195], v248 offset:1024\n"
"v_mfma_f32_32x32x16_bf16 v[48:63], v[196:199], v[148:151], v[48:63]\n"
"v_mfma_f32_32x32x16_bf16 v[16:31], v[196:199], v[144:147], v[16:31]\n"
"ds_read_b64_tr_b16 v[196:197], v249 offset:0\n"
"ds_read_b64_tr_b16 v[198:199], v249 offset:1024\n"
"buffer_load_dwordx4 v[144:147], v246, s[48:51], s5 offen offset:0 sc1\n"
"buffer_load_dwordx4 v[148:151], v247, s[48:51], s5 offen offset:0 sc1\n"
"s_movk_i32 s5, 25600\n"
"s_waitcnt lgkmcnt(4)\n"
"s_add_u32 m0, s81, 10240\n"
"s_nop 0\n"
"buffer_load_dwordx4 v241, s[36:39], s4 offen lds\n"
"s_add_u32 m0, s81, 11264\n"
"s_add_u32 s91, s4, 7168\n"
"buffer_load_dwordx4 v241, s[36:39], s91 offen lds\n"
"s_mov_b32 s4, 344064\n"
"v_mfma_f32_32x32x16_bf16 v[32:47], v[200:203], v[156:159], v[32:47]\n"
"v_mfma_f32_32x32x16_bf16 v[0:15], v[200:203], v[152:155], v[0:15]\n"
"s_waitcnt vmcnt(16)\n"
"ds_read_b64_tr_b16 v[200:201], v248 offset:2048\n"
"ds_read_b64_tr_b16 v[202:203], v248 offset:3072\n"
"v_mfma_f32_32x32x16_bf16 v[48:63], v[204:207], v[156:159], v[48:63]\n"
"v_mfma_f32_32x32x16_bf16 v[16:31], v[204:207], v[152:155], v[16:31]\n"
"ds_read_b64_tr_b16 v[204:205], v249 offset:2048\n"
"ds_read_b64_tr_b16 v[206:207], v249 offset:3072\n"
"buffer_load_dwordx4 v[152:155], v246, s[48:51], s5 offen offset:0 sc1\n"
"buffer_load_dwordx4 v[156:159], v247, s[48:51], s5 offen offset:0 sc1\n"
"s_movk_i32 s5, 26624\n"
"s_waitcnt lgkmcnt(4)\n"
"s_add_u32 m0, s81, 0\n"
"s_nop 0\n"
"buffer_load_dwordx4 v241, s[36:39], s4 offen lds\n"
"s_add_u32 m0, s81, 1024\n"
"s_add_u32 s91, s4, 7168\n"
"buffer_load_dwordx4 v241, s[36:39], s91 offen lds\n"
"s_mov_b32 s4, 358400\n"
"v_mfma_f32_32x32x16_bf16 v[32:47], v[208:211], v[164:167], v[32:47]\n"
"v_mfma_f32_32x32x16_bf16 v[0:15], v[208:211], v[160:163], v[0:15]\n"
"s_waitcnt vmcnt(16)\n"
"ds_read_b64_tr_b16 v[208:209], v248 offset:4096\n"
"ds_read_b64_tr_b16 v[210:211], v248 offset:5120\n"
"v_mfma_f32_32x32x16_bf16 v[48:63], v[212:215], v[164:167], v[48:63]\n"
"v_mfma_f32_32x32x16_bf16 v[16:31], v[212:215], v[160:163], v[16:31]\n"
"ds_read_b64_tr_b16 v[212:213], v249 offset:4096\n"
"ds_read_b64_tr_b16 v[214:215], v249 offset:5120\n"
"buffer_load_dwordx4 v[160:163], v246, s[48:51], s5 offen offset:0 sc1\n"
"buffer_load_dwordx4 v[164:167], v247, s[48:51], s5 offen offset:0 sc1\n"
"s_movk_i32 s5, 27648\n"
"s_waitcnt lgkmcnt(4)\n"
"s_add_u32 m0, s81, 2048\n"
"s_nop 0\n"
"buffer_load_dwordx4 v241, s[36:39], s4 offen lds\n"
"s_add_u32 m0, s81, 3072\n"
"s_add_u32 s91, s4, 7168\n"
"buffer_load_dwordx4 v241, s[36:39], s91 offen lds\n"
"s_mov_b32 s4, 372736\n"
"s_waitcnt lgkmcnt(0)\n"
"s_add_u32 m0, s81, 4096\n"
"s_nop 0\n"
"buffer_load_dwordx4 v241, s[36:39], s4 offen lds\n"
"s_add_u32 m0, s81, 5120\n"
"s_add_u32 s91, s4, 7168\n"
"buffer_load_dwordx4 v241, s[36:39], s91 offen lds\n"
"s_mov_b32 s4, 387072\n"
"s_barrier\n"
"v_mfma_f32_32x32x16_bf16 v[32:47], v[216:219], v[172:175], v[32:47]\n"
"v_mfma_f32_32x32x16_bf16 v[0:15], v[216:219], v[168:171], v[0:15]\n"
"s_waitcnt vmcnt(18)\n"
"ds_read_b64_tr_b16 v[216:217], v248 offset:6144\n"
"ds_read_b64_tr_b16 v[218:219], v248 offset:7168\n"
"v_mfma_f32_32x32x16_bf16 v[48:63], v[220:223], v[172:175], v[48:63]\n"
"v_mfma_f32_32x32x16_bf16 v[16:31], v[220:223], v[168:171], v[16:31]\n"
"ds_read_b64_tr_b16 v[220:221], v249 offset:6144\n"
"ds_read_b64_tr_b16 v[222:223], v249 offset:7168\n"
"buffer_load_dwordx4 v[168:171], v246, s[48:51], s5 offen offset:0 sc1\n"
"buffer_load_dwordx4 v[172:175], v247, s[48:51], s5 offen offset:0 sc1\n"
"s_movk_i32 s5, 28672\n"
"v_mfma_f32_32x32x16_bf16 v[32:47], v[224:227], v[68:71], v[32:47]\n"
"v_mfma_f32_32x32x16_bf16 v[0:15], v[224:227], v[64:67], v[0:15]\n"
"s_waitcnt vmcnt(16)\n"
"ds_read_b64_tr_b16 v[224:225], v248 offset:8192\n"
"ds_read_b64_tr_b16 v[226:227], v248 offset:9216\n"
"v_mfma_f32_32x32x16_bf16 v[48:63], v[228:231], v[68:71], v[48:63]\n"
"v_mfma_f32_32x32x16_bf16 v[16:31], v[228:231], v[64:67], v[16:31]\n"
"ds_read_b64_tr_b16 v[228:229], v249 offset:8192\n"
"ds_read_b64_tr_b16 v[230:231], v249 offset:9216\n"
"s_waitcnt lgkmcnt(4)\n"
"s_add_u32 m0, s81, 6144\n"
"s_nop 0\n"
"buffer_load_dwordx4 v241, s[36:39], s4 offen lds\n"
"s_add_u32 m0, s81, 7168\n"
"s_add_u32 s91, s4, 7168\n"
"buffer_load_dwordx4 v241, s[36:39], s91 offen lds\n"
"s_mov_b32 s4, 401408\n"
"v_mfma_f32_32x32x16_bf16 v[32:47], v[232:235], v[76:79], v[32:47]\n"
"v_mfma_f32_32x32x16_bf16 v[0:15], v[232:235], v[72:75], v[0:15]\n"
"s_waitcnt vmcnt(14)\n"
"ds_read_b64_tr_b16 v[232:233], v248 offset:10240\n"
"ds_read_b64_tr_b16 v[234:235], v248 offset:11264\n"
"v_mfma_f32_32x32x16_bf16 v[48:63], v[236:239], v[76:79], v[48:63]\n"
"v_mfma_f32_32x32x16_bf16 v[16:31], v[236:239], v[72:75], v[16:31]\n"
"ds_read_b64_tr_b16 v[236:237], v249 offset:10240\n"
"ds_read_b64_tr_b16 v[238:239], v249 offset:11264\n"
"v_mfma_f32_32x32x16_bf16 v[32:47], v[176:179], v[84:87], v[32:47]\n"
"v_mfma_f32_32x32x16_bf16 v[0:15], v[176:179], v[80:83], v[0:15]\n"
"s_waitcnt vmcnt(10)\n"
"ds_read_b64_tr_b16 v[176:177], v248 offset:0\n"
"ds_read_b64_tr_b16 v[178:179], v248 offset:1024\n"
"v_mfma_f32_32x32x16_bf16 v[48:63], v[180:183], v[84:87], v[48:63]\n"
"v_mfma_f32_32x32x16_bf16 v[16:31], v[180:183], v[80:83], v[16:31]\n"
"ds_read_b64_tr_b16 v[180:181], v249 offset:0\n"
"ds_read_b64_tr_b16 v[182:183], v249 offset:1024\n"
"v_mfma_f32_32x32x16_bf16 v[32:47], v[184:187], v[92:95], v[32:47]\n"
"v_mfma_f32_32x32x16_bf16 v[0:15], v[184:187], v[88:91], v[0:15]\n"
"s_waitcnt vmcnt(6)\n"
"ds_read_b64_tr_b16 v[184:185], v248 offset:2048\n"
"ds_read_b64_tr_b16 v[186:187], v248 offset:3072\n"
"v_mfma_f32_32x32x16_bf16 v[48:63], v[188:191], v[92:95], v[48:63]\n"
"v_mfma_f32_32x32x16_bf16 v[16:31], v[188:191], v[88:91], v[16:31]\n"
"ds_read_b64_tr_b16 v[188:189], v249 offset:2048\n"
"ds_read_b64_tr_b16 v[190:191], v249 offset:3072\n"
"v_mfma_f32_32x32x16_bf16 v[32:47], v[192:195], v[100:103], v[32:47]\n"
"v_mfma_f32_32x32x16_bf16 v[0:15], v[192:195], v[96:99], v[0:15]\n"
"s_waitcnt vmcnt(4)\n"
"ds_read_b64_tr_b16 v[192:193], v248 offset:4096\n"
"ds_read_b64_tr_b16 v[194:195], v248 offset:5120\n"
"v_mfma_f32_32x32x16_bf16 v[48:63], v[196:199], v[100:103], v[48:63]\n"
"v_mfma_f32_32x32x16_bf16 v[16:31], v[196:199], v[96:99], v[16:31]\n"
"ds_read_b64_tr_b16 v[196:197], v249 offset:4096\n"
"ds_read_b64_tr_b16 v[198:199], v249 offset:5120\n"
"v_mfma_f32_32x32x16_bf16 v[32:47], v[200:203], v[108:111], v[32:47]\n"
"v_mfma_f32_32x32x16_bf16 v[0:15], v[200:203], v[104:107], v[0:15]\n"
"s_waitcnt vmcnt(0)\n"
"ds_read_b64_tr_b16 v[200:201], v248 offset:6144\n"
"ds_read_b64_tr_b16 v[202:203], v248 offset:7168\n"
"v_mfma_f32_32x32x16_bf16 v[48:63], v[204:207], v[108:111], v[48:63]\n"
"v_mfma_f32_32x32x16_bf16 v[16:31], v[204:207], v[104:107], v[16:31]\n"
"ds_read_b64_tr_b16 v[204:205], v249 offset:6144\n"
"ds_read_b64_tr_b16 v[206:207], v249 offset:7168\n"
"v_mfma_f32_32x32x16_bf16 v[32:47], v[208:211], v[116:119], v[32:47]\n"
"v_mfma_f32_32x32x16_bf16 v[0:15], v[208:211], v[112:115], v[0:15]\n"
"v_mfma_f32_32x32x16_bf16 v[48:63], v[212:215], v[116:119], v[48:63]\n"
"v_mfma_f32_32x32x16_bf16 v[16:31], v[212:215], v[112:115], v[16:31]\n"
"ds_read_b32 v252, v243 offset:28\n"
"v_mfma_f32_32x32x16_bf16 v[32:47], v[216:219], v[124:127], v[32:47]\n"
"v_mfma_f32_32x32x16_bf16 v[0:15], v[216:219], v[120:123], v[0:15]\n"
"v_mfma_f32_32x32x16_bf16 v[48:63], v[220:223], v[124:127], v[48:63]\n"
"v_mfma_f32_32x32x16_bf16 v[16:31], v[220:223], v[120:123], v[16:31]\n"
"v_add_u32 v247, s81, v245\n"
"s_xor_b32 s84, s70, 1\n"
"s_mul_i32 s84, s84, 12288\n"
"s_add_u32 s84, s84, 107520\n"
"v_add_u32 v241, s84, v245\n"
"s_xor_b32 s84, s70, 2\n"
"s_mul_i32 s84, s84, 12288\n"
"s_add_u32 s84, s84, 111616\n"
"v_add_u32 v248, s84, v245\n"
"s_xor_b32 s84, s70, 3\n"
"s_mul_i32 s84, s84, 12288\n"
"s_add_u32 s84, s84, 115712\n"
"v_add_u32 v249, s84, v245\n"
"s_waitcnt lgkmcnt(0)\n"
"v_cmp_gt_u32 vcc, 4, v252\n"
"s_nop 0\n"
"s_cbranch_vccnz Lcs44_%=\n"
"Lcb45_%=:\n"
"v_mfma_f32_32x32x16_bf16 v[48:63], v[228:231], v[132:135], v[48:63]\n"
"v_mfma_f32_32x32x16_bf16 v[48:63], v[236:239], v[140:143], v[48:63]\n"
"v_mfma_f32_32x32x16_bf16 v[48:63], v[180:183], v[148:151], v[48:63]\n"
"v_mfma_f32_32x32x16_bf16 v[48:63], v[188:191], v[156:159], v[48:63]\n"
"v_mfma_f32_32x32x16_bf16 v[48:63], v[196:199], v[164:167], v[48:63]\n"
"v_mfma_f32_32x32x16_bf16 v[48:63], v[204:207], v[172:175], v[48:63]\n"
"v_mfma_f32_32x32x16_bf16 v[16:31], v[228:231], v[128:131], v[16:31]\n"
"s_nop 4\n"
"v_mfma_f32_32x32x16_bf16 v[16:31], v[236:239], v[136:139], v[16:31]\n"
"s_nop 4\n"
"v_mfma_f32_32x32x16_bf16 v[16:31], v[180:183], v[144:147], v[16:31]\n"
"ds_write_b128 v247, v[48:51] offset:8192\n"
"v_mfma_f32_32x32x16_bf16 v[16:31], v[188:191], v[152:155], v[16:31]\n"
"ds_write_b128 v247, v[52:55] offset:9216\n"
"v_mfma_f32_32x32x16_bf16 v[16:31], v[196:199], v[160:163], v[16:31]\n"
"ds_write_b128 v247, v[56:59] offset:10240\n"
"v_mfma_f32_32x32x16_bf16 v[16:31], v[204:207], v[168:171], v[16:31]\n"
"ds_write_b128 v247, v[60:63] offset:11264\n"
"v_mfma_f32_32x32x16_bf16 v[32:47], v[224:227], v[132:135], v[32:47]\n"
"s_nop 4\n"
"v_mfma_f32_32x32x16_bf16 v[32:47], v[232:235], v[140:143], v[32:47]\n"
"s_nop 4\n"
"v_mfma_f32_32x32x16_bf16 v[32:47], v[176:179], v[148:151], v[32:47]\n"
"ds_write_b128 v247, v[16:19] offset:0\n"
"v_mfma_f32_32x32x16_bf16 v[32:47], v[184:187], v[156:159], v[32:47]\n"
"ds_write_b128 v247, v[20:23] offset:1024\n"
"v_mfma_f32_32x32x16_bf16 v[32:47], v[192:195], v[164:167], v[32:47]\n"
"ds_write_b128 v247, v[24:27] offset:2048\n"
"v_mfma_f32_32x32x16_bf16 v[32:47], v[200:203], v[172:175], v[32:47]\n"
"ds_write_b128 v247, v[28:31] offset:3072\n"
"v_mfma_f32_32x32x16_bf16 v[0:15], v[224:227], v[128:131], v[0:15]\n"
"s_nop 4\n"
"v_mfma_f32_32x32x16_bf16 v[0:15], v[232:235], v[136:139], v[0:15]\n"
"s_nop 4\n"
"v_mfma_f32_32x32x16_bf16 v[0:15], v[176:179], v[144:147], v[0:15]\n"
"ds_write_b128 v247, v[32:35] offset:4096\n"
"v_mfma_f32_32x32x16_bf16 v[0:15], v[184:187], v[152:155], v[0:15]\n"
"ds_write_b128 v247, v[36:39] offset:5120\n"
"v_mfma_f32_32x32x16_bf16 v[0:15], v[192:195], v[160:163], v[0:15]\n"
"ds_write_b128 v247, v[40:43] offset:6144\n"
"v_mfma_f32_32x32x16_bf16 v[0:15], v[200:203], v[168:171], v[0:15]\n"
"ds_write_b128 v247, v[44:47] offset:7168\n"
"s_setprio 3\n"
"s_mov_b64 exec, 1\n"
"ds_add_u32 v243, v244 offset:24\n"
"s_mov_b64 exec, -1\n"
"ds_read_b32 v252, v243 offset:24\n"
"s_waitcnt lgkmcnt(0)\n"
"v_cmp_gt_u32 vcc, 4, v252\n"
"s_nop 0\n"
"s_cbranch_vccnz Lcs47_%=\n"
"Lcb48_%=:\n"
"ds_read_b128 v[16:19], v241 offset:0\n"
"ds_read_b128 v[32:35], v248 offset:0\n"
"ds_read_b128 v[48:51], v249 offset:0\n"
"ds_read_b128 v[20:23], v241 offset:1024\n"
"ds_read_b128 v[36:39], v248 offset:1024\n"
"ds_read_b128 v[52:55], v249 offset:1024\n"
"ds_read_b128 v[24:27], v241 offset:2048\n"
"ds_read_b128 v[40:43], v248 offset:2048\n"
"ds_read_b128 v[56:59], v249 offset:2048\n"
"ds_read_b128 v[28:31], v241 offset:3072\n"
"ds_read_b128 v[44:47], v248 offset:3072\n"
"ds_read_b128 v[60:63], v249 offset:3072\n"
"s_waitcnt lgkmcnt(6)\n"
"v_add_f32 v16, v16, v32\n"
"v_add_f32 v0, v0, v48\n"
"v_add_f32 v17, v17, v33\n"
"v_add_f32 v1, v1, v49\n"
"v_add_f32 v18, v18, v34\n"
"v_add_f32 v2, v2, v50\n"
"v_add_f32 v19, v19, v35\n"
"v_add_f32 v3, v3, v51\n"
"v_add_f32 v20, v20, v36\n"
"v_add_f32 v4, v4, v52\n"
"v_add_f32 v21, v21, v37\n"
"v_add_f32 v5, v5, v53\n"
"v_add_f32 v22, v22, v38\n"
"v_add_f32 v6, v6, v54\n"
"v_add_f32 v23, v23, v39\n"
"v_add_f32 v7, v7, v55\n"
"v_add_f32 v0, v0, v16\n"
"v_add_f32 v1, v1, v17\n"
"v_add_f32 v2, v2, v18\n"
"v_add_f32 v3, v3, v19\n"
"v_add_f32 v4, v4, v20\n"
"v_add_f32 v5, v5, v21\n"
"v_add_f32 v6, v6, v22\n"
"v_add_f32 v7, v7, v23\n"
"v_mul_f32 v16, 0xbfb8aa3b, v0\n"
"v_mul_f32 v17, 0xbfb8aa3b, v1\n"
"v_mul_f32 v18, 0xbfb8aa3b, v2\n"
"v_mul_f32 v19, 0xbfb8aa3b, v3\n"
"v_mul_f32 v20, 0xbfb8aa3b, v4\n"
"v_mul_f32 v21, 0xbfb8aa3b, v5\n"
"v_mul_f32 v22, 0xbfb8aa3b, v6\n"
"v_mul_f32 v23, 0xbfb8aa3b, v7\n"
"v_exp_f32 v16, v16\n"
"v_exp_f32 v17, v17\n"
"v_exp_f32 v18, v18\n"
"v_exp_f32 v19, v19\n"
"v_exp_f32 v20, v20\n"
"v_exp_f32 v21, v21\n"
"v_exp_f32 v22, v22\n"
"v_exp_f32 v23, v23\n"
"v_add_f32 v16, 1.0, v16\n"
"v_add_f32 v17, 1.0, v17\n"
"v_add_f32 v18, 1.0, v18\n"
"v_add_f32 v19, 1.0, v19\n"
"v_add_f32 v20, 1.0, v20\n"
"v_add_f32 v21, 1.0, v21\n"
"v_add_f32 v22, 1.0, v22\n"
"v_add_f32 v23, 1.0, v23\n"
"v_rcp_f32 v16, v16\n"
"v_rcp_f32 v17, v17\n"
"v_rcp_f32 v18, v18\n"
"v_rcp_f32 v19, v19\n"
"v_rcp_f32 v20, v20\n"
"v_rcp_f32 v21, v21\n"
"v_rcp_f32 v22, v22\n"
"v_rcp_f32 v23, v23\n"
"v_mul_f32 v0, v0, v16\n"
"v_mul_f32 v1, v1, v17\n"
"v_mul_f32 v2, v2, v18\n"
"v_mul_f32 v3, v3, v19\n"
"v_mul_f32 v4, v4, v20\n"
"v_mul_f32 v5, v5, v21\n"
"v_mul_f32 v6, v6, v22\n"
"v_mul_f32 v7, v7, v23\n"
"ds_write_b128 v250, v[0:3] offset:0\n"
"ds_write_b128 v250, v[4:7] offset:32\n"
"s_waitcnt lgkmcnt(2)\n"
"v_add_f32 v24, v24, v40\n"
"v_add_f32 v8, v8, v56\n"
"v_add_f32 v25, v25, v41\n"
"v_add_f32 v9, v9, v57\n"
"v_add_f32 v26, v26, v42\n"
"v_add_f32 v10, v10, v58\n"
"v_add_f32 v27, v27, v43\n"
"v_add_f32 v11, v11, v59\n"
"v_add_f32 v28, v28, v44\n"
"v_add_f32 v12, v12, v60\n"
"v_add_f32 v29, v29, v45\n"
"v_add_f32 v13, v13, v61\n"
"v_add_f32 v30, v30, v46\n"
"v_add_f32 v14, v14, v62\n"
"v_add_f32 v31, v31, v47\n"
"v_add_f32 v15, v15, v63\n"
"v_add_f32 v8, v8, v24\n"
"v_add_f32 v9, v9, v25\n"
"v_add_f32 v10, v10, v26\n"
"v_add_f32 v11, v11, v27\n"
"v_add_f32 v12, v12, v28\n"
"v_add_f32 v13, v13, v29\n"
"v_add_f32 v14, v14, v30\n"
"v_add_f32 v15, v15, v31\n"
"v_mul_f32 v24, 0xbfb8aa3b, v8\n"
"v_mul_f32 v25, 0xbfb8aa3b, v9\n"
"v_mul_f32 v26, 0xbfb8aa3b, v10\n"
"v_mul_f32 v27, 0xbfb8aa3b, v11\n"
"v_mul_f32 v28, 0xbfb8aa3b, v12\n"
"v_mul_f32 v29, 0xbfb8aa3b, v13\n"
"v_mul_f32 v30, 0xbfb8aa3b, v14\n"
"v_mul_f32 v31, 0xbfb8aa3b, v15\n"
"v_exp_f32 v24, v24\n"
"v_exp_f32 v25, v25\n"
"v_exp_f32 v26, v26\n"
"v_exp_f32 v27, v27\n"
"v_exp_f32 v28, v28\n"
"v_exp_f32 v29, v29\n"
"v_exp_f32 v30, v30\n"
"v_exp_f32 v31, v31\n"
"v_add_f32 v24, 1.0, v24\n"
"v_add_f32 v25, 1.0, v25\n"
"v_add_f32 v26, 1.0, v26\n"
"v_add_f32 v27, 1.0, v27\n"
"v_add_f32 v28, 1.0, v28\n"
"v_add_f32 v29, 1.0, v29\n"
"v_add_f32 v30, 1.0, v30\n"
"v_add_f32 v31, 1.0, v31\n"
"v_rcp_f32 v24, v24\n"
"v_rcp_f32 v25, v25\n"
"v_rcp_f32 v26, v26\n"
"v_rcp_f32 v27, v27\n"
"v_rcp_f32 v28, v28\n"
"v_rcp_f32 v29, v29\n"
"v_rcp_f32 v30, v30\n"
"v_rcp_f32 v31, v31\n"
"v_mul_f32 v8, v8, v24\n"
"v_mul_f32 v9, v9, v25\n"
"v_mul_f32 v10, v10, v26\n"
"v_mul_f32 v11, v11, v27\n"
"v_mul_f32 v12, v12, v28\n"
"v_mul_f32 v13, v13, v29\n"
"v_mul_f32 v14, v14, v30\n"
"v_mul_f32 v15, v15, v31\n"
"ds_write_b128 v250, v[8:11] offset:64\n"
"ds_write_b128 v250, v[12:15] offset:96\n"
"s_mov_b64 exec, 1\n"
"ds_add_u32 v243, v244 offset:32\n"
"s_mov_b64 exec, -1\n"
"s_endpgm\n"
"LA2_%=:\n"
"s_lshl_b32 s65, s65, 2\n"
"s_lshr_b32 s65, 0x54763210, s65\n"
"s_and_b32 s65, s65, 7\n"
"s_and_b32 s87, s65, 1\n"
"s_and_b32 s84, s66, 7\n"
"s_lshl3_add_u32 s67, s87, s84\n"
"s_and_b32 s70, s69, 3\n"
"s_lshr_b32 s85, s65, 1\n"
"s_lshr_b32 s86, s66, 3\n"
"s_xor_b32 s86, s86, 3\n"
"s_lshl2_add_u32 s68, s85, s86\n"
"s_lshl_b32 s80, s68, 7\n"
"s_mul_i32 s87, s85, 384\n"
"s_add_u32 s90, s87, s80\n"
"s_add_u32 s87, s90, 384\n"
"s_lshl_b32 s88, s85, 8\n"
"s_addk_i32 s88, 1792\n"
"s_add_u32 s89, s88, 128\n"
"s_cmp_eq_u32 s85, 3\n"
"s_cselect_b32 s89, 1073741824, s89\n"
"s_movk_i32 s84, 3584\n"
"s_cmp_eq_u32 s86, 3\n"
"s_cselect_b32 s76, 1, 0\n"
"s_cselect_b32 s77, 2688, s84\n"
"s_cselect_b32 s78, s88, s87\n"
"s_cselect_b32 s79, s89, s87\n"
"s_cselect_b32 s3, s87, s90\n"
"v_mbcnt_lo_u32_b32 v224, -1, 0\n"
"v_mbcnt_hi_u32_b32 v224, -1, v224\n"
"s_getpc_b64 s[60:61]\n"
"LPC51_%=:\n"
"s_sub_u32 s60, s60, LPC51_%=-LS1_%=\n"
"s_subb_u32 s61, s61, 0\n"
"s_mov_b32 s62, LEND50_%=-LS1_%=\n"
"s_mov_b32 s63, 0x20000\n"
"s_lshl2_add_u32 s87, s66, s70\n"
"s_lshl_b32 s87, s87, 7\n"
"v_lshl_add_u32 v9, v224, 14, s87\n"
"s_mov_b64 exec, 15\n"
"buffer_load_dword v254, v9, s[60:63], 0 offen\n"
"s_mov_b64 exec, -1\n"
"v_mov_b32 v251, 162048\n"
"v_mov_b32 v252, 1\n"
"v_lshlrev_b32 v253, 4, v224\n"
"s_lshr_b32 s8, s70, 1\n"
"s_and_b32 s9, s70, 1\n"
"v_lshrrev_b32 v232, 5, v224\n"
"v_bfe_u32 v233, v224, 4, 1\n"
"v_bfe_u32 v234, v224, 2, 2\n"
"v_and_b32 v235, 3, v224\n"
"v_and_b32 v236, 2, v234\n"
"v_or_b32 v236, v236, v233\n"
"s_waitcnt lgkmcnt(0)\n"
"s_load_dword s71, s[48:49], 0\n"
"s_and_b32 s86, s64, 7\n"
"s_and_b32 s84, s66, 1\n"
"s_lshl1_add_u32 s72, s86, s84\n"
"s_lshr_b32 s73, s66, 1\n"
"s_bitcmp1_b32 s64, 0\n"
"s_cselect_b32 s75, 28, 0\n"
"s_cselect_b32 s83, 28, 28\n"
"s_add_u32 s75, s75, s66\n"
"s_mul_i32 s84, s75, 37\n"
"s_lshr_b32 s84, s84, 8\n"
"s_mul_i32 s85, s84, 7\n"
"s_sub_u32 s75, s75, s85\n"
"s_lshr_b32 s86, s86, 1\n"
"s_lshl3_add_u32 s74, s86, s84\n"
"v_and_b32 v225, 15, v224\n"
"v_lshrrev_b32 v226, 4, v224\n"
"s_lshl_b32 s84, s8, 6\n"
"v_add_u32 v240, s84, v225\n"
"v_lshlrev_b32 v165, 2, v240\n"
"v_add_u32 v165, 159744, v165\n"
"v_lshrrev_b32 v152, 1, v225\n"
"v_xor_b32 v152, v152, v226\n"
"v_lshlrev_b32 v152, 4, v152\n"
"v_lshl_or_b32 v152, v240, 7, v152\n"
"v_xor_b32 v153, 64, v152\n"
"v_and_b32 v240, 2, v234\n"
"v_lshlrev_b32 v240, 5, v240\n"
"v_lshl_or_b32 v240, v234, 7, v240\n"
"s_lshl_b32 s15, s9, 6\n"
"v_xor_b32 v154, s15, v240\n"
"v_lshl_or_b32 v154, v235, 4, v154\n"
"v_lshl_or_b32 v154, v226, 10, v154\n"
"v_add_u32 v154, 49152, v154\n"
"v_and_b32 v241, 1, v224\n"
"v_lshl_or_b32 v240, v241, 3, v240\n"
"v_lshl_or_b32 v240, v226, 11, v240\n"
"s_lshl_b32 s84, s9, 7\n"
"s_add_u32 s84, s84, 160768\n"
"v_lshl_add_u32 v166, v226, 5, s84\n"
"s_lshl1_add_u32 s10, s67, s8\n"
"s_lshl_b32 s12, s10, 13\n"
"s_movk_i32 s11, 4096\n"
"s_mov_b32 s87, 1048576\n"
"s_sub_u32 s86, s68, 7\n"
"s_cmp_lt_u32 s68, 7\n"
"s_cselect_b32 s85, 5242880, s87\n"
"s_cselect_b32 s86, s68, s86\n"
"s_mov_b32 s87, 0x3e8b97f6\n"
"s_cselect_b32 s13, s87, 1.0\n"
"s_cmp_lt_u32 s68, 14\n"
"s_cselect_b32 s85, s85, 1073741824\n"
"s_cselect_b32 s86, s86, 0\n"
"s_add_u32 s85, s85, s12\n"
"s_lshl1_add_u32 s86, s86, s9\n"
"s_lshl_b32 s86, s86, 2\n"
"v_lshl_add_u32 v241, v225, 4, s85\n"
"v_add_u32 v243, s86, v226\n"
"v_mul_u32_u24 v244, 37, v243\n"
"v_lshrrev_b32 v244, 8, v244\n"
"v_mul_u32_u24 v245, 7, v244\n"
"v_sub_u32 v245, v243, v245\n"
"v_cmp_eq_u32 s[4:5], 3, v245\n"
"v_lshl_add_u32 v159, v244, 19, v241\n"
"v_lshrrev_b32 v243, 2, v245\n"
"v_lshl_add_u32 v159, v243, 18, v159\n"
"v_and_b32 v245, 3, v245\n"
"v_lshl_add_u32 v159, v245, 9, v159\n"
"v_add_u32 v160, 262144, v159\n"
"s_lshl4_add_u32 s14, s67, s68\n"
"s_lshl_b32 s14, s14, 7\n"
"s_add_u32 s14, s14, 4096\n"
"s_lshr_b32 s84, s72, 1\n"
"s_mul_i32 s85, s72, 3\n"
"s_add_u32 s84, s84, s85\n"
"s_add_u32 s85, s84, 3\n"
"s_lshr_b32 s84, s84, 3\n"
"s_lshr_b32 s85, s85, 3\n"
"s_sub_u32 s85, s85, s84\n"
"v_and_b32 v241, s85, v224\n"
"v_add_u32 v241, s84, v241\n"
"s_lshl_b32 s84, s70, 6\n"
"v_lshrrev_b32 v242, 1, v224\n"
"v_lshl_add_u32 v242, v242, 4, s84\n"
"v_add3_u32 v227, v241, v242, 7\n"
"v_lshrrev_b32 v228, 1, v241\n"
"v_lshl_add_u32 v228, v228, 2, v242\n"
"v_add_u32 v228, 3555, v228\n"
"s_lshl_b32 s85, s73, 4\n"
"s_add_u32 s84, s84, s72\n"
"s_mov_b64 exec, 0xf00\n"
"v_add_u32 v227, s85, v241\n"
"v_lshl_add_u32 v228, v235, 4, s84\n"
"s_mov_b64 exec, -1\n"
"s_mov_b32 s84, 4096\n"
"v_lshl_add_u32 v227, v227, 7, s84\n"
"v_lshl_add_u32 v228, v228, 7, s84\n"
"s_lshl_b32 s16, s72, 18\n"
"s_lshl_b32 s84, s70, 16\n"
"s_add_u32 s84, s84, s16\n"
"s_add_u32 s84, s84, 1048576\n"
"v_add_u32 v249, s84, v253\n"
"s_lshl2_add_u32 s17, s73, s70\n"
"s_lshl_b32 s19, s17, 12\n"
"s_add_u32 s19, s19, s16\n"
"s_add_u32 s19, s19, 5242880\n"
"s_and_b32 s18, s73, 3\n"
"s_lshl_b32 s18, s18, 14\n"
"s_lshl2_add_u32 s84, s74, s70\n"
"s_mul_i32 s84, s84, 14336\n"
"s_lshl_b32 s85, s75, 7\n"
"s_add_u32 s84, s84, s85\n"
"v_mul_u32_u24 v248, 896, v225\n"
"v_lshl_add_u32 v248, v233, 5, v248\n"
"v_lshl_add_u32 v248, v232, 4, v248\n"
"v_add_u32 v248, s84, v248\n"
"s_xor_b32 s84, s70, 3\n"
"s_lshl_b32 s84, s84, 11\n"
"s_add_u32 s84, s84, 16384\n"
"v_add_u32 v229, s84, v253\n"
"s_xor_b32 s84, s70, 2\n"
"s_lshl_b32 s84, s84, 11\n"
"s_add_u32 s84, s84, 16384\n"
"v_add_u32 v230, s84, v253\n"
"s_xor_b32 s84, s70, 1\n"
"s_lshl_b32 s84, s84, 11\n"
"s_add_u32 s84, s84, 16384\n"
"v_add_u32 v237, s84, v253\n"
"s_xor_b32 s84, s70, 0\n"
"s_lshl_b32 s84, s84, 11\n"
"s_add_u32 s84, s84, 16384\n"
"v_add_u32 v238, s84, v253\n"
"s_xor_b32 s84, s70, 3\n"
"s_mul_i32 s84, s84, 12288\n"
"s_add_u32 s84, s84, 0\n"
"v_add_u32 v245, s84, v253\n"
"s_xor_b32 s84, s70, 2\n"
"s_mul_i32 s84, s84, 12288\n"
"s_add_u32 s84, s84, 4096\n"
"v_add_u32 v246, s84, v253\n"
"s_xor_b32 s84, s70, 1\n"
"s_mul_i32 s84, s84, 12288\n"
"s_add_u32 s84, s84, 8192\n"
"v_add_u32 v247, s84, v253\n"
"s_mul_i32 s84, s70, 12288\n"
"v_add_u32 v250, s84, v253\n"
"s_lshl4_add_u32 s17, s17, s72\n"
"s_lshl_b32 s17, s17, 11\n"
"s_add_u32 s17, s17, 16777216\n"
"s_cmp_eq_u32 s76, 1\n"
"s_cbranch_scc1 LY52_%=\n"
"v_xor_b32 v240, s15, v240\n"
"v_and_b32 v241, 2, v224\n"
"v_lshl_or_b32 v240, v241, 4, v240\n"
"v_lshl_or_b32 v240, v233, 4, v240\n"
"v_add_u32 v155, 73728, v240\n"
"v_xor_b32 v156, 16, v155\n"
"s_lshl_b32 s84, s70, 12\n"
"v_lshl_add_u32 v231, v226, 5, v225\n"
"v_lshl_add_u32 v231, v231, 4, s84\n"
"s_lshl_b32 s84, s9, 7\n"
"s_add_u32 s84, s84, 161024\n"
"v_lshl_add_u32 v167, v232, 6, s84\n"
"v_lshl_add_u32 v167, v233, 4, v167\n"
"s_lshr_b32 s84, s68, 2\n"
"s_sub_u32 s84, s68, s84\n"
"s_mul_i32 s85, s67, 12\n"
"s_add_u32 s84, s84, s85\n"
"s_lshl2_add_u32 s84, s84, s70\n"
"s_mul_i32 s85, s84, 2176\n"
"s_mul_i32 s84, s84, 640\n"
"v_lshl_add_u32 v210, v224, 7, s85\n"
"v_lshl_add_u32 v211, v224, 7, s84\n"
"s_waitcnt lgkmcnt(0)\n"
"s_mul_i32 s71, s71, 1664525\n"
"s_add_u32 s71, s71, 1013904223\n"
"s_setprio 2\n"
"s_barrier\n"
"ds_read_b64_tr_b16 v[96:97], v154 offset:0\n"
"ds_read_b64_tr_b16 v[98:99], v154 offset:512\n"
"ds_read_b128 v[120:123], v152 offset:0\n"
"ds_read_b128 v[124:127], v152 offset:2048\n"
"ds_read_b128 v[128:131], v152 offset:4096\n"
"ds_read_b128 v[132:135], v152 offset:6144\n"
"ds_read_b64_tr_b16 v[100:101], v154 offset:8\n"
"ds_read_b64_tr_b16 v[102:103], v154 offset:520\n"
"ds_read_b64_tr_b16 v[104:105], v155 offset:0\n"
"ds_read_b64_tr_b16 v[106:107], v155 offset:512\n"
"ds_read_b64_tr_b16 v[108:109], v156 offset:0\n"
"ds_read_b64_tr_b16 v[110:111], v156 offset:512\n"
"ds_read_b64_tr_b16 v[112:113], v155 offset:1024\n"
"ds_read_b64_tr_b16 v[114:115], v155 offset:1536\n"
"s_waitcnt lgkmcnt(11)\n"
"v_mfma_f32_16x16x32_bf16 v[0:3], v[96:99], v[120:123], 0\n"
"ds_read_b64_tr_b16 v[116:117], v156 offset:1024\n"
"s_waitcnt lgkmcnt(11)\n"
"v_mfma_f32_16x16x32_bf16 v[24:27], v[96:99], v[124:127], 0\n"
"ds_read_b64_tr_b16 v[118:119], v156 offset:1536\n"
"s_waitcnt lgkmcnt(11)\n"
"v_mfma_f32_16x16x32_bf16 v[48:51], v[96:99], v[128:131], 0\n"
"s_waitcnt lgkmcnt(10)\n"
"v_mfma_f32_16x16x32_bf16 v[72:75], v[96:99], v[132:135], 0\n"
"s_waitcnt lgkmcnt(8)\n"
"v_mfma_f32_16x16x32_bf16 v[4:7], v[100:103], v[120:123], 0\n"
"ds_read_b64_tr_b16 v[96:97], v154 offset:4096\n"
"v_mfma_f32_16x16x32_bf16 v[28:31], v[100:103], v[124:127], 0\n"
"ds_read_b64_tr_b16 v[98:99], v154 offset:4608\n"
"v_mfma_f32_16x16x32_bf16 v[52:55], v[100:103], v[128:131], 0\n"
"ds_read_b128 v[136:139], v153 offset:0\n"
"v_mfma_f32_16x16x32_bf16 v[76:79], v[100:103], v[132:135], 0\n"
"s_waitcnt lgkmcnt(9)\n"
"v_mfma_f32_16x16x32_bf16 v[8:11], v[104:107], v[120:123], 0\n"
"ds_read_b64_tr_b16 v[100:101], v154 offset:4104\n"
"v_mfma_f32_16x16x32_bf16 v[32:35], v[104:107], v[124:127], 0\n"
"ds_read_b64_tr_b16 v[102:103], v154 offset:4616\n"
"v_mfma_f32_16x16x32_bf16 v[56:59], v[104:107], v[128:131], 0\n"
"ds_read_b128 v[140:143], v153 offset:2048\n"
"v_mfma_f32_16x16x32_bf16 v[80:83], v[104:107], v[132:135], 0\n"
"s_waitcnt lgkmcnt(10)\n"
"v_mfma_f32_16x16x32_bf16 v[12:15], v[108:111], v[120:123], 0\n"
"ds_read_b64_tr_b16 v[104:105], v155 offset:8192\n"
"v_mfma_f32_16x16x32_bf16 v[36:39], v[108:111], v[124:127], 0\n"
"ds_read_b64_tr_b16 v[106:107], v155 offset:8704\n"
"v_mfma_f32_16x16x32_bf16 v[60:63], v[108:111], v[128:131], 0\n"
"ds_read_b128 v[144:147], v153 offset:4096\n"
"v_mfma_f32_16x16x32_bf16 v[84:87], v[108:111], v[132:135], 0\n"
"s_waitcnt lgkmcnt(11)\n"
"v_mfma_f32_16x16x32_bf16 v[16:19], v[112:115], v[120:123], 0\n"
"ds_read_b64_tr_b16 v[108:109], v156 offset:8192\n"
"v_mfma_f32_16x16x32_bf16 v[40:43], v[112:115], v[124:127], 0\n"
"ds_read_b64_tr_b16 v[110:111], v156 offset:8704\n"
"v_mfma_f32_16x16x32_bf16 v[64:67], v[112:115], v[128:131], 0\n"
"ds_read_b128 v[148:151], v153 offset:6144\n"
"v_mfma_f32_16x16x32_bf16 v[88:91], v[112:115], v[132:135], 0\n"
"s_waitcnt lgkmcnt(12)\n"
"v_mfma_f32_16x16x32_bf16 v[20:23], v[116:119], v[120:123], 0\n"
"ds_read_b64_tr_b16 v[112:113], v155 offset:9216\n"
"v_mfma_f32_16x16x32_bf16 v[44:47], v[116:119], v[124:127], 0\n"
"ds_read_b64_tr_b16 v[114:115], v155 offset:9728\n"
"v_mfma_f32_16x16x32_bf16 v[68:71], v[116:119], v[128:131], 0\n"
"v_mfma_f32_16x16x32_bf16 v[92:95], v[116:119], v[132:135], 0\n"
"s_waitcnt lgkmcnt(2)\n"
"v_mfma_f32_16x16x32_bf16 v[0:3], v[96:99], v[136:139], v[0:3]\n"
"ds_read_b64_tr_b16 v[116:117], v156 offset:9216\n"
"v_mfma_f32_16x16x32_bf16 v[24:27], v[96:99], v[140:143], v[24:27]\n"
"ds_read_b64_tr_b16 v[118:119], v156 offset:9728\n"
"v_mfma_f32_16x16x32_bf16 v[48:51], v[96:99], v[144:147], v[48:51]\n"
"v_mfma_f32_16x16x32_bf16 v[72:75], v[96:99], v[148:151], v[72:75]\n"
"v_mfma_f32_16x16x32_bf16 v[4:7], v[100:103], v[136:139], v[4:7]\n"
"v_mfma_f32_16x16x32_bf16 v[28:31], v[100:103], v[140:143], v[28:31]\n"
"v_mfma_f32_16x16x32_bf16 v[52:55], v[100:103], v[144:147], v[52:55]\n"
"v_mfma_f32_16x16x32_bf16 v[76:79], v[100:103], v[148:151], v[76:79]\n"
"s_waitcnt lgkmcnt(0)\n"
"s_barrier\n"
"ds_read_b64_tr_b16 v[96:97], v154 offset:8192\n"
"v_mfma_f32_16x16x32_bf16 v[8:11], v[104:107], v[136:139], v[8:11]\n"
"ds_read_b64_tr_b16 v[98:99], v154 offset:8704\n"
"v_mfma_f32_16x16x32_bf16 v[32:35], v[104:107], v[140:143], v[32:35]\n"
"ds_read_b128 v[120:123], v152 offset:16384\n"
"v_mfma_f32_16x16x32_bf16 v[56:59], v[104:107], v[144:147], v[56:59]\n"
"ds_read_b64_tr_b16 v[100:101], v154 offset:8200\n"
"v_mfma_f32_16x16x32_bf16 v[80:83], v[104:107], v[148:151], v[80:83]\n"
"ds_read_b64_tr_b16 v[102:103], v154 offset:8712\n"
"v_mfma_f32_16x16x32_bf16 v[12:15], v[108:111], v[136:139], v[12:15]\n"
"ds_read_b128 v[124:127], v152 offset:18432\n"
"v_mfma_f32_16x16x32_bf16 v[36:39], v[108:111], v[140:143], v[36:39]\n"
"ds_read_b64_tr_b16 v[104:105], v155 offset:16384\n"
"v_mfma_f32_16x16x32_bf16 v[60:63], v[108:111], v[144:147], v[60:63]\n"
"ds_read_b64_tr_b16 v[106:107], v155 offset:16896\n"
"v_mfma_f32_16x16x32_bf16 v[84:87], v[108:111], v[148:151], v[84:87]\n"
"ds_read_b128 v[128:131], v152 offset:20480\n"
"v_mfma_f32_16x16x32_bf16 v[16:19], v[112:115], v[136:139], v[16:19]\n"
"ds_read_b64_tr_b16 v[108:109], v156 offset:16384\n"
"v_mfma_f32_16x16x32_bf16 v[40:43], v[112:115], v[140:143], v[40:43]\n"
"ds_read_b64_tr_b16 v[110:111], v156 offset:16896\n"
"v_mfma_f32_16x16x32_bf16 v[64:67], v[112:115], v[144:147], v[64:67]\n"
"ds_read_b128 v[132:135], v152 offset:22528\n"
"v_mfma_f32_16x16x32_bf16 v[88:91], v[112:115], v[148:151], v[88:91]\n"
"v_mfma_f32_16x16x32_bf16 v[20:23], v[116:119], v[136:139], v[20:23]\n"
"ds_read_b64_tr_b16 v[112:113], v155 offset:17408\n"
"v_mfma_f32_16x16x32_bf16 v[44:47], v[116:119], v[140:143], v[44:47]\n"
"ds_read_b64_tr_b16 v[114:115], v155 offset:17920\n"
"v_mfma_f32_16x16x32_bf16 v[68:71], v[116:119], v[144:147], v[68:71]\n"
"v_mfma_f32_16x16x32_bf16 v[92:95], v[116:119], v[148:151], v[92:95]\n"
"s_waitcnt lgkmcnt(2)\n"
"v_mfma_f32_16x16x32_bf16 v[0:3], v[96:99], v[120:123], v[0:3]\n"
"ds_read_b64_tr_b16 v[116:117], v156 offset:17408\n"
"v_mfma_f32_16x16x32_bf16 v[24:27], v[96:99], v[124:127], v[24:27]\n"
"ds_read_b64_tr_b16 v[118:119], v156 offset:17920\n"
"v_mfma_f32_16x16x32_bf16 v[48:51], v[96:99], v[128:131], v[48:51]\n"
"v_mfma_f32_16x16x32_bf16 v[72:75], v[96:99], v[132:135], v[72:75]\n"
"v_mfma_f32_16x16x32_bf16 v[4:7], v[100:103], v[120:123], v[4:7]\n"
"ds_read_b64_tr_b16 v[96:97], v154 offset:12288\n"
"v_mfma_f32_16x16x32_bf16 v[28:31], v[100:103], v[124:127], v[28:31]\n"
"ds_read_b64_tr_b16 v[98:99], v154 offset:12800\n"
"v_mfma_f32_16x16x32_bf16 v[52:55], v[100:103], v[128:131], v[52:55]\n"
"ds_read_b128 v[136:139], v153 offset:16384\n"
"v_mfma_f32_16x16x32_bf16 v[76:79], v[100:103], v[132:135], v[76:79]\n"
"v_mfma_f32_16x16x32_bf16 v[8:11], v[104:107], v[120:123], v[8:11]\n"
"ds_read_b64_tr_b16 v[100:101], v154 offset:12296\n"
"v_mfma_f32_16x16x32_bf16 v[32:35], v[104:107], v[124:127], v[32:35]\n"
"ds_read_b64_tr_b16 v[102:103], v154 offset:12808\n"
"v_mfma_f32_16x16x32_bf16 v[56:59], v[104:107], v[128:131], v[56:59]\n"
"ds_read_b128 v[140:143], v153 offset:18432\n"
"v_mfma_f32_16x16x32_bf16 v[80:83], v[104:107], v[132:135], v[80:83]\n"
"v_mfma_f32_16x16x32_bf16 v[12:15], v[108:111], v[120:123], v[12:15]\n"
"ds_read_b64_tr_b16 v[104:105], v155 offset:24576\n"
"v_mfma_f32_16x16x32_bf16 v[36:39], v[108:111], v[124:127], v[36:39]\n"
"ds_read_b64_tr_b16 v[106:107], v155 offset:25088\n"
"v_mfma_f32_16x16x32_bf16 v[60:63], v[108:111], v[128:131], v[60:63]\n"
"ds_read_b128 v[144:147], v153 offset:20480\n"
"v_mfma_f32_16x16x32_bf16 v[84:87], v[108:111], v[132:135], v[84:87]\n"
"s_waitcnt lgkmcnt(11)\n"
"v_mfma_f32_16x16x32_bf16 v[16:19], v[112:115], v[120:123], v[16:19]\n"
"ds_read_b64_tr_b16 v[108:109], v156 offset:24576\n"
"v_mfma_f32_16x16x32_bf16 v[40:43], v[112:115], v[124:127], v[40:43]\n"
"ds_read_b64_tr_b16 v[110:111], v156 offset:25088\n"
"v_mfma_f32_16x16x32_bf16 v[64:67], v[112:115], v[128:131], v[64:67]\n"
"ds_read_b128 v[148:151], v153 offset:22528\n"
"v_mfma_f32_16x16x32_bf16 v[88:91], v[112:115], v[132:135], v[88:91]\n"
"s_waitcnt lgkmcnt(12)\n"
"v_mfma_f32_16x16x32_bf16 v[20:23], v[116:119], v[120:123], v[20:23]\n"
"ds_read_b64_tr_b16 v[112:113], v155 offset:25600\n"
"v_mfma_f32_16x16x32_bf16 v[44:47], v[116:119], v[124:127], v[44:47]\n"
"ds_read_b64_tr_b16 v[114:115], v155 offset:26112\n"
"v_mfma_f32_16x16x32_bf16 v[68:71], v[116:119], v[128:131], v[68:71]\n"
"v_mfma_f32_16x16x32_bf16 v[92:95], v[116:119], v[132:135], v[92:95]\n"
"s_waitcnt lgkmcnt(2)\n"
"v_mfma_f32_16x16x32_bf16 v[0:3], v[96:99], v[136:139], v[0:3]\n"
"ds_read_b64_tr_b16 v[116:117], v156 offset:25600\n"
"v_mfma_f32_16x16x32_bf16 v[24:27], v[96:99], v[140:143], v[24:27]\n"
"ds_read_b64_tr_b16 v[118:119], v156 offset:26112\n"
"v_mfma_f32_16x16x32_bf16 v[48:51], v[96:99], v[144:147], v[48:51]\n"
"v_mfma_f32_16x16x32_bf16 v[72:75], v[96:99], v[148:151], v[72:75]\n"
"v_mfma_f32_16x16x32_bf16 v[4:7], v[100:103], v[136:139], v[4:7]\n"
"v_mfma_f32_16x16x32_bf16 v[28:31], v[100:103], v[140:143], v[28:31]\n"
"v_mfma_f32_16x16x32_bf16 v[52:55], v[100:103], v[144:147], v[52:55]\n"
"v_mfma_f32_16x16x32_bf16 v[76:79], v[100:103], v[148:151], v[76:79]\n"
"s_waitcnt lgkmcnt(0)\n"
"s_barrier\n"
"ds_read_b64_tr_b16 v[96:97], v154 offset:16384\n"
"v_mfma_f32_16x16x32_bf16 v[8:11], v[104:107], v[136:139], v[8:11]\n"
"ds_read_b64_tr_b16 v[98:99], v154 offset:16896\n"
"v_mfma_f32_16x16x32_bf16 v[32:35], v[104:107], v[140:143], v[32:35]\n"
"ds_read_b128 v[120:123], v152 offset:32768\n"
"v_mfma_f32_16x16x32_bf16 v[56:59], v[104:107], v[144:147], v[56:59]\n"
"ds_read_b64_tr_b16 v[100:101], v154 offset:16392\n"
"v_mfma_f32_16x16x32_bf16 v[80:83], v[104:107], v[148:151], v[80:83]\n"
"ds_read_b64_tr_b16 v[102:103], v154 offset:16904\n"
"v_mfma_f32_16x16x32_bf16 v[12:15], v[108:111], v[136:139], v[12:15]\n"
"ds_read_b128 v[124:127], v152 offset:34816\n"
"v_mfma_f32_16x16x32_bf16 v[36:39], v[108:111], v[140:143], v[36:39]\n"
"ds_read_b64_tr_b16 v[104:105], v155 offset:32768\n"
"v_mfma_f32_16x16x32_bf16 v[60:63], v[108:111], v[144:147], v[60:63]\n"
"ds_read_b64_tr_b16 v[106:107], v155 offset:33280\n"
"v_mfma_f32_16x16x32_bf16 v[84:87], v[108:111], v[148:151], v[84:87]\n"
"ds_read_b128 v[128:131], v152 offset:36864\n"
"v_mfma_f32_16x16x32_bf16 v[16:19], v[112:115], v[136:139], v[16:19]\n"
"ds_read_b64_tr_b16 v[108:109], v156 offset:32768\n"
"v_mfma_f32_16x16x32_bf16 v[40:43], v[112:115], v[140:143], v[40:43]\n"
"ds_read_b64_tr_b16 v[110:111], v156 offset:33280\n"
"v_mfma_f32_16x16x32_bf16 v[64:67], v[112:115], v[144:147], v[64:67]\n"
"ds_read_b128 v[132:135], v152 offset:38912\n"
"v_mfma_f32_16x16x32_bf16 v[88:91], v[112:115], v[148:151], v[88:91]\n"
"v_mfma_f32_16x16x32_bf16 v[20:23], v[116:119], v[136:139], v[20:23]\n"
"ds_read_b64_tr_b16 v[112:113], v155 offset:33792\n"
"v_mfma_f32_16x16x32_bf16 v[44:47], v[116:119], v[140:143], v[44:47]\n"
"ds_read_b64_tr_b16 v[114:115], v155 offset:34304\n"
"v_mfma_f32_16x16x32_bf16 v[68:71], v[116:119], v[144:147], v[68:71]\n"
"v_mfma_f32_16x16x32_bf16 v[92:95], v[116:119], v[148:151], v[92:95]\n"
"s_waitcnt lgkmcnt(2)\n"
"v_mfma_f32_16x16x32_bf16 v[0:3], v[96:99], v[120:123], v[0:3]\n"
"ds_read_b64_tr_b16 v[116:117], v156 offset:33792\n"
"v_mfma_f32_16x16x32_bf16 v[24:27], v[96:99], v[124:127], v[24:27]\n"
"ds_read_b64_tr_b16 v[118:119], v156 offset:34304\n"
"v_mfma_f32_16x16x32_bf16 v[48:51], v[96:99], v[128:131], v[48:51]\n"
"v_mfma_f32_16x16x32_bf16 v[72:75], v[96:99], v[132:135], v[72:75]\n"
"v_mfma_f32_16x16x32_bf16 v[4:7], v[100:103], v[120:123], v[4:7]\n"
"ds_read_b64_tr_b16 v[96:97], v154 offset:20480\n"
"v_mfma_f32_16x16x32_bf16 v[28:31], v[100:103], v[124:127], v[28:31]\n"
"ds_read_b64_tr_b16 v[98:99], v154 offset:20992\n"
"v_mfma_f32_16x16x32_bf16 v[52:55], v[100:103], v[128:131], v[52:55]\n"
"ds_read_b128 v[136:139], v153 offset:32768\n"
"v_mfma_f32_16x16x32_bf16 v[76:79], v[100:103], v[132:135], v[76:79]\n"
"v_mfma_f32_16x16x32_bf16 v[8:11], v[104:107], v[120:123], v[8:11]\n"
"ds_read_b64_tr_b16 v[100:101], v154 offset:20488\n"
"v_mfma_f32_16x16x32_bf16 v[32:35], v[104:107], v[124:127], v[32:35]\n"
"ds_read_b64_tr_b16 v[102:103], v154 offset:21000\n"
"v_mfma_f32_16x16x32_bf16 v[56:59], v[104:107], v[128:131], v[56:59]\n"
"ds_read_b128 v[140:143], v153 offset:34816\n"
"v_mfma_f32_16x16x32_bf16 v[80:83], v[104:107], v[132:135], v[80:83]\n"
"v_mfma_f32_16x16x32_bf16 v[12:15], v[108:111], v[120:123], v[12:15]\n"
"ds_read_b64_tr_b16 v[104:105], v155 offset:40960\n"
"v_mfma_f32_16x16x32_bf16 v[36:39], v[108:111], v[124:127], v[36:39]\n"
"ds_read_b64_tr_b16 v[106:107], v155 offset:41472\n"
"v_mfma_f32_16x16x32_bf16 v[60:63], v[108:111], v[128:131], v[60:63]\n"
"ds_read_b128 v[144:147], v153 offset:36864\n"
"v_mfma_f32_16x16x32_bf16 v[84:87], v[108:111], v[132:135], v[84:87]\n"
"s_waitcnt lgkmcnt(11)\n"
"v_mfma_f32_16x16x32_bf16 v[16:19], v[112:115], v[120:123], v[16:19]\n"
"ds_read_b64_tr_b16 v[108:109], v156 offset:40960\n"
"v_mfma_f32_16x16x32_bf16 v[40:43], v[112:115], v[124:127], v[40:43]\n"
"ds_read_b64_tr_b16 v[110:111], v156 offset:41472\n"
"v_mfma_f32_16x16x32_bf16 v[64:67], v[112:115], v[128:131], v[64:67]\n"
"ds_read_b128 v[148:151], v153 offset:38912\n"
"v_mfma_f32_16x16x32_bf16 v[88:91], v[112:115], v[132:135], v[88:91]\n"
"s_waitcnt lgkmcnt(12)\n"
"v_mfma_f32_16x16x32_bf16 v[20:23], v[116:119], v[120:123], v[20:23]\n"
"ds_read_b64_tr_b16 v[112:113], v155 offset:41984\n"
"v_mfma_f32_16x16x32_bf16 v[44:47], v[116:119], v[124:127], v[44:47]\n"
"ds_read_b64_tr_b16 v[114:115], v155 offset:42496\n"
"v_mfma_f32_16x16x32_bf16 v[68:71], v[116:119], v[128:131], v[68:71]\n"
"v_mfma_f32_16x16x32_bf16 v[92:95], v[116:119], v[132:135], v[92:95]\n"
"s_waitcnt lgkmcnt(2)\n"
"v_mfma_f32_16x16x32_bf16 v[0:3], v[96:99], v[136:139], v[0:3]\n"
"ds_read_b64_tr_b16 v[116:117], v156 offset:41984\n"
"v_mfma_f32_16x16x32_bf16 v[24:27], v[96:99], v[140:143], v[24:27]\n"
"ds_read_b64_tr_b16 v[118:119], v156 offset:42496\n"
"v_mfma_f32_16x16x32_bf16 v[48:51], v[96:99], v[144:147], v[48:51]\n"
"v_mfma_f32_16x16x32_bf16 v[72:75], v[96:99], v[148:151], v[72:75]\n"
"v_mfma_f32_16x16x32_bf16 v[4:7], v[100:103], v[136:139], v[4:7]\n"
"v_mfma_f32_16x16x32_bf16 v[28:31], v[100:103], v[140:143], v[28:31]\n"
"v_mfma_f32_16x16x32_bf16 v[52:55], v[100:103], v[144:147], v[52:55]\n"
"v_mfma_f32_16x16x32_bf16 v[76:79], v[100:103], v[148:151], v[76:79]\n"
"s_waitcnt lgkmcnt(0)\n"
"s_barrier\n"
"ds_read_b64_tr_b16 v[96:97], v154 offset:0\n"
"v_mfma_f32_16x16x32_bf16 v[8:11], v[104:107], v[136:139], v[8:11]\n"
"ds_read_b64_tr_b16 v[98:99], v154 offset:512\n"
"v_mfma_f32_16x16x32_bf16 v[32:35], v[104:107], v[140:143], v[32:35]\n"
"ds_read_b128 v[120:123], v152 offset:0\n"
"v_mfma_f32_16x16x32_bf16 v[56:59], v[104:107], v[144:147], v[56:59]\n"
"ds_read_b64_tr_b16 v[100:101], v154 offset:8\n"
"v_mfma_f32_16x16x32_bf16 v[80:83], v[104:107], v[148:151], v[80:83]\n"
"ds_read_b64_tr_b16 v[102:103], v154 offset:520\n"
"v_mfma_f32_16x16x32_bf16 v[12:15], v[108:111], v[136:139], v[12:15]\n"
"ds_read_b128 v[124:127], v152 offset:2048\n"
"v_mfma_f32_16x16x32_bf16 v[36:39], v[108:111], v[140:143], v[36:39]\n"
"ds_read_b64_tr_b16 v[104:105], v155 offset:0\n"
"v_mfma_f32_16x16x32_bf16 v[60:63], v[108:111], v[144:147], v[60:63]\n"
"ds_read_b64_tr_b16 v[106:107], v155 offset:512\n"
"v_mfma_f32_16x16x32_bf16 v[84:87], v[108:111], v[148:151], v[84:87]\n"
"ds_read_b128 v[128:131], v152 offset:4096\n"
"v_mfma_f32_16x16x32_bf16 v[16:19], v[112:115], v[136:139], v[16:19]\n"
"ds_read_b64_tr_b16 v[108:109], v156 offset:0\n"
"v_mfma_f32_16x16x32_bf16 v[40:43], v[112:115], v[140:143], v[40:43]\n"
"ds_read_b64_tr_b16 v[110:111], v156 offset:512\n"
"v_mfma_f32_16x16x32_bf16 v[64:67], v[112:115], v[144:147], v[64:67]\n"
"ds_read_b128 v[132:135], v152 offset:6144\n"
"v_mfma_f32_16x16x32_bf16 v[88:91], v[112:115], v[148:151], v[88:91]\n"
"v_mfma_f32_16x16x32_bf16 v[20:23], v[116:119], v[136:139], v[20:23]\n"
"ds_read_b64_tr_b16 v[112:113], v155 offset:1024\n"
"v_mfma_f32_16x16x32_bf16 v[44:47], v[116:119], v[140:143], v[44:47]\n"
"ds_read_b64_tr_b16 v[114:115], v155 offset:1536\n"
"v_mfma_f32_16x16x32_bf16 v[68:71], v[116:119], v[144:147], v[68:71]\n"
"v_mfma_f32_16x16x32_bf16 v[92:95], v[116:119], v[148:151], v[92:95]\n"
"s_waitcnt lgkmcnt(2)\n"
"v_mfma_f32_16x16x32_bf16 v[0:3], v[96:99], v[120:123], v[0:3]\n"
"ds_read_b64_tr_b16 v[116:117], v156 offset:1024\n"
"v_mfma_f32_16x16x32_bf16 v[24:27], v[96:99], v[124:127], v[24:27]\n"
"ds_read_b64_tr_b16 v[118:119], v156 offset:1536\n"
"v_mfma_f32_16x16x32_bf16 v[48:51], v[96:99], v[128:131], v[48:51]\n"
"v_mfma_f32_16x16x32_bf16 v[72:75], v[96:99], v[132:135], v[72:75]\n"
"v_mfma_f32_16x16x32_bf16 v[4:7], v[100:103], v[120:123], v[4:7]\n"
"ds_read_b64_tr_b16 v[96:97], v154 offset:4096\n"
"v_mfma_f32_16x16x32_bf16 v[28:31], v[100:103], v[124:127], v[28:31]\n"
"ds_read_b64_tr_b16 v[98:99], v154 offset:4608\n"
"v_mfma_f32_16x16x32_bf16 v[52:55], v[100:103], v[128:131], v[52:55]\n"
"ds_read_b128 v[136:139], v153 offset:0\n"
"v_mfma_f32_16x16x32_bf16 v[76:79], v[100:103], v[132:135], v[76:79]\n"
"v_mfma_f32_16x16x32_bf16 v[8:11], v[104:107], v[120:123], v[8:11]\n"
"ds_read_b64_tr_b16 v[100:101], v154 offset:4104\n"
"v_mfma_f32_16x16x32_bf16 v[32:35], v[104:107], v[124:127], v[32:35]\n"
"ds_read_b64_tr_b16 v[102:103], v154 offset:4616\n"
"v_mfma_f32_16x16x32_bf16 v[56:59], v[104:107], v[128:131], v[56:59]\n"
"ds_read_b128 v[140:143], v153 offset:2048\n"
"v_mfma_f32_16x16x32_bf16 v[80:83], v[104:107], v[132:135], v[80:83]\n"
"v_mfma_f32_16x16x32_bf16 v[12:15], v[108:111], v[120:123], v[12:15]\n"
"ds_read_b64_tr_b16 v[104:105], v155 offset:8192\n"
"v_mfma_f32_16x16x32_bf16 v[36:39], v[108:111], v[124:127], v[36:39]\n"
"ds_read_b64_tr_b16 v[106:107], v155 offset:8704\n"
"v_mfma_f32_16x16x32_bf16 v[60:63], v[108:111], v[128:131], v[60:63]\n"
"ds_read_b128 v[144:147], v153 offset:4096\n"
"v_mfma_f32_16x16x32_bf16 v[84:87], v[108:111], v[132:135], v[84:87]\n"
"s_waitcnt lgkmcnt(11)\n"
"v_mfma_f32_16x16x32_bf16 v[16:19], v[112:115], v[120:123], v[16:19]\n"
"ds_read_b64_tr_b16 v[108:109], v156 offset:8192\n"
"v_mfma_f32_16x16x32_bf16 v[40:43], v[112:115], v[124:127], v[40:43]\n"
"ds_read_b64_tr_b16 v[110:111], v156 offset:8704\n"
"v_mfma_f32_16x16x32_bf16 v[64:67], v[112:115], v[128:131], v[64:67]\n"
"ds_read_b128 v[148:151], v153 offset:6144\n"
"v_mfma_f32_16x16x32_bf16 v[88:91], v[112:115], v[132:135], v[88:91]\n"
"s_waitcnt lgkmcnt(12)\n"
"v_mfma_f32_16x16x32_bf16 v[20:23], v[116:119], v[120:123], v[20:23]\n"
"ds_read_b64_tr_b16 v[112:113], v155 offset:9216\n"
"v_mfma_f32_16x16x32_bf16 v[44:47], v[116:119], v[124:127], v[44:47]\n"
"ds_read_b64_tr_b16 v[114:115], v155 offset:9728\n"
"v_mfma_f32_16x16x32_bf16 v[68:71], v[116:119], v[128:131], v[68:71]\n"
"v_mfma_f32_16x16x32_bf16 v[92:95], v[116:119], v[132:135], v[92:95]\n"
"s_waitcnt lgkmcnt(2)\n"
"v_mfma_f32_16x16x32_bf16 v[0:3], v[96:99], v[136:139], v[0:3]\n"
"ds_read_b64_tr_b16 v[116:117], v156 offset:9216\n"
"v_mfma_f32_16x16x32_bf16 v[24:27], v[96:99], v[140:143], v[24:27]\n"
"ds_read_b64_tr_b16 v[118:119], v156 offset:9728\n"
"v_mfma_f32_16x16x32_bf16 v[48:51], v[96:99], v[144:147], v[48:51]\n"
"v_mfma_f32_16x16x32_bf16 v[72:75], v[96:99], v[148:151], v[72:75]\n"
"v_mfma_f32_16x16x32_bf16 v[4:7], v[100:103], v[136:139], v[4:7]\n"
"v_mfma_f32_16x16x32_bf16 v[28:31], v[100:103], v[140:143], v[28:31]\n"
"v_mfma_f32_16x16x32_bf16 v[52:55], v[100:103], v[144:147], v[52:55]\n"
"v_mfma_f32_16x16x32_bf16 v[76:79], v[100:103], v[148:151], v[76:79]\n"
"s_waitcnt lgkmcnt(0)\n"
"s_barrier\n"
"ds_read_b64_tr_b16 v[96:97], v154 offset:8192\n"
"v_mfma_f32_16x16x32_bf16 v[8:11], v[104:107], v[136:139], v[8:11]\n"
"ds_read_b64_tr_b16 v[98:99], v154 offset:8704\n"
"v_mfma_f32_16x16x32_bf16 v[32:35], v[104:107], v[140:143], v[32:35]\n"
"ds_read_b128 v[120:123], v152 offset:16384\n"
"v_mfma_f32_16x16x32_bf16 v[56:59], v[104:107], v[144:147], v[56:59]\n"
"ds_read_b64_tr_b16 v[100:101], v154 offset:8200\n"
"v_mfma_f32_16x16x32_bf16 v[80:83], v[104:107], v[148:151], v[80:83]\n"
"ds_read_b64_tr_b16 v[102:103], v154 offset:8712\n"
"v_mfma_f32_16x16x32_bf16 v[12:15], v[108:111], v[136:139], v[12:15]\n"
"ds_read_b128 v[124:127], v152 offset:18432\n"
"v_mfma_f32_16x16x32_bf16 v[36:39], v[108:111], v[140:143], v[36:39]\n"
"ds_read_b64_tr_b16 v[104:105], v155 offset:16384\n"
"v_mfma_f32_16x16x32_bf16 v[60:63], v[108:111], v[144:147], v[60:63]\n"
"ds_read_b64_tr_b16 v[106:107], v155 offset:16896\n"
"v_mfma_f32_16x16x32_bf16 v[84:87], v[108:111], v[148:151], v[84:87]\n"
"ds_read_b128 v[128:131], v152 offset:20480\n"
"v_mfma_f32_16x16x32_bf16 v[16:19], v[112:115], v[136:139], v[16:19]\n"
"ds_read_b64_tr_b16 v[108:109], v156 offset:16384\n"
"v_mfma_f32_16x16x32_bf16 v[40:43], v[112:115], v[140:143], v[40:43]\n"
"ds_read_b64_tr_b16 v[110:111], v156 offset:16896\n"
"v_mfma_f32_16x16x32_bf16 v[64:67], v[112:115], v[144:147], v[64:67]\n"
"ds_read_b128 v[132:135], v152 offset:22528\n"
"v_mfma_f32_16x16x32_bf16 v[88:91], v[112:115], v[148:151], v[88:91]\n"
"v_mfma_f32_16x16x32_bf16 v[20:23], v[116:119], v[136:139], v[20:23]\n"
"ds_read_b64_tr_b16 v[112:113], v155 offset:17408\n"
"v_mfma_f32_16x16x32_bf16 v[44:47], v[116:119], v[140:143], v[44:47]\n"
"ds_read_b64_tr_b16 v[114:115], v155 offset:17920\n"
"v_mfma_f32_16x16x32_bf16 v[68:71], v[116:119], v[144:147], v[68:71]\n"
"v_mfma_f32_16x16x32_bf16 v[92:95], v[116:119], v[148:151], v[92:95]\n"
"s_waitcnt lgkmcnt(2)\n"
"v_mfma_f32_16x16x32_bf16 v[0:3], v[96:99], v[120:123], v[0:3]\n"
"ds_read_b64_tr_b16 v[116:117], v156 offset:17408\n"
"v_mfma_f32_16x16x32_bf16 v[24:27], v[96:99], v[124:127], v[24:27]\n"
"ds_read_b64_tr_b16 v[118:119], v156 offset:17920\n"
"v_mfma_f32_16x16x32_bf16 v[48:51], v[96:99], v[128:131], v[48:51]\n"
"v_mfma_f32_16x16x32_bf16 v[72:75], v[96:99], v[132:135], v[72:75]\n"
"v_mfma_f32_16x16x32_bf16 v[4:7], v[100:103], v[120:123], v[4:7]\n"
"ds_read_b64_tr_b16 v[96:97], v154 offset:12288\n"
"v_mfma_f32_16x16x32_bf16 v[28:31], v[100:103], v[124:127], v[28:31]\n"
"ds_read_b64_tr_b16 v[98:99], v154 offset:12800\n"
"v_mfma_f32_16x16x32_bf16 v[52:55], v[100:103], v[128:131], v[52:55]\n"
"ds_read_b128 v[136:139], v153 offset:16384\n"
"v_mfma_f32_16x16x32_bf16 v[76:79], v[100:103], v[132:135], v[76:79]\n"
"v_mfma_f32_16x16x32_bf16 v[8:11], v[104:107], v[120:123], v[8:11]\n"
"ds_read_b64_tr_b16 v[100:101], v154 offset:12296\n"
"v_mfma_f32_16x16x32_bf16 v[32:35], v[104:107], v[124:127], v[32:35]\n"
"ds_read_b64_tr_b16 v[102:103], v154 offset:12808\n"
"v_mfma_f32_16x16x32_bf16 v[56:59], v[104:107], v[128:131], v[56:59]\n"
"ds_read_b128 v[140:143], v153 offset:18432\n"
"v_mfma_f32_16x16x32_bf16 v[80:83], v[104:107], v[132:135], v[80:83]\n"
"v_mfma_f32_16x16x32_bf16 v[12:15], v[108:111], v[120:123], v[12:15]\n"
"ds_read_b64_tr_b16 v[104:105], v155 offset:24576\n"
"v_mfma_f32_16x16x32_bf16 v[36:39], v[108:111], v[124:127], v[36:39]\n"
"ds_read_b64_tr_b16 v[106:107], v155 offset:25088\n"
"v_mfma_f32_16x16x32_bf16 v[60:63], v[108:111], v[128:131], v[60:63]\n"
"ds_read_b128 v[144:147], v153 offset:20480\n"
"v_mfma_f32_16x16x32_bf16 v[84:87], v[108:111], v[132:135], v[84:87]\n"
"s_waitcnt lgkmcnt(11)\n"
"v_mfma_f32_16x16x32_bf16 v[16:19], v[112:115], v[120:123], v[16:19]\n"
"ds_read_b64_tr_b16 v[108:109], v156 offset:24576\n"
"v_mfma_f32_16x16x32_bf16 v[40:43], v[112:115], v[124:127], v[40:43]\n"
"ds_read_b64_tr_b16 v[110:111], v156 offset:25088\n"
"v_mfma_f32_16x16x32_bf16 v[64:67], v[112:115], v[128:131], v[64:67]\n"
"ds_read_b128 v[148:151], v153 offset:22528\n"
"v_mfma_f32_16x16x32_bf16 v[88:91], v[112:115], v[132:135], v[88:91]\n"
"s_waitcnt lgkmcnt(12)\n"
"v_mfma_f32_16x16x32_bf16 v[20:23], v[116:119], v[120:123], v[20:23]\n"
"ds_read_b64_tr_b16 v[112:113], v155 offset:25600\n"
"v_mfma_f32_16x16x32_bf16 v[44:47], v[116:119], v[124:127], v[44:47]\n"
"ds_read_b64_tr_b16 v[114:115], v155 offset:26112\n"
"v_mfma_f32_16x16x32_bf16 v[68:71], v[116:119], v[128:131], v[68:71]\n"
"v_mfma_f32_16x16x32_bf16 v[92:95], v[116:119], v[132:135], v[92:95]\n"
"s_waitcnt lgkmcnt(2)\n"
"v_mfma_f32_16x16x32_bf16 v[0:3], v[96:99], v[136:139], v[0:3]\n"
"ds_read_b64_tr_b16 v[116:117], v156 offset:25600\n"
"v_mfma_f32_16x16x32_bf16 v[24:27], v[96:99], v[140:143], v[24:27]\n"
"ds_read_b64_tr_b16 v[118:119], v156 offset:26112\n"
"v_mfma_f32_16x16x32_bf16 v[48:51], v[96:99], v[144:147], v[48:51]\n"
"v_mfma_f32_16x16x32_bf16 v[72:75], v[96:99], v[148:151], v[72:75]\n"
"v_mfma_f32_16x16x32_bf16 v[4:7], v[100:103], v[136:139], v[4:7]\n"
"v_mfma_f32_16x16x32_bf16 v[28:31], v[100:103], v[140:143], v[28:31]\n"
"v_mfma_f32_16x16x32_bf16 v[52:55], v[100:103], v[144:147], v[52:55]\n"
"v_mfma_f32_16x16x32_bf16 v[76:79], v[100:103], v[148:151], v[76:79]\n"
"s_waitcnt lgkmcnt(0)\n"
"s_barrier\n"
"ds_read_b64_tr_b16 v[96:97], v154 offset:16384\n"
"v_mfma_f32_16x16x32_bf16 v[8:11], v[104:107], v[136:139], v[8:11]\n"
"ds_read_b64_tr_b16 v[98:99], v154 offset:16896\n"
"v_mfma_f32_16x16x32_bf16 v[32:35], v[104:107], v[140:143], v[32:35]\n"
"ds_read_b128 v[120:123], v152 offset:32768\n"
"v_mfma_f32_16x16x32_bf16 v[56:59], v[104:107], v[144:147], v[56:59]\n"
"ds_read_b64_tr_b16 v[100:101], v154 offset:16392\n"
"v_mfma_f32_16x16x32_bf16 v[80:83], v[104:107], v[148:151], v[80:83]\n"
"ds_read_b64_tr_b16 v[102:103], v154 offset:16904\n"
"v_mfma_f32_16x16x32_bf16 v[12:15], v[108:111], v[136:139], v[12:15]\n"
"ds_read_b128 v[124:127], v152 offset:34816\n"
"v_mfma_f32_16x16x32_bf16 v[36:39], v[108:111], v[140:143], v[36:39]\n"
"ds_read_b64_tr_b16 v[104:105], v155 offset:32768\n"
"v_mfma_f32_16x16x32_bf16 v[60:63], v[108:111], v[144:147], v[60:63]\n"
"ds_read_b64_tr_b16 v[106:107], v155 offset:33280\n"
"v_mfma_f32_16x16x32_bf16 v[84:87], v[108:111], v[148:151], v[84:87]\n"
"ds_read_b128 v[128:131], v152 offset:36864\n"
"v_mfma_f32_16x16x32_bf16 v[16:19], v[112:115], v[136:139], v[16:19]\n"
"ds_read_b64_tr_b16 v[108:109], v156 offset:32768\n"
"v_mfma_f32_16x16x32_bf16 v[40:43], v[112:115], v[140:143], v[40:43]\n"
"ds_read_b64_tr_b16 v[110:111], v156 offset:33280\n"
"v_mfma_f32_16x16x32_bf16 v[64:67], v[112:115], v[144:147], v[64:67]\n"
"ds_read_b128 v[132:135], v152 offset:38912\n"
"v_mfma_f32_16x16x32_bf16 v[88:91], v[112:115], v[148:151], v[88:91]\n"
"v_mfma_f32_16x16x32_bf16 v[20:23], v[116:119], v[136:139], v[20:23]\n"
"ds_read_b64_tr_b16 v[112:113], v155 offset:33792\n"
"v_mfma_f32_16x16x32_bf16 v[44:47], v[116:119], v[140:143], v[44:47]\n"
"ds_read_b64_tr_b16 v[114:115], v155 offset:34304\n"
"v_mfma_f32_16x16x32_bf16 v[68:71], v[116:119], v[144:147], v[68:71]\n"
"v_mfma_f32_16x16x32_bf16 v[92:95], v[116:119], v[148:151], v[92:95]\n"
"s_waitcnt lgkmcnt(2)\n"
"v_mfma_f32_16x16x32_bf16 v[0:3], v[96:99], v[120:123], v[0:3]\n"
"ds_read_b64_tr_b16 v[116:117], v156 offset:33792\n"
"v_mfma_f32_16x16x32_bf16 v[24:27], v[96:99], v[124:127], v[24:27]\n"
"ds_read_b64_tr_b16 v[118:119], v156 offset:34304\n"
"v_mfma_f32_16x16x32_bf16 v[48:51], v[96:99], v[128:131], v[48:51]\n"
"v_mfma_f32_16x16x32_bf16 v[72:75], v[96:99], v[132:135], v[72:75]\n"
"v_mfma_f32_16x16x32_bf16 v[4:7], v[100:103], v[120:123], v[4:7]\n"
"ds_read_b64_tr_b16 v[96:97], v154 offset:20480\n"
"v_mfma_f32_16x16x32_bf16 v[28:31], v[100:103], v[124:127], v[28:31]\n"
"ds_read_b64_tr_b16 v[98:99], v154 offset:20992\n"
"v_mfma_f32_16x16x32_bf16 v[52:55], v[100:103], v[128:131], v[52:55]\n"
"ds_read_b128 v[136:139], v153 offset:32768\n"
"v_mfma_f32_16x16x32_bf16 v[76:79], v[100:103], v[132:135], v[76:79]\n"
"v_mfma_f32_16x16x32_bf16 v[8:11], v[104:107], v[120:123], v[8:11]\n"
"ds_read_b64_tr_b16 v[100:101], v154 offset:20488\n"
"v_mfma_f32_16x16x32_bf16 v[32:35], v[104:107], v[124:127], v[32:35]\n"
"ds_read_b64_tr_b16 v[102:103], v154 offset:21000\n"
"v_mfma_f32_16x16x32_bf16 v[56:59], v[104:107], v[128:131], v[56:59]\n"
"ds_read_b128 v[140:143], v153 offset:34816\n"
"v_mfma_f32_16x16x32_bf16 v[80:83], v[104:107], v[132:135], v[80:83]\n"
"v_mfma_f32_16x16x32_bf16 v[12:15], v[108:111], v[120:123], v[12:15]\n"
"ds_read_b64_tr_b16 v[104:105], v155 offset:40960\n"
"v_mfma_f32_16x16x32_bf16 v[36:39], v[108:111], v[124:127], v[36:39]\n"
"ds_read_b64_tr_b16 v[106:107], v155 offset:41472\n"
"v_mfma_f32_16x16x32_bf16 v[60:63], v[108:111], v[128:131], v[60:63]\n"
"ds_read_b128 v[144:147], v153 offset:36864\n"
"v_mfma_f32_16x16x32_bf16 v[84:87], v[108:111], v[132:135], v[84:87]\n"
"s_waitcnt lgkmcnt(11)\n"
"v_mfma_f32_16x16x32_bf16 v[16:19], v[112:115], v[120:123], v[16:19]\n"
"ds_read_b64_tr_b16 v[108:109], v156 offset:40960\n"
"v_mfma_f32_16x16x32_bf16 v[40:43], v[112:115], v[124:127], v[40:43]\n"
"ds_read_b64_tr_b16 v[110:111], v156 offset:41472\n"
"v_mfma_f32_16x16x32_bf16 v[64:67], v[112:115], v[128:131], v[64:67]\n"
"ds_read_b128 v[148:151], v153 offset:38912\n"
"v_mfma_f32_16x16x32_bf16 v[88:91], v[112:115], v[132:135], v[88:91]\n"
"s_waitcnt lgkmcnt(12)\n"
"v_mfma_f32_16x16x32_bf16 v[20:23], v[116:119], v[120:123], v[20:23]\n"
"ds_read_b64_tr_b16 v[112:113], v155 offset:41984\n"
"v_mfma_f32_16x16x32_bf16 v[44:47], v[116:119], v[124:127], v[44:47]\n"
"ds_read_b64_tr_b16 v[114:115], v155 offset:42496\n"
"v_mfma_f32_16x16x32_bf16 v[68:71], v[116:119], v[128:131], v[68:71]\n"
"v_mfma_f32_16x16x32_bf16 v[92:95], v[116:119], v[132:135], v[92:95]\n"
"s_waitcnt lgkmcnt(2)\n"
"v_mfma_f32_16x16x32_bf16 v[0:3], v[96:99], v[136:139], v[0:3]\n"
"ds_read_b64_tr_b16 v[116:117], v156 offset:41984\n"
"v_mfma_f32_16x16x32_bf16 v[24:27], v[96:99], v[140:143], v[24:27]\n"
"ds_read_b64_tr_b16 v[118:119], v156 offset:42496\n"
"v_mfma_f32_16x16x32_bf16 v[48:51], v[96:99], v[144:147], v[48:51]\n"
"v_mfma_f32_16x16x32_bf16 v[72:75], v[96:99], v[148:151], v[72:75]\n"
"v_mfma_f32_16x16x32_bf16 v[4:7], v[100:103], v[136:139], v[4:7]\n"
"v_mfma_f32_16x16x32_bf16 v[28:31], v[100:103], v[140:143], v[28:31]\n"
"v_mfma_f32_16x16x32_bf16 v[52:55], v[100:103], v[144:147], v[52:55]\n"
"v_mfma_f32_16x16x32_bf16 v[76:79], v[100:103], v[148:151], v[76:79]\n"
"s_waitcnt lgkmcnt(0)\n"
"s_barrier\n"
"ds_read_b64_tr_b16 v[96:97], v154 offset:0\n"
"v_mfma_f32_16x16x32_bf16 v[8:11], v[104:107], v[136:139], v[8:11]\n"
"ds_read_b64_tr_b16 v[98:99], v154 offset:512\n"
"v_mfma_f32_16x16x32_bf16 v[32:35], v[104:107], v[140:143], v[32:35]\n"
"ds_read_b128 v[120:123], v152 offset:0\n"
"v_mfma_f32_16x16x32_bf16 v[56:59], v[104:107], v[144:147], v[56:59]\n"
"ds_read_b64_tr_b16 v[100:101], v154 offset:8\n"
"v_mfma_f32_16x16x32_bf16 v[80:83], v[104:107], v[148:151], v[80:83]\n"
"ds_read_b64_tr_b16 v[102:103], v154 offset:520\n"
"v_mfma_f32_16x16x32_bf16 v[12:15], v[108:111], v[136:139], v[12:15]\n"
"ds_read_b128 v[124:127], v152 offset:2048\n"
"v_mfma_f32_16x16x32_bf16 v[36:39], v[108:111], v[140:143], v[36:39]\n"
"ds_read_b64_tr_b16 v[104:105], v155 offset:0\n"
"v_mfma_f32_16x16x32_bf16 v[60:63], v[108:111], v[144:147], v[60:63]\n"
"ds_read_b64_tr_b16 v[106:107], v155 offset:512\n"
"v_mfma_f32_16x16x32_bf16 v[84:87], v[108:111], v[148:151], v[84:87]\n"
"ds_read_b128 v[128:131], v152 offset:4096\n"
"v_mfma_f32_16x16x32_bf16 v[16:19], v[112:115], v[136:139], v[16:19]\n"
"ds_read_b64_tr_b16 v[108:109], v156 offset:0\n"
"v_mfma_f32_16x16x32_bf16 v[40:43], v[112:115], v[140:143], v[40:43]\n"
"ds_read_b64_tr_b16 v[110:111], v156 offset:512\n"
"v_mfma_f32_16x16x32_bf16 v[64:67], v[112:115], v[144:147], v[64:67]\n"
"ds_read_b128 v[132:135], v152 offset:6144\n"
"v_mfma_f32_16x16x32_bf16 v[88:91], v[112:115], v[148:151], v[88:91]\n"
"v_mfma_f32_16x16x32_bf16 v[20:23], v[116:119], v[136:139], v[20:23]\n"
"ds_read_b64_tr_b16 v[112:113], v155 offset:1024\n"
"v_mfma_f32_16x16x32_bf16 v[44:47], v[116:119], v[140:143], v[44:47]\n"
"ds_read_b64_tr_b16 v[114:115], v155 offset:1536\n"
"v_mfma_f32_16x16x32_bf16 v[68:71], v[116:119], v[144:147], v[68:71]\n"
"v_mfma_f32_16x16x32_bf16 v[92:95], v[116:119], v[148:151], v[92:95]\n"
"s_waitcnt lgkmcnt(2)\n"
"v_mfma_f32_16x16x32_bf16 v[0:3], v[96:99], v[120:123], v[0:3]\n"
"ds_read_b64_tr_b16 v[116:117], v156 offset:1024\n"
"v_mfma_f32_16x16x32_bf16 v[24:27], v[96:99], v[124:127], v[24:27]\n"
"ds_read_b64_tr_b16 v[118:119], v156 offset:1536\n"
"v_mfma_f32_16x16x32_bf16 v[48:51], v[96:99], v[128:131], v[48:51]\n"
"v_mfma_f32_16x16x32_bf16 v[72:75], v[96:99], v[132:135], v[72:75]\n"
"v_mfma_f32_16x16x32_bf16 v[4:7], v[100:103], v[120:123], v[4:7]\n"
"ds_read_b64_tr_b16 v[96:97], v154 offset:4096\n"
"v_mfma_f32_16x16x32_bf16 v[28:31], v[100:103], v[124:127], v[28:31]\n"
"ds_read_b64_tr_b16 v[98:99], v154 offset:4608\n"
"v_mfma_f32_16x16x32_bf16 v[52:55], v[100:103], v[128:131], v[52:55]\n"
"ds_read_b128 v[136:139], v153 offset:0\n"
"v_mfma_f32_16x16x32_bf16 v[76:79], v[100:103], v[132:135], v[76:79]\n"
"v_mfma_f32_16x16x32_bf16 v[8:11], v[104:107], v[120:123], v[8:11]\n"
"ds_read_b64_tr_b16 v[100:101], v154 offset:4104\n"
"v_mfma_f32_16x16x32_bf16 v[32:35], v[104:107], v[124:127], v[32:35]\n"
"ds_read_b64_tr_b16 v[102:103], v154 offset:4616\n"
"v_mfma_f32_16x16x32_bf16 v[56:59], v[104:107], v[128:131], v[56:59]\n"
"ds_read_b128 v[140:143], v153 offset:2048\n"
"v_mfma_f32_16x16x32_bf16 v[80:83], v[104:107], v[132:135], v[80:83]\n"
"v_mfma_f32_16x16x32_bf16 v[12:15], v[108:111], v[120:123], v[12:15]\n"
"ds_read_b64_tr_b16 v[104:105], v155 offset:8192\n"
"v_mfma_f32_16x16x32_bf16 v[36:39], v[108:111], v[124:127], v[36:39]\n"
"ds_read_b64_tr_b16 v[106:107], v155 offset:8704\n"
"v_mfma_f32_16x16x32_bf16 v[60:63], v[108:111], v[128:131], v[60:63]\n"
"ds_read_b128 v[144:147], v153 offset:4096\n"
"v_mfma_f32_16x16x32_bf16 v[84:87], v[108:111], v[132:135], v[84:87]\n"
"s_waitcnt lgkmcnt(11)\n"
"v_mfma_f32_16x16x32_bf16 v[16:19], v[112:115], v[120:123], v[16:19]\n"
"ds_read_b64_tr_b16 v[108:109], v156 offset:8192\n"
"v_mfma_f32_16x16x32_bf16 v[40:43], v[112:115], v[124:127], v[40:43]\n"
"ds_read_b64_tr_b16 v[110:111], v156 offset:8704\n"
"v_mfma_f32_16x16x32_bf16 v[64:67], v[112:115], v[128:131], v[64:67]\n"
"ds_read_b128 v[148:151], v153 offset:6144\n"
"v_mfma_f32_16x16x32_bf16 v[88:91], v[112:115], v[132:135], v[88:91]\n"
"s_waitcnt lgkmcnt(12)\n"
"v_mfma_f32_16x16x32_bf16 v[20:23], v[116:119], v[120:123], v[20:23]\n"
"ds_read_b64_tr_b16 v[112:113], v155 offset:9216\n"
"v_mfma_f32_16x16x32_bf16 v[44:47], v[116:119], v[124:127], v[44:47]\n"
"ds_read_b64_tr_b16 v[114:115], v155 offset:9728\n"
"v_mfma_f32_16x16x32_bf16 v[68:71], v[116:119], v[128:131], v[68:71]\n"
"v_mfma_f32_16x16x32_bf16 v[92:95], v[116:119], v[132:135], v[92:95]\n"
"s_waitcnt lgkmcnt(2)\n"
"v_mfma_f32_16x16x32_bf16 v[0:3], v[96:99], v[136:139], v[0:3]\n"
"ds_read_b64_tr_b16 v[116:117], v156 offset:9216\n"
"v_mfma_f32_16x16x32_bf16 v[24:27], v[96:99], v[140:143], v[24:27]\n"
"ds_read_b64_tr_b16 v[118:119], v156 offset:9728\n"
"v_mfma_f32_16x16x32_bf16 v[48:51], v[96:99], v[144:147], v[48:51]\n"
"v_mfma_f32_16x16x32_bf16 v[72:75], v[96:99], v[148:151], v[72:75]\n"
"s_mov_b64 exec, 1\n"
"ds_add_u32 v251, v252 offset:72\n"
"s_mov_b64 exec, -1\n"
"ds_read_b32 v240, v251 offset:60\n"
"v_mfma_f32_16x16x32_bf16 v[4:7], v[100:103], v[136:139], v[4:7]\n"
"v_mfma_f32_16x16x32_bf16 v[28:31], v[100:103], v[140:143], v[28:31]\n"
"v_mfma_f32_16x16x32_bf16 v[52:55], v[100:103], v[144:147], v[52:55]\n"
"v_mfma_f32_16x16x32_bf16 v[76:79], v[100:103], v[148:151], v[76:79]\n"
"s_waitcnt lgkmcnt(0)\n"
"v_cmp_gt_u32 vcc, 4, v240\n"
"s_nop 0\n"
"s_cbranch_vccnz Lcs55_%=\n"
"Lcb56_%=:\n"
"ds_read2_b32 v[212:213], v165 offset1:16\n"
"v_mfma_f32_16x16x32_bf16 v[8:11], v[104:107], v[136:139], v[8:11]\n"
"ds_read2_b32 v[214:215], v165 offset0:32 offset1:48\n"
"v_mfma_f32_16x16x32_bf16 v[32:35], v[104:107], v[140:143], v[32:35]\n"
"ds_read2_b32 v[216:217], v165 offset0:128 offset1:144\n"
"v_mfma_f32_16x16x32_bf16 v[56:59], v[104:107], v[144:147], v[56:59]\n"
"ds_read2_b32 v[218:219], v165 offset0:160 offset1:176\n"
"v_mfma_f32_16x16x32_bf16 v[80:83], v[104:107], v[148:151], v[80:83]\n"
"ds_read_b128 v[170:173], v166 offset:0\n"
"v_mfma_f32_16x16x32_bf16 v[12:15], v[108:111], v[136:139], v[12:15]\n"
"ds_read_b128 v[174:177], v166 offset:16\n"
"v_mfma_f32_16x16x32_bf16 v[36:39], v[108:111], v[140:143], v[36:39]\n"
"ds_read_b128 v[178:181], v167 offset:0\n"
"v_mfma_f32_16x16x32_bf16 v[60:63], v[108:111], v[144:147], v[60:63]\n"
"ds_read_b128 v[182:185], v167 offset:32\n"
"v_mfma_f32_16x16x32_bf16 v[84:87], v[108:111], v[148:151], v[84:87]\n"
"v_mfma_f32_16x16x32_bf16 v[16:19], v[112:115], v[136:139], v[16:19]\n"
"ds_read_b128 v[186:189], v167 offset:256\n"
"v_mfma_f32_16x16x32_bf16 v[40:43], v[112:115], v[140:143], v[40:43]\n"
"ds_read_b128 v[190:193], v167 offset:288\n"
"v_mfma_f32_16x16x32_bf16 v[64:67], v[112:115], v[144:147], v[64:67]\n"
"v_mfma_f32_16x16x32_bf16 v[88:91], v[112:115], v[148:151], v[88:91]\n"
"v_mfma_f32_16x16x32_bf16 v[20:23], v[116:119], v[136:139], v[20:23]\n"
"v_mfma_f32_16x16x32_bf16 v[44:47], v[116:119], v[140:143], v[44:47]\n"
"v_mfma_f32_16x16x32_bf16 v[68:71], v[116:119], v[144:147], v[68:71]\n"
"v_mfma_f32_16x16x32_bf16 v[92:95], v[116:119], v[148:151], v[92:95]\n"
"s_waitcnt lgkmcnt(4)\n"
"v_mul_f32 v240, s13, v216\n"
"v_mul_f32 v241, s13, v217\n"
"v_mul_f32 v242, s13, v218\n"
"v_mul_f32 v243, s13, v219\n"
"v_mov_b32 v244, 0x3f80\n"
"v_fmac_f32 v0, v212, v170\n"
"v_fmac_f32 v1, v212, v171\n"
"v_fmac_f32 v2, v212, v172\n"
"v_fmac_f32 v3, v212, v173\n"
"v_fmac_f32 v4, v212, v174\n"
"v_fmac_f32 v5, v212, v175\n"
"v_fmac_f32 v6, v212, v176\n"
"v_fmac_f32 v7, v212, v177\n"
"v_mul_f32 v0, v240, v0\n"
"v_mul_f32 v1, v240, v1\n"
"v_mul_f32 v2, v240, v2\n"
"v_mul_f32 v3, v240, v3\n"
"v_mul_f32 v4, v240, v4\n"
"v_mul_f32 v5, v240, v5\n"
"v_mul_f32 v6, v240, v6\n"
"v_mul_f32 v7, v240, v7\n"
"v_cvt_pk_bf16_f32 v100, v4, v5\n"
"v_cvt_pk_bf16_f32 v101, v6, v7\n"
"v_cvt_pk_bf16_f32 v96, v0, v1\n"
"v_cvt_pk_bf16_f32 v97, v2, v3\n"
"v_cndmask_b32_e64 v98, v100, v244, s[4:5]\n"
"v_cndmask_b32_e64 v99, v101, 0, s[4:5]\n"
"buffer_store_dwordx4 v[96:99], v159, s[48:51], 0 offen offset:0 sc1\n"
"v_fmac_f32 v24, v213, v170\n"
"v_fmac_f32 v25, v213, v171\n"
"v_fmac_f32 v26, v213, v172\n"
"v_fmac_f32 v27, v213, v173\n"
"v_fmac_f32 v28, v213, v174\n"
"v_fmac_f32 v29, v213, v175\n"
"v_fmac_f32 v30, v213, v176\n"
"v_fmac_f32 v31, v213, v177\n"
"v_mul_f32 v24, v241, v24\n"
"v_mul_f32 v25, v241, v25\n"
"v_mul_f32 v26, v241, v26\n"
"v_mul_f32 v27, v241, v27\n"
"v_mul_f32 v28, v241, v28\n"
"v_mul_f32 v29, v241, v29\n"
"v_mul_f32 v30, v241, v30\n"
"v_mul_f32 v31, v241, v31\n"
"v_cvt_pk_bf16_f32 v108, v28, v29\n"
"v_cvt_pk_bf16_f32 v109, v30, v31\n"
"v_cvt_pk_bf16_f32 v104, v24, v25\n"
"v_cvt_pk_bf16_f32 v105, v26, v27\n"
"v_cndmask_b32_e64 v106, v108, v244, s[4:5]\n"
"v_cndmask_b32_e64 v107, v109, 0, s[4:5]\n"
"buffer_store_dwordx4 v[104:107], v159, s[48:51], 0 offen offset:256 sc1\n"
"v_fmac_f32 v48, v214, v170\n"
"v_fmac_f32 v49, v214, v171\n"
"v_fmac_f32 v50, v214, v172\n"
"v_fmac_f32 v51, v214, v173\n"
"v_fmac_f32 v52, v214, v174\n"
"v_fmac_f32 v53, v214, v175\n"
"v_fmac_f32 v54, v214, v176\n"
"v_fmac_f32 v55, v214, v177\n"
"v_mul_f32 v48, v242, v48\n"
"v_mul_f32 v49, v242, v49\n"
"v_mul_f32 v50, v242, v50\n"
"v_mul_f32 v51, v242, v51\n"
"v_mul_f32 v52, v242, v52\n"
"v_mul_f32 v53, v242, v53\n"
"v_mul_f32 v54, v242, v54\n"
"v_mul_f32 v55, v242, v55\n"
"v_cvt_pk_bf16_f32 v116, v52, v53\n"
"v_cvt_pk_bf16_f32 v117, v54, v55\n"
"v_cvt_pk_bf16_f32 v112, v48, v49\n"
"v_cvt_pk_bf16_f32 v113, v50, v51\n"
"v_cndmask_b32_e64 v114, v116, v244, s[4:5]\n"
"v_cndmask_b32_e64 v115, v117, 0, s[4:5]\n"
"buffer_store_dwordx4 v[112:115], v159, s[48:51], s11 offen offset:0 sc1\n"
"v_fmac_f32 v72, v215, v170\n"
"v_fmac_f32 v73, v215, v171\n"
"v_fmac_f32 v74, v215, v172\n"
"v_fmac_f32 v75, v215, v173\n"
"v_fmac_f32 v76, v215, v174\n"
"v_fmac_f32 v77, v215, v175\n"
"v_fmac_f32 v78, v215, v176\n"
"v_fmac_f32 v79, v215, v177\n"
"v_mul_f32 v72, v243, v72\n"
"v_mul_f32 v73, v243, v73\n"
"v_mul_f32 v74, v243, v74\n"
"v_mul_f32 v75, v243, v75\n"
"v_mul_f32 v76, v243, v76\n"
"v_mul_f32 v77, v243, v77\n"
"v_mul_f32 v78, v243, v78\n"
"v_mul_f32 v79, v243, v79\n"
"v_cvt_pk_bf16_f32 v124, v76, v77\n"
"v_cvt_pk_bf16_f32 v125, v78, v79\n"
"v_cvt_pk_bf16_f32 v120, v72, v73\n"
"v_cvt_pk_bf16_f32 v121, v74, v75\n"
"v_cndmask_b32_e64 v122, v124, v244, s[4:5]\n"
"v_cndmask_b32_e64 v123, v125, 0, s[4:5]\n"
"s_mov_b64 exec, s[4:5]\n"
"s_cbranch_execz Lq58_%=\n"
"v_mov_b32 v102, v244\n"
"v_mov_b32 v103, 0\n"
"buffer_store_dwordx4 v[100:103], v160, s[48:51], 0 offen offset:0 sc1\n"
"v_mov_b32 v110, v244\n"
"v_mov_b32 v111, 0\n"
"buffer_store_dwordx4 v[108:111], v160, s[48:51], 0 offen offset:256 sc1\n"
"v_mov_b32 v118, v244\n"
"v_mov_b32 v119, 0\n"
"buffer_store_dwordx4 v[116:119], v160, s[48:51], s11 offen offset:0 sc1\n"
"v_mov_b32 v126, v244\n"
"v_mov_b32 v127, 0\n"
"buffer_store_dwordx4 v[124:127], v160, s[48:51], s11 offen offset:256 sc1\n"
"Lq58_%=:\n"
"s_mov_b64 exec, -1\n"
"buffer_store_dwordx4 v[120:123], v159, s[48:51], s11 offen offset:256 sc1\n"
"s_setprio 0\n"
"v_mul_f32 v240, 0xbfb8aa3b, v216\n"
"v_mul_f32 v216, v216, v216\n"
"v_mul_f32 v241, 0xbfb8aa3b, v217\n"
"v_mul_f32 v217, v217, v217\n"
"v_mul_f32 v242, 0xbfb8aa3b, v218\n"
"v_mul_f32 v218, v218, v218\n"
"v_mul_f32 v243, 0xbfb8aa3b, v219\n"
"v_mul_f32 v219, v219, v219\n"
"ds_read_b32 v244, v251 offset:72\n"
"v_rcp_f32 v216, v216\n"
"v_rcp_f32 v217, v217\n"
"v_rcp_f32 v218, v218\n"
"v_rcp_f32 v219, v219\n"
"s_waitcnt lgkmcnt(0)\n"
"v_cmp_gt_u32 vcc, 4, v244\n"
"s_nop 0\n"
"s_cbranch_vccnz Lcs59_%=\n"
"Lcb60_%=:\n"
"v_fmac_f32 v8, v212, v178\n"
"v_fmac_f32 v9, v212, v179\n"
"v_fmac_f32 v10, v212, v180\n"
"v_fmac_f32 v11, v212, v181\n"
"v_fmac_f32 v12, v212, v182\n"
"v_fmac_f32 v13, v212, v183\n"
"v_fmac_f32 v14, v212, v184\n"
"v_fmac_f32 v15, v212, v185\n"
"v_fmac_f32 v16, v212, v186\n"
"v_fmac_f32 v17, v212, v187\n"
"v_fmac_f32 v18, v212, v188\n"
"v_fmac_f32 v19, v212, v189\n"
"v_fmac_f32 v20, v212, v190\n"
"v_fmac_f32 v21, v212, v191\n"
"v_fmac_f32 v22, v212, v192\n"
"v_fmac_f32 v23, v212, v193\n"
"v_mul_f32 v128, v240, v8\n"
"v_mul_f32 v129, v240, v9\n"
"v_mul_f32 v130, v240, v10\n"
"v_mul_f32 v131, v240, v11\n"
"v_mul_f32 v132, v240, v12\n"
"v_mul_f32 v133, v240, v13\n"
"v_mul_f32 v134, v240, v14\n"
"v_mul_f32 v135, v240, v15\n"
"v_exp_f32 v128, v128\n"
"v_exp_f32 v129, v129\n"
"v_exp_f32 v130, v130\n"
"v_exp_f32 v131, v131\n"
"v_exp_f32 v132, v132\n"
"v_exp_f32 v133, v133\n"
"v_exp_f32 v134, v134\n"
"v_exp_f32 v135, v135\n"
"v_mul_f32 v8, v8, v16\n"
"v_mul_f32 v9, v9, v17\n"
"v_mul_f32 v10, v10, v18\n"
"v_mul_f32 v11, v11, v19\n"
"v_mul_f32 v12, v12, v20\n"
"v_mul_f32 v13, v13, v21\n"
"v_mul_f32 v14, v14, v22\n"
"v_mul_f32 v15, v15, v23\n"
"v_fma_f32 v128, v128, v216, v216\n"
"v_fma_f32 v129, v129, v216, v216\n"
"v_fma_f32 v130, v130, v216, v216\n"
"v_fma_f32 v131, v131, v216, v216\n"
"v_fma_f32 v132, v132, v216, v216\n"
"v_fma_f32 v133, v133, v216, v216\n"
"v_fma_f32 v134, v134, v216, v216\n"
"v_fma_f32 v135, v135, v216, v216\n"
"v_rcp_f32 v128, v128\n"
"v_rcp_f32 v129, v129\n"
"v_rcp_f32 v130, v130\n"
"v_rcp_f32 v131, v131\n"
"v_rcp_f32 v132, v132\n"
"v_rcp_f32 v133, v133\n"
"v_rcp_f32 v134, v134\n"
"v_rcp_f32 v135, v135\n"
"v_mul_f32 v8, v8, v128\n"
"v_mul_f32 v9, v9, v129\n"
"v_mul_f32 v10, v10, v130\n"
"v_mul_f32 v11, v11, v131\n"
"v_mul_f32 v12, v12, v132\n"
"v_mul_f32 v13, v13, v133\n"
"v_mul_f32 v14, v14, v134\n"
"v_mul_f32 v15, v15, v135\n"
"v_cvt_pk_bf16_f32 v136, v8, v9\n"
"v_cvt_pk_bf16_f32 v137, v10, v11\n"
"v_cvt_pk_bf16_f32 v138, v12, v13\n"
"v_cvt_pk_bf16_f32 v139, v14, v15\n"
"ds_write_b128 v231, v[136:139] offset:0\n"
"s_waitcnt vmcnt(0)\n"
"s_mov_b64 exec, 1\n"
"ds_add_rtn_u32 v244, v251, v252 offset:12\n"
"s_waitcnt lgkmcnt(0)\n"
"v_cmp_ne_u32 vcc, 3, v244\n"
"s_nop 0\n"
"s_cbranch_vccnz Llf62_%=\n"
"v_mov_b32 v244, s71\n"
"buffer_store_dword v244, off, s[48:51], s14 sc1\n"
"Llf62_%=:\n"
"s_mov_b64 exec, -1\n"
"s_mov_b64 exec, 0x1ffff\n"
"buffer_load_dword v210, v210, s[36:39], 0 offen\n"
"s_mov_b64 exec, 31\n"
"buffer_load_dword v211, v211, s[40:43], 0 offen\n"
"s_mov_b64 exec, -1\n"
"v_fmac_f32 v32, v213, v178\n"
"v_fmac_f32 v33, v213, v179\n"
"v_fmac_f32 v34, v213, v180\n"
"v_fmac_f32 v35, v213, v181\n"
"v_fmac_f32 v36, v213, v182\n"
"v_fmac_f32 v37, v213, v183\n"
"v_fmac_f32 v38, v213, v184\n"
"v_fmac_f32 v39, v213, v185\n"
"v_fmac_f32 v40, v213, v186\n"
"v_fmac_f32 v41, v213, v187\n"
"v_fmac_f32 v42, v213, v188\n"
"v_fmac_f32 v43, v213, v189\n"
"v_fmac_f32 v44, v213, v190\n"
"v_fmac_f32 v45, v213, v191\n"
"v_fmac_f32 v46, v213, v192\n"
"v_fmac_f32 v47, v213, v193\n"
"v_mul_f32 v128, v241, v32\n"
"v_mul_f32 v129, v241, v33\n"
"v_mul_f32 v130, v241, v34\n"
"v_mul_f32 v131, v241, v35\n"
"v_mul_f32 v132, v241, v36\n"
"v_mul_f32 v133, v241, v37\n"
"v_mul_f32 v134, v241, v38\n"
"v_mul_f32 v135, v241, v39\n"
"v_exp_f32 v128, v128\n"
"v_exp_f32 v129, v129\n"
"v_exp_f32 v130, v130\n"
"v_exp_f32 v131, v131\n"
"v_exp_f32 v132, v132\n"
"v_exp_f32 v133, v133\n"
"v_exp_f32 v134, v134\n"
"v_exp_f32 v135, v135\n"
"v_mul_f32 v32, v32, v40\n"
"v_mul_f32 v33, v33, v41\n"
"v_mul_f32 v34, v34, v42\n"
"v_mul_f32 v35, v35, v43\n"
"v_mul_f32 v36, v36, v44\n"
"v_mul_f32 v37, v37, v45\n"
"v_mul_f32 v38, v38, v46\n"
"v_mul_f32 v39, v39, v47\n"
"v_fma_f32 v128, v128, v217, v217\n"
"v_fma_f32 v129, v129, v217, v217\n"
"v_fma_f32 v130, v130, v217, v217\n"
"v_fma_f32 v131, v131, v217, v217\n"
"v_fma_f32 v132, v132, v217, v217\n"
"v_fma_f32 v133, v133, v217, v217\n"
"v_fma_f32 v134, v134, v217, v217\n"
"v_fma_f32 v135, v135, v217, v217\n"
"v_rcp_f32 v128, v128\n"
"v_rcp_f32 v129, v129\n"
"v_rcp_f32 v130, v130\n"
"v_rcp_f32 v131, v131\n"
"v_rcp_f32 v132, v132\n"
"v_rcp_f32 v133, v133\n"
"v_rcp_f32 v134, v134\n"
"v_rcp_f32 v135, v135\n"
"v_mul_f32 v32, v32, v128\n"
"v_mul_f32 v33, v33, v129\n"
"v_mul_f32 v34, v34, v130\n"
"v_mul_f32 v35, v35, v131\n"
"v_mul_f32 v36, v36, v132\n"
"v_mul_f32 v37, v37, v133\n"
"v_mul_f32 v38, v38, v134\n"
"v_mul_f32 v39, v39, v135\n"
"v_cvt_pk_bf16_f32 v140, v32, v33\n"
"v_cvt_pk_bf16_f32 v141, v34, v35\n"
"v_cvt_pk_bf16_f32 v142, v36, v37\n"
"v_cvt_pk_bf16_f32 v143, v38, v39\n"
"ds_write_b128 v231, v[140:143] offset:256\n"
"v_fmac_f32 v56, v214, v178\n"
"v_fmac_f32 v57, v214, v179\n"
"v_fmac_f32 v58, v214, v180\n"
"v_fmac_f32 v59, v214, v181\n"
"v_fmac_f32 v60, v214, v182\n"
"v_fmac_f32 v61, v214, v183\n"
"v_fmac_f32 v62, v214, v184\n"
"v_fmac_f32 v63, v214, v185\n"
"v_fmac_f32 v64, v214, v186\n"
"v_fmac_f32 v65, v214, v187\n"
"v_fmac_f32 v66, v214, v188\n"
"v_fmac_f32 v67, v214, v189\n"
"v_fmac_f32 v68, v214, v190\n"
"v_fmac_f32 v69, v214, v191\n"
"v_fmac_f32 v70, v214, v192\n"
"v_fmac_f32 v71, v214, v193\n"
"v_mul_f32 v128, v242, v56\n"
"v_mul_f32 v129, v242, v57\n"
"v_mul_f32 v130, v242, v58\n"
"v_mul_f32 v131, v242, v59\n"
"v_mul_f32 v132, v242, v60\n"
"v_mul_f32 v133, v242, v61\n"
"v_mul_f32 v134, v242, v62\n"
"v_mul_f32 v135, v242, v63\n"
"v_exp_f32 v128, v128\n"
"v_exp_f32 v129, v129\n"
"v_exp_f32 v130, v130\n"
"v_exp_f32 v131, v131\n"
"v_exp_f32 v132, v132\n"
"v_exp_f32 v133, v133\n"
"v_exp_f32 v134, v134\n"
"v_exp_f32 v135, v135\n"
"v_mul_f32 v56, v56, v64\n"
"v_mul_f32 v57, v57, v65\n"
"v_mul_f32 v58, v58, v66\n"
"v_mul_f32 v59, v59, v67\n"
"v_mul_f32 v60, v60, v68\n"
"v_mul_f32 v61, v61, v69\n"
"v_mul_f32 v62, v62, v70\n"
"v_mul_f32 v63, v63, v71\n"
"v_fma_f32 v128, v128, v218, v218\n"
"v_fma_f32 v129, v129, v218, v218\n"
"v_fma_f32 v130, v130, v218, v218\n"
"v_fma_f32 v131, v131, v218, v218\n"
"v_fma_f32 v132, v132, v218, v218\n"
"v_fma_f32 v133, v133, v218, v218\n"
"v_fma_f32 v134, v134, v218, v218\n"
"v_fma_f32 v135, v135, v218, v218\n"
"v_rcp_f32 v128, v128\n"
"v_rcp_f32 v129, v129\n"
"v_rcp_f32 v130, v130\n"
"v_rcp_f32 v131, v131\n"
"v_rcp_f32 v132, v132\n"
"v_rcp_f32 v133, v133\n"
"v_rcp_f32 v134, v134\n"
"v_rcp_f32 v135, v135\n"
"v_mul_f32 v56, v56, v128\n"
"v_mul_f32 v57, v57, v129\n"
"v_mul_f32 v58, v58, v130\n"
"v_mul_f32 v59, v59, v131\n"
"v_mul_f32 v60, v60, v132\n"
"v_mul_f32 v61, v61, v133\n"
"v_mul_f32 v62, v62, v134\n"
"v_mul_f32 v63, v63, v135\n"
"v_cvt_pk_bf16_f32 v144, v56, v57\n"
"v_cvt_pk_bf16_f32 v145, v58, v59\n"
"v_cvt_pk_bf16_f32 v146, v60, v61\n"
"v_cvt_pk_bf16_f32 v147, v62, v63\n"
"ds_write_b128 v231, v[144:147] offset:2048\n"
"s_mov_b64 exec, 0x3ff\n"
"buffer_load_dword v244, v227, s[48:51], 0 offen sc1\n"
"s_mov_b64 exec, -1\n"
"v_fmac_f32 v80, v215, v178\n"
"v_fmac_f32 v81, v215, v179\n"
"v_fmac_f32 v82, v215, v180\n"
"v_fmac_f32 v83, v215, v181\n"
"v_fmac_f32 v84, v215, v182\n"
"v_fmac_f32 v85, v215, v183\n"
"v_fmac_f32 v86, v215, v184\n"
"v_fmac_f32 v87, v215, v185\n"
"v_fmac_f32 v88, v215, v186\n"
"v_fmac_f32 v89, v215, v187\n"
"v_fmac_f32 v90, v215, v188\n"
"v_fmac_f32 v91, v215, v189\n"
"v_fmac_f32 v92, v215, v190\n"
"v_fmac_f32 v93, v215, v191\n"
"v_fmac_f32 v94, v215, v192\n"
"v_fmac_f32 v95, v215, v193\n"
"v_mul_f32 v128, v243, v80\n"
"v_mul_f32 v129, v243, v81\n"
"v_mul_f32 v130, v243, v82\n"
"v_mul_f32 v131, v243, v83\n"
"v_mul_f32 v132, v243, v84\n"
"v_mul_f32 v133, v243, v85\n"
"v_mul_f32 v134, v243, v86\n"
"v_mul_f32 v135, v243, v87\n"
"v_exp_f32 v128, v128\n"
"v_exp_f32 v129, v129\n"
"v_exp_f32 v130, v130\n"
"v_exp_f32 v131, v131\n"
"v_exp_f32 v132, v132\n"
"v_exp_f32 v133, v133\n"
"v_exp_f32 v134, v134\n"
"v_exp_f32 v135, v135\n"
"v_mul_f32 v80, v80, v88\n"
"v_mul_f32 v81, v81, v89\n"
"v_mul_f32 v82, v82, v90\n"
"v_mul_f32 v83, v83, v91\n"
"v_mul_f32 v84, v84, v92\n"
"v_mul_f32 v85, v85, v93\n"
"v_mul_f32 v86, v86, v94\n"
"v_mul_f32 v87, v87, v95\n"
"v_fma_f32 v128, v128, v219, v219\n"
"v_fma_f32 v129, v129, v219, v219\n"
"v_fma_f32 v130, v130, v219, v219\n"
"v_fma_f32 v131, v131, v219, v219\n"
"v_fma_f32 v132, v132, v219, v219\n"
"v_fma_f32 v133, v133, v219, v219\n"
"v_fma_f32 v134, v134, v219, v219\n"
"v_fma_f32 v135, v135, v219, v219\n"
"v_rcp_f32 v128, v128\n"
"v_rcp_f32 v129, v129\n"
"v_rcp_f32 v130, v130\n"
"v_rcp_f32 v131, v131\n"
"v_rcp_f32 v132, v132\n"
"v_rcp_f32 v133, v133\n"
"v_rcp_f32 v134, v134\n"
"v_rcp_f32 v135, v135\n"
"v_mul_f32 v80, v80, v128\n"
"v_mul_f32 v81, v81, v129\n"
"v_mul_f32 v82, v82, v130\n"
"v_mul_f32 v83, v83, v131\n"
"v_mul_f32 v84, v84, v132\n"
"v_mul_f32 v85, v85, v133\n"
"v_mul_f32 v86, v86, v134\n"
"v_mul_f32 v87, v87, v135\n"
"v_cvt_pk_bf16_f32 v148, v80, v81\n"
"v_cvt_pk_bf16_f32 v149, v82, v83\n"
"v_cvt_pk_bf16_f32 v150, v84, v85\n"
"v_cvt_pk_bf16_f32 v151, v86, v87\n"
"ds_write_b128 v231, v[148:151] offset:2304\n"
"s_waitcnt vmcnt(0)\n"
"s_setprio 2\n"
"s_mov_b64 exec, 0x3ff\n"
"v_cmp_ne_u32 vcc, s71, v244\n"
"s_mov_b64 exec, -1\n"
"s_cbranch_vccz LATU54_%=\n"
"s_branch LATT53_%=\n"
"LY52_%=:\n"
"v_and_b32 v241, 2, v224\n"
"v_lshl_or_b32 v240, v241, 3, v240\n"
"v_lshl_or_b32 v240, v233, 5, v240\n"
"s_lshl_b32 s84, s9, 10\n"
"s_add_u32 s84, s84, 73728\n"
"v_add_u32 v155, s84, v240\n"
"v_xor_b32 v156, 32, v155\n"
"v_xor_b32 v157, 64, v155\n"
"v_xor_b32 v158, 96, v155\n"
"s_add_u32 s84, s12, 1050624\n"
"v_lshl_add_u32 v240, v232, 9, s84\n"
"v_lshl_add_u32 v240, v233, 10, v240\n"
"v_mov_b32 v231, 1073741824\n"
"s_lshl_b32 s84, s9, 8\n"
"s_add_u32 s84, s84, 161024\n"
"v_lshl_add_u32 v167, v225, 2, s84\n"
"s_lshl_b32 s84, s8, 8\n"
"s_add_u32 s84, s84, 159744\n"
"v_lshl_add_u32 v168, v226, 4, s84\n"
"s_sub_u32 s85, s78, 1792\n"
"s_lshr_b32 s85, s85, 1\n"
"s_add_u32 s85, s85, s15\n"
"v_add_u32 v241, s85, v225\n"
"v_add_u32 v242, 0, v241\n"
"v_mul_u32_u24 v243, 2341, v242\n"
"v_lshrrev_b32 v243, 16, v243\n"
"v_mul_u32_u24 v244, 28, v243\n"
"v_sub_u32 v244, v242, v244\n"
"v_cmp_gt_u32 vcc, 448, v242\n"
"v_lshl_add_u32 v161, v243, 18, v240\n"
"v_lshl_add_u32 v161, v244, 4, v161\n"
"v_cndmask_b32 v161, v231, v161, vcc\n"
"v_add_u32 v242, 16, v241\n"
"v_mul_u32_u24 v243, 2341, v242\n"
"v_lshrrev_b32 v243, 16, v243\n"
"v_mul_u32_u24 v244, 28, v243\n"
"v_sub_u32 v244, v242, v244\n"
"v_cmp_gt_u32 vcc, 448, v242\n"
"v_lshl_add_u32 v162, v243, 18, v240\n"
"v_lshl_add_u32 v162, v244, 4, v162\n"
"v_cndmask_b32 v162, v231, v162, vcc\n"
"v_add_u32 v242, 32, v241\n"
"v_mul_u32_u24 v243, 2341, v242\n"
"v_lshrrev_b32 v243, 16, v243\n"
"v_mul_u32_u24 v244, 28, v243\n"
"v_sub_u32 v244, v242, v244\n"
"v_cmp_gt_u32 vcc, 448, v242\n"
"v_lshl_add_u32 v163, v243, 18, v240\n"
"v_lshl_add_u32 v163, v244, 4, v163\n"
"v_cndmask_b32 v163, v231, v163, vcc\n"
"v_add_u32 v242, 48, v241\n"
"v_mul_u32_u24 v243, 2341, v242\n"
"v_lshrrev_b32 v243, 16, v243\n"
"v_mul_u32_u24 v244, 28, v243\n"
"v_sub_u32 v244, v242, v244\n"
"v_cmp_gt_u32 vcc, 448, v242\n"
"v_lshl_add_u32 v164, v243, 18, v240\n"
"v_lshl_add_u32 v164, v244, 4, v164\n"
"v_cndmask_b32 v164, v231, v164, vcc\n"
"s_waitcnt lgkmcnt(0)\n"
"s_mul_i32 s71, s71, 1664525\n"
"s_add_u32 s71, s71, 1013904223\n"
"s_setprio 2\n"
"s_barrier\n"
"ds_read_b64_tr_b16 v[96:97], v154 offset:0\n"
"ds_read_b64_tr_b16 v[98:99], v154 offset:512\n"
"ds_read_b128 v[120:123], v152 offset:0\n"
"ds_read_b128 v[124:127], v152 offset:2048\n"
"ds_read_b128 v[128:131], v152 offset:4096\n"
"ds_read_b128 v[132:135], v152 offset:6144\n"
"ds_read_b64_tr_b16 v[100:101], v154 offset:8\n"
"ds_read_b64_tr_b16 v[102:103], v154 offset:520\n"
"ds_read_b64_tr_b16 v[104:105], v155 offset:0\n"
"ds_read_b64_tr_b16 v[106:107], v155 offset:512\n"
"ds_read_b64_tr_b16 v[108:109], v156 offset:0\n"
"ds_read_b64_tr_b16 v[110:111], v156 offset:512\n"
"ds_read_b64_tr_b16 v[112:113], v157 offset:0\n"
"ds_read_b64_tr_b16 v[114:115], v157 offset:512\n"
"s_waitcnt lgkmcnt(11)\n"
"v_mfma_f32_16x16x32_bf16 v[0:3], v[96:99], v[120:123], 0\n"
"ds_read_b64_tr_b16 v[116:117], v158 offset:0\n"
"s_waitcnt lgkmcnt(11)\n"
"v_mfma_f32_16x16x32_bf16 v[24:27], v[96:99], v[124:127], 0\n"
"ds_read_b64_tr_b16 v[118:119], v158 offset:512\n"
"s_waitcnt lgkmcnt(11)\n"
"v_mfma_f32_16x16x32_bf16 v[48:51], v[96:99], v[128:131], 0\n"
"s_waitcnt lgkmcnt(10)\n"
"v_mfma_f32_16x16x32_bf16 v[72:75], v[96:99], v[132:135], 0\n"
"s_waitcnt lgkmcnt(8)\n"
"v_mfma_f32_16x16x32_bf16 v[4:7], v[100:103], v[120:123], 0\n"
"ds_read_b64_tr_b16 v[96:97], v154 offset:4096\n"
"v_mfma_f32_16x16x32_bf16 v[28:31], v[100:103], v[124:127], 0\n"
"ds_read_b64_tr_b16 v[98:99], v154 offset:4608\n"
"v_mfma_f32_16x16x32_bf16 v[52:55], v[100:103], v[128:131], 0\n"
"ds_read_b128 v[136:139], v153 offset:0\n"
"v_mfma_f32_16x16x32_bf16 v[76:79], v[100:103], v[132:135], 0\n"
"s_waitcnt lgkmcnt(9)\n"
"v_mfma_f32_16x16x32_bf16 v[8:11], v[120:123], v[104:107], 0\n"
"ds_read_b64_tr_b16 v[100:101], v154 offset:4104\n"
"v_mfma_f32_16x16x32_bf16 v[32:35], v[124:127], v[104:107], 0\n"
"ds_read_b64_tr_b16 v[102:103], v154 offset:4616\n"
"v_mfma_f32_16x16x32_bf16 v[56:59], v[128:131], v[104:107], 0\n"
"ds_read_b128 v[140:143], v153 offset:2048\n"
"v_mfma_f32_16x16x32_bf16 v[80:83], v[132:135], v[104:107], 0\n"
"s_waitcnt lgkmcnt(10)\n"
"v_mfma_f32_16x16x32_bf16 v[12:15], v[120:123], v[108:111], 0\n"
"ds_read_b64_tr_b16 v[104:105], v155 offset:8192\n"
"v_mfma_f32_16x16x32_bf16 v[36:39], v[124:127], v[108:111], 0\n"
"ds_read_b64_tr_b16 v[106:107], v155 offset:8704\n"
"v_mfma_f32_16x16x32_bf16 v[60:63], v[128:131], v[108:111], 0\n"
"ds_read_b128 v[144:147], v153 offset:4096\n"
"v_mfma_f32_16x16x32_bf16 v[84:87], v[132:135], v[108:111], 0\n"
"s_waitcnt lgkmcnt(11)\n"
"v_mfma_f32_16x16x32_bf16 v[16:19], v[120:123], v[112:115], 0\n"
"ds_read_b64_tr_b16 v[108:109], v156 offset:8192\n"
"v_mfma_f32_16x16x32_bf16 v[40:43], v[124:127], v[112:115], 0\n"
"ds_read_b64_tr_b16 v[110:111], v156 offset:8704\n"
"v_mfma_f32_16x16x32_bf16 v[64:67], v[128:131], v[112:115], 0\n"
"ds_read_b128 v[148:151], v153 offset:6144\n"
"v_mfma_f32_16x16x32_bf16 v[88:91], v[132:135], v[112:115], 0\n"
"s_waitcnt lgkmcnt(12)\n"
"v_mfma_f32_16x16x32_bf16 v[20:23], v[120:123], v[116:119], 0\n"
"ds_read_b64_tr_b16 v[112:113], v157 offset:8192\n"
"v_mfma_f32_16x16x32_bf16 v[44:47], v[124:127], v[116:119], 0\n"
"ds_read_b64_tr_b16 v[114:115], v157 offset:8704\n"
"v_mfma_f32_16x16x32_bf16 v[68:71], v[128:131], v[116:119], 0\n"
"v_mfma_f32_16x16x32_bf16 v[92:95], v[132:135], v[116:119], 0\n"
"s_waitcnt lgkmcnt(2)\n"
"v_mfma_f32_16x16x32_bf16 v[0:3], v[96:99], v[136:139], v[0:3]\n"
"ds_read_b64_tr_b16 v[116:117], v158 offset:8192\n"
"v_mfma_f32_16x16x32_bf16 v[24:27], v[96:99], v[140:143], v[24:27]\n"
"ds_read_b64_tr_b16 v[118:119], v158 offset:8704\n"
"v_mfma_f32_16x16x32_bf16 v[48:51], v[96:99], v[144:147], v[48:51]\n"
"v_mfma_f32_16x16x32_bf16 v[72:75], v[96:99], v[148:151], v[72:75]\n"
"v_mfma_f32_16x16x32_bf16 v[4:7], v[100:103], v[136:139], v[4:7]\n"
"v_mfma_f32_16x16x32_bf16 v[28:31], v[100:103], v[140:143], v[28:31]\n"
"v_mfma_f32_16x16x32_bf16 v[52:55], v[100:103], v[144:147], v[52:55]\n"
"v_mfma_f32_16x16x32_bf16 v[76:79], v[100:103], v[148:151], v[76:79]\n"
"s_waitcnt lgkmcnt(0)\n"
"s_barrier\n"
"ds_read_b64_tr_b16 v[96:97], v154 offset:8192\n"
"v_mfma_f32_16x16x32_bf16 v[8:11], v[136:139], v[104:107], v[8:11]\n"
"ds_read_b64_tr_b16 v[98:99], v154 offset:8704\n"
"v_mfma_f32_16x16x32_bf16 v[32:35], v[140:143], v[104:107], v[32:35]\n"
"ds_read_b128 v[120:123], v152 offset:16384\n"
"v_mfma_f32_16x16x32_bf16 v[56:59], v[144:147], v[104:107], v[56:59]\n"
"ds_read_b64_tr_b16 v[100:101], v154 offset:8200\n"
"v_mfma_f32_16x16x32_bf16 v[80:83], v[148:151], v[104:107], v[80:83]\n"
"ds_read_b64_tr_b16 v[102:103], v154 offset:8712\n"
"v_mfma_f32_16x16x32_bf16 v[12:15], v[136:139], v[108:111], v[12:15]\n"
"ds_read_b128 v[124:127], v152 offset:18432\n"
"v_mfma_f32_16x16x32_bf16 v[36:39], v[140:143], v[108:111], v[36:39]\n"
"ds_read_b64_tr_b16 v[104:105], v155 offset:16384\n"
"v_mfma_f32_16x16x32_bf16 v[60:63], v[144:147], v[108:111], v[60:63]\n"
"ds_read_b64_tr_b16 v[106:107], v155 offset:16896\n"
"v_mfma_f32_16x16x32_bf16 v[84:87], v[148:151], v[108:111], v[84:87]\n"
"ds_read_b128 v[128:131], v152 offset:20480\n"
"v_mfma_f32_16x16x32_bf16 v[16:19], v[136:139], v[112:115], v[16:19]\n"
"ds_read_b64_tr_b16 v[108:109], v156 offset:16384\n"
"v_mfma_f32_16x16x32_bf16 v[40:43], v[140:143], v[112:115], v[40:43]\n"
"ds_read_b64_tr_b16 v[110:111], v156 offset:16896\n"
"v_mfma_f32_16x16x32_bf16 v[64:67], v[144:147], v[112:115], v[64:67]\n"
"ds_read_b128 v[132:135], v152 offset:22528\n"
"v_mfma_f32_16x16x32_bf16 v[88:91], v[148:151], v[112:115], v[88:91]\n"
"v_mfma_f32_16x16x32_bf16 v[20:23], v[136:139], v[116:119], v[20:23]\n"
"ds_read_b64_tr_b16 v[112:113], v157 offset:16384\n"
"v_mfma_f32_16x16x32_bf16 v[44:47], v[140:143], v[116:119], v[44:47]\n"
"ds_read_b64_tr_b16 v[114:115], v157 offset:16896\n"
"v_mfma_f32_16x16x32_bf16 v[68:71], v[144:147], v[116:119], v[68:71]\n"
"v_mfma_f32_16x16x32_bf16 v[92:95], v[148:151], v[116:119], v[92:95]\n"
"s_waitcnt lgkmcnt(2)\n"
"v_mfma_f32_16x16x32_bf16 v[0:3], v[96:99], v[120:123], v[0:3]\n"
"ds_read_b64_tr_b16 v[116:117], v158 offset:16384\n"
"v_mfma_f32_16x16x32_bf16 v[24:27], v[96:99], v[124:127], v[24:27]\n"
"ds_read_b64_tr_b16 v[118:119], v158 offset:16896\n"
"v_mfma_f32_16x16x32_bf16 v[48:51], v[96:99], v[128:131], v[48:51]\n"
"v_mfma_f32_16x16x32_bf16 v[72:75], v[96:99], v[132:135], v[72:75]\n"
"v_mfma_f32_16x16x32_bf16 v[4:7], v[100:103], v[120:123], v[4:7]\n"
"ds_read_b64_tr_b16 v[96:97], v154 offset:12288\n"
"v_mfma_f32_16x16x32_bf16 v[28:31], v[100:103], v[124:127], v[28:31]\n"
"ds_read_b64_tr_b16 v[98:99], v154 offset:12800\n"
"v_mfma_f32_16x16x32_bf16 v[52:55], v[100:103], v[128:131], v[52:55]\n"
"ds_read_b128 v[136:139], v153 offset:16384\n"
"v_mfma_f32_16x16x32_bf16 v[76:79], v[100:103], v[132:135], v[76:79]\n"
"v_mfma_f32_16x16x32_bf16 v[8:11], v[120:123], v[104:107], v[8:11]\n"
"ds_read_b64_tr_b16 v[100:101], v154 offset:12296\n"
"v_mfma_f32_16x16x32_bf16 v[32:35], v[124:127], v[104:107], v[32:35]\n"
"ds_read_b64_tr_b16 v[102:103], v154 offset:12808\n"
"v_mfma_f32_16x16x32_bf16 v[56:59], v[128:131], v[104:107], v[56:59]\n"
"ds_read_b128 v[140:143], v153 offset:18432\n"
"v_mfma_f32_16x16x32_bf16 v[80:83], v[132:135], v[104:107], v[80:83]\n"
"v_mfma_f32_16x16x32_bf16 v[12:15], v[120:123], v[108:111], v[12:15]\n"
"ds_read_b64_tr_b16 v[104:105], v155 offset:24576\n"
"v_mfma_f32_16x16x32_bf16 v[36:39], v[124:127], v[108:111], v[36:39]\n"
"ds_read_b64_tr_b16 v[106:107], v155 offset:25088\n"
"v_mfma_f32_16x16x32_bf16 v[60:63], v[128:131], v[108:111], v[60:63]\n"
"ds_read_b128 v[144:147], v153 offset:20480\n"
"v_mfma_f32_16x16x32_bf16 v[84:87], v[132:135], v[108:111], v[84:87]\n"
"s_waitcnt lgkmcnt(11)\n"
"v_mfma_f32_16x16x32_bf16 v[16:19], v[120:123], v[112:115], v[16:19]\n"
"ds_read_b64_tr_b16 v[108:109], v156 offset:24576\n"
"v_mfma_f32_16x16x32_bf16 v[40:43], v[124:127], v[112:115], v[40:43]\n"
"ds_read_b64_tr_b16 v[110:111], v156 offset:25088\n"
"v_mfma_f32_16x16x32_bf16 v[64:67], v[128:131], v[112:115], v[64:67]\n"
"ds_read_b128 v[148:151], v153 offset:22528\n"
"v_mfma_f32_16x16x32_bf16 v[88:91], v[132:135], v[112:115], v[88:91]\n"
"s_waitcnt lgkmcnt(12)\n"
"v_mfma_f32_16x16x32_bf16 v[20:23], v[120:123], v[116:119], v[20:23]\n"
"ds_read_b64_tr_b16 v[112:113], v157 offset:24576\n"
"v_mfma_f32_16x16x32_bf16 v[44:47], v[124:127], v[116:119], v[44:47]\n"
"ds_read_b64_tr_b16 v[114:115], v157 offset:25088\n"
"v_mfma_f32_16x16x32_bf16 v[68:71], v[128:131], v[116:119], v[68:71]\n"
"v_mfma_f32_16x16x32_bf16 v[92:95], v[132:135], v[116:119], v[92:95]\n"
"s_waitcnt lgkmcnt(2)\n"
"v_mfma_f32_16x16x32_bf16 v[0:3], v[96:99], v[136:139], v[0:3]\n"
"ds_read_b64_tr_b16 v[116:117], v158 offset:24576\n"
"v_mfma_f32_16x16x32_bf16 v[24:27], v[96:99], v[140:143], v[24:27]\n"
"ds_read_b64_tr_b16 v[118:119], v158 offset:25088\n"
"v_mfma_f32_16x16x32_bf16 v[48:51], v[96:99], v[144:147], v[48:51]\n"
"v_mfma_f32_16x16x32_bf16 v[72:75], v[96:99], v[148:151], v[72:75]\n"
"v_mfma_f32_16x16x32_bf16 v[4:7], v[100:103], v[136:139], v[4:7]\n"
"v_mfma_f32_16x16x32_bf16 v[28:31], v[100:103], v[140:143], v[28:31]\n"
"v_mfma_f32_16x16x32_bf16 v[52:55], v[100:103], v[144:147], v[52:55]\n"
"v_mfma_f32_16x16x32_bf16 v[76:79], v[100:103], v[148:151], v[76:79]\n"
"s_waitcnt lgkmcnt(0)\n"
"s_barrier\n"
"ds_read_b64_tr_b16 v[96:97], v154 offset:16384\n"
"v_mfma_f32_16x16x32_bf16 v[8:11], v[136:139], v[104:107], v[8:11]\n"
"ds_read_b64_tr_b16 v[98:99], v154 offset:16896\n"
"v_mfma_f32_16x16x32_bf16 v[32:35], v[140:143], v[104:107], v[32:35]\n"
"ds_read_b128 v[120:123], v152 offset:32768\n"
"v_mfma_f32_16x16x32_bf16 v[56:59], v[144:147], v[104:107], v[56:59]\n"
"ds_read_b64_tr_b16 v[100:101], v154 offset:16392\n"
"v_mfma_f32_16x16x32_bf16 v[80:83], v[148:151], v[104:107], v[80:83]\n"
"ds_read_b64_tr_b16 v[102:103], v154 offset:16904\n"
"v_mfma_f32_16x16x32_bf16 v[12:15], v[136:139], v[108:111], v[12:15]\n"
"ds_read_b128 v[124:127], v152 offset:34816\n"
"v_mfma_f32_16x16x32_bf16 v[36:39], v[140:143], v[108:111], v[36:39]\n"
"ds_read_b64_tr_b16 v[104:105], v155 offset:32768\n"
"v_mfma_f32_16x16x32_bf16 v[60:63], v[144:147], v[108:111], v[60:63]\n"
"ds_read_b64_tr_b16 v[106:107], v155 offset:33280\n"
"v_mfma_f32_16x16x32_bf16 v[84:87], v[148:151], v[108:111], v[84:87]\n"
"ds_read_b128 v[128:131], v152 offset:36864\n"
"v_mfma_f32_16x16x32_bf16 v[16:19], v[136:139], v[112:115], v[16:19]\n"
"ds_read_b64_tr_b16 v[108:109], v156 offset:32768\n"
"v_mfma_f32_16x16x32_bf16 v[40:43], v[140:143], v[112:115], v[40:43]\n"
"ds_read_b64_tr_b16 v[110:111], v156 offset:33280\n"
"v_mfma_f32_16x16x32_bf16 v[64:67], v[144:147], v[112:115], v[64:67]\n"
"ds_read_b128 v[132:135], v152 offset:38912\n"
"v_mfma_f32_16x16x32_bf16 v[88:91], v[148:151], v[112:115], v[88:91]\n"
"v_mfma_f32_16x16x32_bf16 v[20:23], v[136:139], v[116:119], v[20:23]\n"
"ds_read_b64_tr_b16 v[112:113], v157 offset:32768\n"
"v_mfma_f32_16x16x32_bf16 v[44:47], v[140:143], v[116:119], v[44:47]\n"
"ds_read_b64_tr_b16 v[114:115], v157 offset:33280\n"
"v_mfma_f32_16x16x32_bf16 v[68:71], v[144:147], v[116:119], v[68:71]\n"
"v_mfma_f32_16x16x32_bf16 v[92:95], v[148:151], v[116:119], v[92:95]\n"
"s_waitcnt lgkmcnt(2)\n"
"v_mfma_f32_16x16x32_bf16 v[0:3], v[96:99], v[120:123], v[0:3]\n"
"ds_read_b64_tr_b16 v[116:117], v158 offset:32768\n"
"v_mfma_f32_16x16x32_bf16 v[24:27], v[96:99], v[124:127], v[24:27]\n"
"ds_read_b64_tr_b16 v[118:119], v158 offset:33280\n"
"v_mfma_f32_16x16x32_bf16 v[48:51], v[96:99], v[128:131], v[48:51]\n"
"v_mfma_f32_16x16x32_bf16 v[72:75], v[96:99], v[132:135], v[72:75]\n"
"v_mfma_f32_16x16x32_bf16 v[4:7], v[100:103], v[120:123], v[4:7]\n"
"ds_read_b64_tr_b16 v[96:97], v154 offset:20480\n"
"v_mfma_f32_16x16x32_bf16 v[28:31], v[100:103], v[124:127], v[28:31]\n"
"ds_read_b64_tr_b16 v[98:99], v154 offset:20992\n"
"v_mfma_f32_16x16x32_bf16 v[52:55], v[100:103], v[128:131], v[52:55]\n"
"ds_read_b128 v[136:139], v153 offset:32768\n"
"v_mfma_f32_16x16x32_bf16 v[76:79], v[100:103], v[132:135], v[76:79]\n"
"v_mfma_f32_16x16x32_bf16 v[8:11], v[120:123], v[104:107], v[8:11]\n"
"ds_read_b64_tr_b16 v[100:101], v154 offset:20488\n"
"v_mfma_f32_16x16x32_bf16 v[32:35], v[124:127], v[104:107], v[32:35]\n"
"ds_read_b64_tr_b16 v[102:103], v154 offset:21000\n"
"v_mfma_f32_16x16x32_bf16 v[56:59], v[128:131], v[104:107], v[56:59]\n"
"ds_read_b128 v[140:143], v153 offset:34816\n"
"v_mfma_f32_16x16x32_bf16 v[80:83], v[132:135], v[104:107], v[80:83]\n"
"v_mfma_f32_16x16x32_bf16 v[12:15], v[120:123], v[108:111], v[12:15]\n"
"ds_read_b64_tr_b16 v[104:105], v155 offset:40960\n"
"v_mfma_f32_16x16x32_bf16 v[36:39], v[124:127], v[108:111], v[36:39]\n"
"ds_read_b64_tr_b16 v[106:107], v155 offset:41472\n"
"v_mfma_f32_16x16x32_bf16 v[60:63], v[128:131], v[108:111], v[60:63]\n"
"ds_read_b128 v[144:147], v153 offset:36864\n"
"v_mfma_f32_16x16x32_bf16 v[84:87], v[132:135], v[108:111], v[84:87]\n"
"s_waitcnt lgkmcnt(11)\n"
"v_mfma_f32_16x16x32_bf16 v[16:19], v[120:123], v[112:115], v[16:19]\n"
"ds_read_b64_tr_b16 v[108:109], v156 offset:40960\n"
"v_mfma_f32_16x16x32_bf16 v[40:43], v[124:127], v[112:115], v[40:43]\n"
"ds_read_b64_tr_b16 v[110:111], v156 offset:41472\n"
"v_mfma_f32_16x16x32_bf16 v[64:67], v[128:131], v[112:115], v[64:67]\n"
"ds_read_b128 v[148:151], v153 offset:38912\n"
"v_mfma_f32_16x16x32_bf16 v[88:91], v[132:135], v[112:115], v[88:91]\n"
"s_waitcnt lgkmcnt(12)\n"
"v_mfma_f32_16x16x32_bf16 v[20:23], v[120:123], v[116:119], v[20:23]\n"
"ds_read_b64_tr_b16 v[112:113], v157 offset:40960\n"
"v_mfma_f32_16x16x32_bf16 v[44:47], v[124:127], v[116:119], v[44:47]\n"
"ds_read_b64_tr_b16 v[114:115], v157 offset:41472\n"
"v_mfma_f32_16x16x32_bf16 v[68:71], v[128:131], v[116:119], v[68:71]\n"
"v_mfma_f32_16x16x32_bf16 v[92:95], v[132:135], v[116:119], v[92:95]\n"
"s_waitcnt lgkmcnt(2)\n"
"v_mfma_f32_16x16x32_bf16 v[0:3], v[96:99], v[136:139], v[0:3]\n"
"ds_read_b64_tr_b16 v[116:117], v158 offset:40960\n"
"v_mfma_f32_16x16x32_bf16 v[24:27], v[96:99], v[140:143], v[24:27]\n"
"ds_read_b64_tr_b16 v[118:119], v158 offset:41472\n"
"v_mfma_f32_16x16x32_bf16 v[48:51], v[96:99], v[144:147], v[48:51]\n"
"v_mfma_f32_16x16x32_bf16 v[72:75], v[96:99], v[148:151], v[72:75]\n"
"v_mfma_f32_16x16x32_bf16 v[4:7], v[100:103], v[136:139], v[4:7]\n"
"v_mfma_f32_16x16x32_bf16 v[28:31], v[100:103], v[140:143], v[28:31]\n"
"v_mfma_f32_16x16x32_bf16 v[52:55], v[100:103], v[144:147], v[52:55]\n"
"v_mfma_f32_16x16x32_bf16 v[76:79], v[100:103], v[148:151], v[76:79]\n"
"s_waitcnt lgkmcnt(0)\n"
"s_barrier\n"
"ds_read_b64_tr_b16 v[96:97], v154 offset:0\n"
"v_mfma_f32_16x16x32_bf16 v[8:11], v[136:139], v[104:107], v[8:11]\n"
"ds_read_b64_tr_b16 v[98:99], v154 offset:512\n"
"v_mfma_f32_16x16x32_bf16 v[32:35], v[140:143], v[104:107], v[32:35]\n"
"ds_read_b128 v[120:123], v152 offset:0\n"
"v_mfma_f32_16x16x32_bf16 v[56:59], v[144:147], v[104:107], v[56:59]\n"
"ds_read_b64_tr_b16 v[100:101], v154 offset:8\n"
"v_mfma_f32_16x16x32_bf16 v[80:83], v[148:151], v[104:107], v[80:83]\n"
"ds_read_b64_tr_b16 v[102:103], v154 offset:520\n"
"v_mfma_f32_16x16x32_bf16 v[12:15], v[136:139], v[108:111], v[12:15]\n"
"ds_read_b128 v[124:127], v152 offset:2048\n"
"v_mfma_f32_16x16x32_bf16 v[36:39], v[140:143], v[108:111], v[36:39]\n"
"ds_read_b64_tr_b16 v[104:105], v155 offset:0\n"
"v_mfma_f32_16x16x32_bf16 v[60:63], v[144:147], v[108:111], v[60:63]\n"
"ds_read_b64_tr_b16 v[106:107], v155 offset:512\n"
"v_mfma_f32_16x16x32_bf16 v[84:87], v[148:151], v[108:111], v[84:87]\n"
"ds_read_b128 v[128:131], v152 offset:4096\n"
"v_mfma_f32_16x16x32_bf16 v[16:19], v[136:139], v[112:115], v[16:19]\n"
"ds_read_b64_tr_b16 v[108:109], v156 offset:0\n"
"v_mfma_f32_16x16x32_bf16 v[40:43], v[140:143], v[112:115], v[40:43]\n"
"ds_read_b64_tr_b16 v[110:111], v156 offset:512\n"
"v_mfma_f32_16x16x32_bf16 v[64:67], v[144:147], v[112:115], v[64:67]\n"
"ds_read_b128 v[132:135], v152 offset:6144\n"
"v_mfma_f32_16x16x32_bf16 v[88:91], v[148:151], v[112:115], v[88:91]\n"
"v_mfma_f32_16x16x32_bf16 v[20:23], v[136:139], v[116:119], v[20:23]\n"
"ds_read_b64_tr_b16 v[112:113], v157 offset:0\n"
"v_mfma_f32_16x16x32_bf16 v[44:47], v[140:143], v[116:119], v[44:47]\n"
"ds_read_b64_tr_b16 v[114:115], v157 offset:512\n"
"v_mfma_f32_16x16x32_bf16 v[68:71], v[144:147], v[116:119], v[68:71]\n"
"v_mfma_f32_16x16x32_bf16 v[92:95], v[148:151], v[116:119], v[92:95]\n"
"s_waitcnt lgkmcnt(2)\n"
"v_mfma_f32_16x16x32_bf16 v[0:3], v[96:99], v[120:123], v[0:3]\n"
"ds_read_b64_tr_b16 v[116:117], v158 offset:0\n"
"v_mfma_f32_16x16x32_bf16 v[24:27], v[96:99], v[124:127], v[24:27]\n"
"ds_read_b64_tr_b16 v[118:119], v158 offset:512\n"
"v_mfma_f32_16x16x32_bf16 v[48:51], v[96:99], v[128:131], v[48:51]\n"
"v_mfma_f32_16x16x32_bf16 v[72:75], v[96:99], v[132:135], v[72:75]\n"
"v_mfma_f32_16x16x32_bf16 v[4:7], v[100:103], v[120:123], v[4:7]\n"
"ds_read_b64_tr_b16 v[96:97], v154 offset:4096\n"
"v_mfma_f32_16x16x32_bf16 v[28:31], v[100:103], v[124:127], v[28:31]\n"
"ds_read_b64_tr_b16 v[98:99], v154 offset:4608\n"
"v_mfma_f32_16x16x32_bf16 v[52:55], v[100:103], v[128:131], v[52:55]\n"
"ds_read_b128 v[136:139], v153 offset:0\n"
"v_mfma_f32_16x16x32_bf16 v[76:79], v[100:103], v[132:135], v[76:79]\n"
"v_mfma_f32_16x16x32_bf16 v[8:11], v[120:123], v[104:107], v[8:11]\n"
"ds_read_b64_tr_b16 v[100:101], v154 offset:4104\n"
"v_mfma_f32_16x16x32_bf16 v[32:35], v[124:127], v[104:107], v[32:35]\n"
"ds_read_b64_tr_b16 v[102:103], v154 offset:4616\n"
"v_mfma_f32_16x16x32_bf16 v[56:59], v[128:131], v[104:107], v[56:59]\n"
"ds_read_b128 v[140:143], v153 offset:2048\n"
"v_mfma_f32_16x16x32_bf16 v[80:83], v[132:135], v[104:107], v[80:83]\n"
"v_mfma_f32_16x16x32_bf16 v[12:15], v[120:123], v[108:111], v[12:15]\n"
"ds_read_b64_tr_b16 v[104:105], v155 offset:8192\n"
"v_mfma_f32_16x16x32_bf16 v[36:39], v[124:127], v[108:111], v[36:39]\n"
"ds_read_b64_tr_b16 v[106:107], v155 offset:8704\n"
"v_mfma_f32_16x16x32_bf16 v[60:63], v[128:131], v[108:111], v[60:63]\n"
"ds_read_b128 v[144:147], v153 offset:4096\n"
"v_mfma_f32_16x16x32_bf16 v[84:87], v[132:135], v[108:111], v[84:87]\n"
"s_waitcnt lgkmcnt(11)\n"
"v_mfma_f32_16x16x32_bf16 v[16:19], v[120:123], v[112:115], v[16:19]\n"
"ds_read_b64_tr_b16 v[108:109], v156 offset:8192\n"
"v_mfma_f32_16x16x32_bf16 v[40:43], v[124:127], v[112:115], v[40:43]\n"
"ds_read_b64_tr_b16 v[110:111], v156 offset:8704\n"
"v_mfma_f32_16x16x32_bf16 v[64:67], v[128:131], v[112:115], v[64:67]\n"
"ds_read_b128 v[148:151], v153 offset:6144\n"
"v_mfma_f32_16x16x32_bf16 v[88:91], v[132:135], v[112:115], v[88:91]\n"
"s_waitcnt lgkmcnt(12)\n"
"v_mfma_f32_16x16x32_bf16 v[20:23], v[120:123], v[116:119], v[20:23]\n"
"ds_read_b64_tr_b16 v[112:113], v157 offset:8192\n"
"v_mfma_f32_16x16x32_bf16 v[44:47], v[124:127], v[116:119], v[44:47]\n"
"ds_read_b64_tr_b16 v[114:115], v157 offset:8704\n"
"v_mfma_f32_16x16x32_bf16 v[68:71], v[128:131], v[116:119], v[68:71]\n"
"v_mfma_f32_16x16x32_bf16 v[92:95], v[132:135], v[116:119], v[92:95]\n"
"s_waitcnt lgkmcnt(2)\n"
"v_mfma_f32_16x16x32_bf16 v[0:3], v[96:99], v[136:139], v[0:3]\n"
"ds_read_b64_tr_b16 v[116:117], v158 offset:8192\n"
"v_mfma_f32_16x16x32_bf16 v[24:27], v[96:99], v[140:143], v[24:27]\n"
"ds_read_b64_tr_b16 v[118:119], v158 offset:8704\n"
"v_mfma_f32_16x16x32_bf16 v[48:51], v[96:99], v[144:147], v[48:51]\n"
"v_mfma_f32_16x16x32_bf16 v[72:75], v[96:99], v[148:151], v[72:75]\n"
"v_mfma_f32_16x16x32_bf16 v[4:7], v[100:103], v[136:139], v[4:7]\n"
"v_mfma_f32_16x16x32_bf16 v[28:31], v[100:103], v[140:143], v[28:31]\n"
"v_mfma_f32_16x16x32_bf16 v[52:55], v[100:103], v[144:147], v[52:55]\n"
"v_mfma_f32_16x16x32_bf16 v[76:79], v[100:103], v[148:151], v[76:79]\n"
"s_waitcnt lgkmcnt(0)\n"
"s_barrier\n"
"ds_read_b64_tr_b16 v[96:97], v154 offset:8192\n"
"v_mfma_f32_16x16x32_bf16 v[8:11], v[136:139], v[104:107], v[8:11]\n"
"ds_read_b64_tr_b16 v[98:99], v154 offset:8704\n"
"v_mfma_f32_16x16x32_bf16 v[32:35], v[140:143], v[104:107], v[32:35]\n"
"ds_read_b128 v[120:123], v152 offset:16384\n"
"v_mfma_f32_16x16x32_bf16 v[56:59], v[144:147], v[104:107], v[56:59]\n"
"ds_read_b64_tr_b16 v[100:101], v154 offset:8200\n"
"v_mfma_f32_16x16x32_bf16 v[80:83], v[148:151], v[104:107], v[80:83]\n"
"ds_read_b64_tr_b16 v[102:103], v154 offset:8712\n"
"v_mfma_f32_16x16x32_bf16 v[12:15], v[136:139], v[108:111], v[12:15]\n"
"ds_read_b128 v[124:127], v152 offset:18432\n"
"v_mfma_f32_16x16x32_bf16 v[36:39], v[140:143], v[108:111], v[36:39]\n"
"ds_read_b64_tr_b16 v[104:105], v155 offset:16384\n"
"v_mfma_f32_16x16x32_bf16 v[60:63], v[144:147], v[108:111], v[60:63]\n"
"ds_read_b64_tr_b16 v[106:107], v155 offset:16896\n"
"v_mfma_f32_16x16x32_bf16 v[84:87], v[148:151], v[108:111], v[84:87]\n"
"ds_read_b128 v[128:131], v152 offset:20480\n"
"v_mfma_f32_16x16x32_bf16 v[16:19], v[136:139], v[112:115], v[16:19]\n"
"ds_read_b64_tr_b16 v[108:109], v156 offset:16384\n"
"v_mfma_f32_16x16x32_bf16 v[40:43], v[140:143], v[112:115], v[40:43]\n"
"ds_read_b64_tr_b16 v[110:111], v156 offset:16896\n"
"v_mfma_f32_16x16x32_bf16 v[64:67], v[144:147], v[112:115], v[64:67]\n"
"ds_read_b128 v[132:135], v152 offset:22528\n"
"v_mfma_f32_16x16x32_bf16 v[88:91], v[148:151], v[112:115], v[88:91]\n"
"v_mfma_f32_16x16x32_bf16 v[20:23], v[136:139], v[116:119], v[20:23]\n"
"ds_read_b64_tr_b16 v[112:113], v157 offset:16384\n"
"v_mfma_f32_16x16x32_bf16 v[44:47], v[140:143], v[116:119], v[44:47]\n"
"ds_read_b64_tr_b16 v[114:115], v157 offset:16896\n"
"v_mfma_f32_16x16x32_bf16 v[68:71], v[144:147], v[116:119], v[68:71]\n"
"v_mfma_f32_16x16x32_bf16 v[92:95], v[148:151], v[116:119], v[92:95]\n"
"s_waitcnt lgkmcnt(2)\n"
"v_mfma_f32_16x16x32_bf16 v[0:3], v[96:99], v[120:123], v[0:3]\n"
"ds_read_b64_tr_b16 v[116:117], v158 offset:16384\n"
"v_mfma_f32_16x16x32_bf16 v[24:27], v[96:99], v[124:127], v[24:27]\n"
"ds_read_b64_tr_b16 v[118:119], v158 offset:16896\n"
"v_mfma_f32_16x16x32_bf16 v[48:51], v[96:99], v[128:131], v[48:51]\n"
"v_mfma_f32_16x16x32_bf16 v[72:75], v[96:99], v[132:135], v[72:75]\n"
"v_mfma_f32_16x16x32_bf16 v[4:7], v[100:103], v[120:123], v[4:7]\n"
"ds_read_b64_tr_b16 v[96:97], v154 offset:12288\n"
"v_mfma_f32_16x16x32_bf16 v[28:31], v[100:103], v[124:127], v[28:31]\n"
"ds_read_b64_tr_b16 v[98:99], v154 offset:12800\n"
"v_mfma_f32_16x16x32_bf16 v[52:55], v[100:103], v[128:131], v[52:55]\n"
"ds_read_b128 v[136:139], v153 offset:16384\n"
"v_mfma_f32_16x16x32_bf16 v[76:79], v[100:103], v[132:135], v[76:79]\n"
"v_mfma_f32_16x16x32_bf16 v[8:11], v[120:123], v[104:107], v[8:11]\n"
"ds_read_b64_tr_b16 v[100:101], v154 offset:12296\n"
"v_mfma_f32_16x16x32_bf16 v[32:35], v[124:127], v[104:107], v[32:35]\n"
"ds_read_b64_tr_b16 v[102:103], v154 offset:12808\n"
"v_mfma_f32_16x16x32_bf16 v[56:59], v[128:131], v[104:107], v[56:59]\n"
"ds_read_b128 v[140:143], v153 offset:18432\n"
"v_mfma_f32_16x16x32_bf16 v[80:83], v[132:135], v[104:107], v[80:83]\n"
"v_mfma_f32_16x16x32_bf16 v[12:15], v[120:123], v[108:111], v[12:15]\n"
"ds_read_b64_tr_b16 v[104:105], v155 offset:24576\n"
"v_mfma_f32_16x16x32_bf16 v[36:39], v[124:127], v[108:111], v[36:39]\n"
"ds_read_b64_tr_b16 v[106:107], v155 offset:25088\n"
"v_mfma_f32_16x16x32_bf16 v[60:63], v[128:131], v[108:111], v[60:63]\n"
"ds_read_b128 v[144:147], v153 offset:20480\n"
"v_mfma_f32_16x16x32_bf16 v[84:87], v[132:135], v[108:111], v[84:87]\n"
"s_waitcnt lgkmcnt(11)\n"
"v_mfma_f32_16x16x32_bf16 v[16:19], v[120:123], v[112:115], v[16:19]\n"
"ds_read_b64_tr_b16 v[108:109], v156 offset:24576\n"
"v_mfma_f32_16x16x32_bf16 v[40:43], v[124:127], v[112:115], v[40:43]\n"
"ds_read_b64_tr_b16 v[110:111], v156 offset:25088\n"
"v_mfma_f32_16x16x32_bf16 v[64:67], v[128:131], v[112:115], v[64:67]\n"
"ds_read_b128 v[148:151], v153 offset:22528\n"
"v_mfma_f32_16x16x32_bf16 v[88:91], v[132:135], v[112:115], v[88:91]\n"
"s_waitcnt lgkmcnt(12)\n"
"v_mfma_f32_16x16x32_bf16 v[20:23], v[120:123], v[116:119], v[20:23]\n"
"ds_read_b64_tr_b16 v[112:113], v157 offset:24576\n"
"v_mfma_f32_16x16x32_bf16 v[44:47], v[124:127], v[116:119], v[44:47]\n"
"ds_read_b64_tr_b16 v[114:115], v157 offset:25088\n"
"v_mfma_f32_16x16x32_bf16 v[68:71], v[128:131], v[116:119], v[68:71]\n"
"v_mfma_f32_16x16x32_bf16 v[92:95], v[132:135], v[116:119], v[92:95]\n"
"s_waitcnt lgkmcnt(2)\n"
"v_mfma_f32_16x16x32_bf16 v[0:3], v[96:99], v[136:139], v[0:3]\n"
"ds_read_b64_tr_b16 v[116:117], v158 offset:24576\n"
"v_mfma_f32_16x16x32_bf16 v[24:27], v[96:99], v[140:143], v[24:27]\n"
"ds_read_b64_tr_b16 v[118:119], v158 offset:25088\n"
"v_mfma_f32_16x16x32_bf16 v[48:51], v[96:99], v[144:147], v[48:51]\n"
"v_mfma_f32_16x16x32_bf16 v[72:75], v[96:99], v[148:151], v[72:75]\n"
"v_mfma_f32_16x16x32_bf16 v[4:7], v[100:103], v[136:139], v[4:7]\n"
"v_mfma_f32_16x16x32_bf16 v[28:31], v[100:103], v[140:143], v[28:31]\n"
"v_mfma_f32_16x16x32_bf16 v[52:55], v[100:103], v[144:147], v[52:55]\n"
"v_mfma_f32_16x16x32_bf16 v[76:79], v[100:103], v[148:151], v[76:79]\n"
"s_waitcnt lgkmcnt(0)\n"
"s_barrier\n"
"ds_read_b64_tr_b16 v[96:97], v154 offset:16384\n"
"v_mfma_f32_16x16x32_bf16 v[8:11], v[136:139], v[104:107], v[8:11]\n"
"ds_read_b64_tr_b16 v[98:99], v154 offset:16896\n"
"v_mfma_f32_16x16x32_bf16 v[32:35], v[140:143], v[104:107], v[32:35]\n"
"ds_read_b128 v[120:123], v152 offset:32768\n"
"v_mfma_f32_16x16x32_bf16 v[56:59], v[144:147], v[104:107], v[56:59]\n"
"ds_read_b64_tr_b16 v[100:101], v154 offset:16392\n"
"v_mfma_f32_16x16x32_bf16 v[80:83], v[148:151], v[104:107], v[80:83]\n"
"ds_read_b64_tr_b16 v[102:103], v154 offset:16904\n"
"v_mfma_f32_16x16x32_bf16 v[12:15], v[136:139], v[108:111], v[12:15]\n"
"ds_read_b128 v[124:127], v152 offset:34816\n"
"v_mfma_f32_16x16x32_bf16 v[36:39], v[140:143], v[108:111], v[36:39]\n"
"ds_read_b64_tr_b16 v[104:105], v155 offset:32768\n"
"v_mfma_f32_16x16x32_bf16 v[60:63], v[144:147], v[108:111], v[60:63]\n"
"ds_read_b64_tr_b16 v[106:107], v155 offset:33280\n"
"v_mfma_f32_16x16x32_bf16 v[84:87], v[148:151], v[108:111], v[84:87]\n"
"ds_read_b128 v[128:131], v152 offset:36864\n"
"v_mfma_f32_16x16x32_bf16 v[16:19], v[136:139], v[112:115], v[16:19]\n"
"ds_read_b64_tr_b16 v[108:109], v156 offset:32768\n"
"v_mfma_f32_16x16x32_bf16 v[40:43], v[140:143], v[112:115], v[40:43]\n"
"ds_read_b64_tr_b16 v[110:111], v156 offset:33280\n"
"v_mfma_f32_16x16x32_bf16 v[64:67], v[144:147], v[112:115], v[64:67]\n"
"ds_read_b128 v[132:135], v152 offset:38912\n"
"v_mfma_f32_16x16x32_bf16 v[88:91], v[148:151], v[112:115], v[88:91]\n"
"v_mfma_f32_16x16x32_bf16 v[20:23], v[136:139], v[116:119], v[20:23]\n"
"ds_read_b64_tr_b16 v[112:113], v157 offset:32768\n"
"v_mfma_f32_16x16x32_bf16 v[44:47], v[140:143], v[116:119], v[44:47]\n"
"ds_read_b64_tr_b16 v[114:115], v157 offset:33280\n"
"v_mfma_f32_16x16x32_bf16 v[68:71], v[144:147], v[116:119], v[68:71]\n"
"v_mfma_f32_16x16x32_bf16 v[92:95], v[148:151], v[116:119], v[92:95]\n"
"s_waitcnt lgkmcnt(2)\n"
"v_mfma_f32_16x16x32_bf16 v[0:3], v[96:99], v[120:123], v[0:3]\n"
"ds_read_b64_tr_b16 v[116:117], v158 offset:32768\n"
"v_mfma_f32_16x16x32_bf16 v[24:27], v[96:99], v[124:127], v[24:27]\n"
"ds_read_b64_tr_b16 v[118:119], v158 offset:33280\n"
"v_mfma_f32_16x16x32_bf16 v[48:51], v[96:99], v[128:131], v[48:51]\n"
"v_mfma_f32_16x16x32_bf16 v[72:75], v[96:99], v[132:135], v[72:75]\n"
"v_mfma_f32_16x16x32_bf16 v[4:7], v[100:103], v[120:123], v[4:7]\n"
"ds_read_b64_tr_b16 v[96:97], v154 offset:20480\n"
"v_mfma_f32_16x16x32_bf16 v[28:31], v[100:103], v[124:127], v[28:31]\n"
"ds_read_b64_tr_b16 v[98:99], v154 offset:20992\n"
"v_mfma_f32_16x16x32_bf16 v[52:55], v[100:103], v[128:131], v[52:55]\n"
"ds_read_b128 v[136:139], v153 offset:32768\n"
"v_mfma_f32_16x16x32_bf16 v[76:79], v[100:103], v[132:135], v[76:79]\n"
"v_mfma_f32_16x16x32_bf16 v[8:11], v[120:123], v[104:107], v[8:11]\n"
"ds_read_b64_tr_b16 v[100:101], v154 offset:20488\n"
"v_mfma_f32_16x16x32_bf16 v[32:35], v[124:127], v[104:107], v[32:35]\n"
"ds_read_b64_tr_b16 v[102:103], v154 offset:21000\n"
"v_mfma_f32_16x16x32_bf16 v[56:59], v[128:131], v[104:107], v[56:59]\n"
"ds_read_b128 v[140:143], v153 offset:34816\n"
"v_mfma_f32_16x16x32_bf16 v[80:83], v[132:135], v[104:107], v[80:83]\n"
"v_mfma_f32_16x16x32_bf16 v[12:15], v[120:123], v[108:111], v[12:15]\n"
"ds_read_b64_tr_b16 v[104:105], v155 offset:40960\n"
"v_mfma_f32_16x16x32_bf16 v[36:39], v[124:127], v[108:111], v[36:39]\n"
"ds_read_b64_tr_b16 v[106:107], v155 offset:41472\n"
"v_mfma_f32_16x16x32_bf16 v[60:63], v[128:131], v[108:111], v[60:63]\n"
"ds_read_b128 v[144:147], v153 offset:36864\n"
"v_mfma_f32_16x16x32_bf16 v[84:87], v[132:135], v[108:111], v[84:87]\n"
"s_waitcnt lgkmcnt(11)\n"
"v_mfma_f32_16x16x32_bf16 v[16:19], v[120:123], v[112:115], v[16:19]\n"
"ds_read_b64_tr_b16 v[108:109], v156 offset:40960\n"
"v_mfma_f32_16x16x32_bf16 v[40:43], v[124:127], v[112:115], v[40:43]\n"
"ds_read_b64_tr_b16 v[110:111], v156 offset:41472\n"
"v_mfma_f32_16x16x32_bf16 v[64:67], v[128:131], v[112:115], v[64:67]\n"
"ds_read_b128 v[148:151], v153 offset:38912\n"
"v_mfma_f32_16x16x32_bf16 v[88:91], v[132:135], v[112:115], v[88:91]\n"
"s_waitcnt lgkmcnt(12)\n"
"v_mfma_f32_16x16x32_bf16 v[20:23], v[120:123], v[116:119], v[20:23]\n"
"ds_read_b64_tr_b16 v[112:113], v157 offset:40960\n"
"v_mfma_f32_16x16x32_bf16 v[44:47], v[124:127], v[116:119], v[44:47]\n"
"ds_read_b64_tr_b16 v[114:115], v157 offset:41472\n"
"v_mfma_f32_16x16x32_bf16 v[68:71], v[128:131], v[116:119], v[68:71]\n"
"v_mfma_f32_16x16x32_bf16 v[92:95], v[132:135], v[116:119], v[92:95]\n"
"s_waitcnt lgkmcnt(2)\n"
"v_mfma_f32_16x16x32_bf16 v[0:3], v[96:99], v[136:139], v[0:3]\n"
"ds_read_b64_tr_b16 v[116:117], v158 offset:40960\n"
"v_mfma_f32_16x16x32_bf16 v[24:27], v[96:99], v[140:143], v[24:27]\n"
"ds_read_b64_tr_b16 v[118:119], v158 offset:41472\n"
"v_mfma_f32_16x16x32_bf16 v[48:51], v[96:99], v[144:147], v[48:51]\n"
"v_mfma_f32_16x16x32_bf16 v[72:75], v[96:99], v[148:151], v[72:75]\n"
"v_mfma_f32_16x16x32_bf16 v[4:7], v[100:103], v[136:139], v[4:7]\n"
"v_mfma_f32_16x16x32_bf16 v[28:31], v[100:103], v[140:143], v[28:31]\n"
"v_mfma_f32_16x16x32_bf16 v[52:55], v[100:103], v[144:147], v[52:55]\n"
"v_mfma_f32_16x16x32_bf16 v[76:79], v[100:103], v[148:151], v[76:79]\n"
"s_waitcnt lgkmcnt(0)\n"
"s_barrier\n"
"ds_read_b64_tr_b16 v[96:97], v154 offset:0\n"
"v_mfma_f32_16x16x32_bf16 v[8:11], v[136:139], v[104:107], v[8:11]\n"
"ds_read_b64_tr_b16 v[98:99], v154 offset:512\n"
"v_mfma_f32_16x16x32_bf16 v[32:35], v[140:143], v[104:107], v[32:35]\n"
"ds_read_b128 v[120:123], v152 offset:0\n"
"v_mfma_f32_16x16x32_bf16 v[56:59], v[144:147], v[104:107], v[56:59]\n"
"ds_read_b64_tr_b16 v[100:101], v154 offset:8\n"
"v_mfma_f32_16x16x32_bf16 v[80:83], v[148:151], v[104:107], v[80:83]\n"
"ds_read_b64_tr_b16 v[102:103], v154 offset:520\n"
"v_mfma_f32_16x16x32_bf16 v[12:15], v[136:139], v[108:111], v[12:15]\n"
"ds_read_b128 v[124:127], v152 offset:2048\n"
"v_mfma_f32_16x16x32_bf16 v[36:39], v[140:143], v[108:111], v[36:39]\n"
"ds_read_b64_tr_b16 v[104:105], v155 offset:0\n"
"v_mfma_f32_16x16x32_bf16 v[60:63], v[144:147], v[108:111], v[60:63]\n"
"ds_read_b64_tr_b16 v[106:107], v155 offset:512\n"
"v_mfma_f32_16x16x32_bf16 v[84:87], v[148:151], v[108:111], v[84:87]\n"
"ds_read_b128 v[128:131], v152 offset:4096\n"
"v_mfma_f32_16x16x32_bf16 v[16:19], v[136:139], v[112:115], v[16:19]\n"
"ds_read_b64_tr_b16 v[108:109], v156 offset:0\n"
"v_mfma_f32_16x16x32_bf16 v[40:43], v[140:143], v[112:115], v[40:43]\n"
"ds_read_b64_tr_b16 v[110:111], v156 offset:512\n"
"v_mfma_f32_16x16x32_bf16 v[64:67], v[144:147], v[112:115], v[64:67]\n"
"ds_read_b128 v[132:135], v152 offset:6144\n"
"v_mfma_f32_16x16x32_bf16 v[88:91], v[148:151], v[112:115], v[88:91]\n"
"v_mfma_f32_16x16x32_bf16 v[20:23], v[136:139], v[116:119], v[20:23]\n"
"ds_read_b64_tr_b16 v[112:113], v157 offset:0\n"
"v_mfma_f32_16x16x32_bf16 v[44:47], v[140:143], v[116:119], v[44:47]\n"
"ds_read_b64_tr_b16 v[114:115], v157 offset:512\n"
"v_mfma_f32_16x16x32_bf16 v[68:71], v[144:147], v[116:119], v[68:71]\n"
"v_mfma_f32_16x16x32_bf16 v[92:95], v[148:151], v[116:119], v[92:95]\n"
"s_waitcnt lgkmcnt(2)\n"
"v_mfma_f32_16x16x32_bf16 v[0:3], v[96:99], v[120:123], v[0:3]\n"
"ds_read_b64_tr_b16 v[116:117], v158 offset:0\n"
"v_mfma_f32_16x16x32_bf16 v[24:27], v[96:99], v[124:127], v[24:27]\n"
"ds_read_b64_tr_b16 v[118:119], v158 offset:512\n"
"v_mfma_f32_16x16x32_bf16 v[48:51], v[96:99], v[128:131], v[48:51]\n"
"v_mfma_f32_16x16x32_bf16 v[72:75], v[96:99], v[132:135], v[72:75]\n"
"v_mfma_f32_16x16x32_bf16 v[4:7], v[100:103], v[120:123], v[4:7]\n"
"ds_read_b64_tr_b16 v[96:97], v154 offset:4096\n"
"v_mfma_f32_16x16x32_bf16 v[28:31], v[100:103], v[124:127], v[28:31]\n"
"ds_read_b64_tr_b16 v[98:99], v154 offset:4608\n"
"v_mfma_f32_16x16x32_bf16 v[52:55], v[100:103], v[128:131], v[52:55]\n"
"ds_read_b128 v[136:139], v153 offset:0\n"
"v_mfma_f32_16x16x32_bf16 v[76:79], v[100:103], v[132:135], v[76:79]\n"
"v_mfma_f32_16x16x32_bf16 v[8:11], v[120:123], v[104:107], v[8:11]\n"
"ds_read_b64_tr_b16 v[100:101], v154 offset:4104\n"
"v_mfma_f32_16x16x32_bf16 v[32:35], v[124:127], v[104:107], v[32:35]\n"
"ds_read_b64_tr_b16 v[102:103], v154 offset:4616\n"
"v_mfma_f32_16x16x32_bf16 v[56:59], v[128:131], v[104:107], v[56:59]\n"
"ds_read_b128 v[140:143], v153 offset:2048\n"
"v_mfma_f32_16x16x32_bf16 v[80:83], v[132:135], v[104:107], v[80:83]\n"
"v_mfma_f32_16x16x32_bf16 v[12:15], v[120:123], v[108:111], v[12:15]\n"
"ds_read_b64_tr_b16 v[104:105], v155 offset:8192\n"
"v_mfma_f32_16x16x32_bf16 v[36:39], v[124:127], v[108:111], v[36:39]\n"
"ds_read_b64_tr_b16 v[106:107], v155 offset:8704\n"
"v_mfma_f32_16x16x32_bf16 v[60:63], v[128:131], v[108:111], v[60:63]\n"
"ds_read_b128 v[144:147], v153 offset:4096\n"
"v_mfma_f32_16x16x32_bf16 v[84:87], v[132:135], v[108:111], v[84:87]\n"
"s_waitcnt lgkmcnt(11)\n"
"v_mfma_f32_16x16x32_bf16 v[16:19], v[120:123], v[112:115], v[16:19]\n"
"ds_read_b64_tr_b16 v[108:109], v156 offset:8192\n"
"v_mfma_f32_16x16x32_bf16 v[40:43], v[124:127], v[112:115], v[40:43]\n"
"ds_read_b64_tr_b16 v[110:111], v156 offset:8704\n"
"v_mfma_f32_16x16x32_bf16 v[64:67], v[128:131], v[112:115], v[64:67]\n"
"ds_read_b128 v[148:151], v153 offset:6144\n"
"v_mfma_f32_16x16x32_bf16 v[88:91], v[132:135], v[112:115], v[88:91]\n"
"s_waitcnt lgkmcnt(12)\n"
"v_mfma_f32_16x16x32_bf16 v[20:23], v[120:123], v[116:119], v[20:23]\n"
"ds_read_b64_tr_b16 v[112:113], v157 offset:8192\n"
"v_mfma_f32_16x16x32_bf16 v[44:47], v[124:127], v[116:119], v[44:47]\n"
"ds_read_b64_tr_b16 v[114:115], v157 offset:8704\n"
"v_mfma_f32_16x16x32_bf16 v[68:71], v[128:131], v[116:119], v[68:71]\n"
"v_mfma_f32_16x16x32_bf16 v[92:95], v[132:135], v[116:119], v[92:95]\n"
"s_waitcnt lgkmcnt(2)\n"
"v_mfma_f32_16x16x32_bf16 v[0:3], v[96:99], v[136:139], v[0:3]\n"
"ds_read_b64_tr_b16 v[116:117], v158 offset:8192\n"
"v_mfma_f32_16x16x32_bf16 v[24:27], v[96:99], v[140:143], v[24:27]\n"
"ds_read_b64_tr_b16 v[118:119], v158 offset:8704\n"
"v_mfma_f32_16x16x32_bf16 v[48:51], v[96:99], v[144:147], v[48:51]\n"
"v_mfma_f32_16x16x32_bf16 v[72:75], v[96:99], v[148:151], v[72:75]\n"
"s_mov_b64 exec, 1\n"
"ds_add_u32 v251, v252 offset:72\n"
"s_mov_b64 exec, -1\n"
"ds_read_b32 v240, v251 offset:60\n"
"v_mfma_f32_16x16x32_bf16 v[4:7], v[100:103], v[136:139], v[4:7]\n"
"v_mfma_f32_16x16x32_bf16 v[28:31], v[100:103], v[140:143], v[28:31]\n"
"v_mfma_f32_16x16x32_bf16 v[52:55], v[100:103], v[144:147], v[52:55]\n"
"v_mfma_f32_16x16x32_bf16 v[76:79], v[100:103], v[148:151], v[76:79]\n"
"s_waitcnt lgkmcnt(0)\n"
"v_cmp_gt_u32 vcc, 4, v240\n"
"s_nop 0\n"
"s_cbranch_vccnz Lcs63_%=\n"
"Lcb64_%=:\n"
"ds_read2_b32 v[212:213], v165 offset1:16\n"
"v_mfma_f32_16x16x32_bf16 v[8:11], v[136:139], v[104:107], v[8:11]\n"
"ds_read2_b32 v[214:215], v165 offset0:32 offset1:48\n"
"v_mfma_f32_16x16x32_bf16 v[32:35], v[140:143], v[104:107], v[32:35]\n"
"ds_read2_b32 v[216:217], v165 offset0:128 offset1:144\n"
"v_mfma_f32_16x16x32_bf16 v[56:59], v[144:147], v[104:107], v[56:59]\n"
"ds_read2_b32 v[218:219], v165 offset0:160 offset1:176\n"
"v_mfma_f32_16x16x32_bf16 v[80:83], v[148:151], v[104:107], v[80:83]\n"
"ds_read_b128 v[170:173], v166 offset:0\n"
"v_mfma_f32_16x16x32_bf16 v[12:15], v[136:139], v[108:111], v[12:15]\n"
"ds_read_b128 v[174:177], v166 offset:16\n"
"v_mfma_f32_16x16x32_bf16 v[36:39], v[140:143], v[108:111], v[36:39]\n"
"ds_read2_b32 v[220:221], v167 offset0:0 offset1:16\n"
"v_mfma_f32_16x16x32_bf16 v[60:63], v[144:147], v[108:111], v[60:63]\n"
"ds_read2_b32 v[222:223], v167 offset0:32 offset1:48\n"
"v_mfma_f32_16x16x32_bf16 v[84:87], v[148:151], v[108:111], v[84:87]\n"
"ds_read_b128 v[178:181], v168 offset:0\n"
"v_mfma_f32_16x16x32_bf16 v[16:19], v[136:139], v[112:115], v[16:19]\n"
"ds_read_b128 v[182:185], v168 offset:64\n"
"v_mfma_f32_16x16x32_bf16 v[40:43], v[140:143], v[112:115], v[40:43]\n"
"ds_read_b128 v[186:189], v168 offset:128\n"
"v_mfma_f32_16x16x32_bf16 v[64:67], v[144:147], v[112:115], v[64:67]\n"
"ds_read_b128 v[190:193], v168 offset:192\n"
"v_mfma_f32_16x16x32_bf16 v[88:91], v[148:151], v[112:115], v[88:91]\n"
"ds_read_b128 v[194:197], v168 offset:512\n"
"v_mfma_f32_16x16x32_bf16 v[20:23], v[136:139], v[116:119], v[20:23]\n"
"ds_read_b128 v[198:201], v168 offset:576\n"
"v_mfma_f32_16x16x32_bf16 v[44:47], v[140:143], v[116:119], v[44:47]\n"
"ds_read_b128 v[202:205], v168 offset:640\n"
"v_mfma_f32_16x16x32_bf16 v[68:71], v[144:147], v[116:119], v[68:71]\n"
"ds_read_b128 v[206:209], v168 offset:704\n"
"v_mfma_f32_16x16x32_bf16 v[92:95], v[148:151], v[116:119], v[92:95]\n"
"s_waitcnt lgkmcnt(10)\n"
"v_mul_f32 v240, s13, v216\n"
"v_mul_f32 v241, s13, v217\n"
"v_mul_f32 v242, s13, v218\n"
"v_mul_f32 v243, s13, v219\n"
"v_mov_b32 v244, 0x3f80\n"
"v_fmac_f32 v0, v212, v170\n"
"v_fmac_f32 v1, v212, v171\n"
"v_fmac_f32 v2, v212, v172\n"
"v_fmac_f32 v3, v212, v173\n"
"v_fmac_f32 v4, v212, v174\n"
"v_fmac_f32 v5, v212, v175\n"
"v_fmac_f32 v6, v212, v176\n"
"v_fmac_f32 v7, v212, v177\n"
"v_mul_f32 v0, v240, v0\n"
"v_mul_f32 v1, v240, v1\n"
"v_mul_f32 v2, v240, v2\n"
"v_mul_f32 v3, v240, v3\n"
"v_mul_f32 v4, v240, v4\n"
"v_mul_f32 v5, v240, v5\n"
"v_mul_f32 v6, v240, v6\n"
"v_mul_f32 v7, v240, v7\n"
"v_cvt_pk_bf16_f32 v100, v4, v5\n"
"v_cvt_pk_bf16_f32 v101, v6, v7\n"
"v_cvt_pk_bf16_f32 v96, v0, v1\n"
"v_cvt_pk_bf16_f32 v97, v2, v3\n"
"v_cndmask_b32_e64 v98, v100, v244, s[4:5]\n"
"v_cndmask_b32_e64 v99, v101, 0, s[4:5]\n"
"buffer_store_dwordx4 v[96:99], v159, s[48:51], 0 offen offset:0 sc1\n"
"v_fmac_f32 v24, v213, v170\n"
"v_fmac_f32 v25, v213, v171\n"
"v_fmac_f32 v26, v213, v172\n"
"v_fmac_f32 v27, v213, v173\n"
"v_fmac_f32 v28, v213, v174\n"
"v_fmac_f32 v29, v213, v175\n"
"v_fmac_f32 v30, v213, v176\n"
"v_fmac_f32 v31, v213, v177\n"
"v_mul_f32 v24, v241, v24\n"
"v_mul_f32 v25, v241, v25\n"
"v_mul_f32 v26, v241, v26\n"
"v_mul_f32 v27, v241, v27\n"
"v_mul_f32 v28, v241, v28\n"
"v_mul_f32 v29, v241, v29\n"
"v_mul_f32 v30, v241, v30\n"
"v_mul_f32 v31, v241, v31\n"
"v_cvt_pk_bf16_f32 v108, v28, v29\n"
"v_cvt_pk_bf16_f32 v109, v30, v31\n"
"v_cvt_pk_bf16_f32 v104, v24, v25\n"
"v_cvt_pk_bf16_f32 v105, v26, v27\n"
"v_cndmask_b32_e64 v106, v108, v244, s[4:5]\n"
"v_cndmask_b32_e64 v107, v109, 0, s[4:5]\n"
"buffer_store_dwordx4 v[104:107], v159, s[48:51], 0 offen offset:256 sc1\n"
"v_fmac_f32 v48, v214, v170\n"
"v_fmac_f32 v49, v214, v171\n"
"v_fmac_f32 v50, v214, v172\n"
"v_fmac_f32 v51, v214, v173\n"
"v_fmac_f32 v52, v214, v174\n"
"v_fmac_f32 v53, v214, v175\n"
"v_fmac_f32 v54, v214, v176\n"
"v_fmac_f32 v55, v214, v177\n"
"v_mul_f32 v48, v242, v48\n"
"v_mul_f32 v49, v242, v49\n"
"v_mul_f32 v50, v242, v50\n"
"v_mul_f32 v51, v242, v51\n"
"v_mul_f32 v52, v242, v52\n"
"v_mul_f32 v53, v242, v53\n"
"v_mul_f32 v54, v242, v54\n"
"v_mul_f32 v55, v242, v55\n"
"v_cvt_pk_bf16_f32 v116, v52, v53\n"
"v_cvt_pk_bf16_f32 v117, v54, v55\n"
"v_cvt_pk_bf16_f32 v112, v48, v49\n"
"v_cvt_pk_bf16_f32 v113, v50, v51\n"
"v_cndmask_b32_e64 v114, v116, v244, s[4:5]\n"
"v_cndmask_b32_e64 v115, v117, 0, s[4:5]\n"
"buffer_store_dwordx4 v[112:115], v159, s[48:51], s11 offen offset:0 sc1\n"
"v_fmac_f32 v72, v215, v170\n"
"v_fmac_f32 v73, v215, v171\n"
"v_fmac_f32 v74, v215, v172\n"
"v_fmac_f32 v75, v215, v173\n"
"v_fmac_f32 v76, v215, v174\n"
"v_fmac_f32 v77, v215, v175\n"
"v_fmac_f32 v78, v215, v176\n"
"v_fmac_f32 v79, v215, v177\n"
"v_mul_f32 v72, v243, v72\n"
"v_mul_f32 v73, v243, v73\n"
"v_mul_f32 v74, v243, v74\n"
"v_mul_f32 v75, v243, v75\n"
"v_mul_f32 v76, v243, v76\n"
"v_mul_f32 v77, v243, v77\n"
"v_mul_f32 v78, v243, v78\n"
"v_mul_f32 v79, v243, v79\n"
"v_cvt_pk_bf16_f32 v124, v76, v77\n"
"v_cvt_pk_bf16_f32 v125, v78, v79\n"
"v_cvt_pk_bf16_f32 v120, v72, v73\n"
"v_cvt_pk_bf16_f32 v121, v74, v75\n"
"v_cndmask_b32_e64 v122, v124, v244, s[4:5]\n"
"v_cndmask_b32_e64 v123, v125, 0, s[4:5]\n"
"s_mov_b64 exec, s[4:5]\n"
"s_cbranch_execz Lq66_%=\n"
"v_mov_b32 v102, v244\n"
"v_mov_b32 v103, 0\n"
"buffer_store_dwordx4 v[100:103], v160, s[48:51], 0 offen offset:0 sc1\n"
"v_mov_b32 v110, v244\n"
"v_mov_b32 v111, 0\n"
"buffer_store_dwordx4 v[108:111], v160, s[48:51], 0 offen offset:256 sc1\n"
"v_mov_b32 v118, v244\n"
"v_mov_b32 v119, 0\n"
"buffer_store_dwordx4 v[116:119], v160, s[48:51], s11 offen offset:0 sc1\n"
"v_mov_b32 v126, v244\n"
"v_mov_b32 v127, 0\n"
"buffer_store_dwordx4 v[124:127], v160, s[48:51], s11 offen offset:256 sc1\n"
"Lq66_%=:\n"
"s_mov_b64 exec, -1\n"
"buffer_store_dwordx4 v[120:123], v159, s[48:51], s11 offen offset:256 sc1\n"
"s_waitcnt lgkmcnt(0)\n"
"v_fmac_f32 v8, v220, v178\n"
"v_fmac_f32 v9, v220, v179\n"
"v_fmac_f32 v10, v220, v180\n"
"v_fmac_f32 v11, v220, v181\n"
"v_fmac_f32 v32, v220, v182\n"
"v_fmac_f32 v33, v220, v183\n"
"v_fmac_f32 v34, v220, v184\n"
"v_fmac_f32 v35, v220, v185\n"
"v_mul_f32 v8, v8, v194\n"
"v_mul_f32 v9, v9, v195\n"
"v_mul_f32 v10, v10, v196\n"
"v_mul_f32 v11, v11, v197\n"
"v_mul_f32 v32, v32, v198\n"
"v_mul_f32 v33, v33, v199\n"
"v_mul_f32 v34, v34, v200\n"
"v_mul_f32 v35, v35, v201\n"
"v_cvt_pk_bf16_f32 v128, v8, v9\n"
"v_cvt_pk_bf16_f32 v129, v10, v11\n"
"v_cvt_pk_bf16_f32 v130, v32, v33\n"
"v_cvt_pk_bf16_f32 v131, v34, v35\n"
"v_fmac_f32 v12, v221, v178\n"
"v_fmac_f32 v13, v221, v179\n"
"v_fmac_f32 v14, v221, v180\n"
"v_fmac_f32 v15, v221, v181\n"
"v_fmac_f32 v36, v221, v182\n"
"v_fmac_f32 v37, v221, v183\n"
"v_fmac_f32 v38, v221, v184\n"
"v_fmac_f32 v39, v221, v185\n"
"v_permlane16_swap_b32 v128, v130\n"
"v_permlane16_swap_b32 v129, v131\n"
"buffer_store_dwordx4 v[128:131], v161, s[48:51], 0 offen offset:0 sc1\n"
"v_mul_f32 v12, v12, v194\n"
"v_mul_f32 v13, v13, v195\n"
"v_mul_f32 v14, v14, v196\n"
"v_mul_f32 v15, v15, v197\n"
"v_mul_f32 v36, v36, v198\n"
"v_mul_f32 v37, v37, v199\n"
"v_mul_f32 v38, v38, v200\n"
"v_mul_f32 v39, v39, v201\n"
"v_cvt_pk_bf16_f32 v132, v12, v13\n"
"v_cvt_pk_bf16_f32 v133, v14, v15\n"
"v_cvt_pk_bf16_f32 v134, v36, v37\n"
"v_cvt_pk_bf16_f32 v135, v38, v39\n"
"v_fmac_f32 v16, v222, v178\n"
"v_fmac_f32 v17, v222, v179\n"
"v_fmac_f32 v18, v222, v180\n"
"v_fmac_f32 v19, v222, v181\n"
"v_fmac_f32 v40, v222, v182\n"
"v_fmac_f32 v41, v222, v183\n"
"v_fmac_f32 v42, v222, v184\n"
"v_fmac_f32 v43, v222, v185\n"
"v_permlane16_swap_b32 v132, v134\n"
"v_permlane16_swap_b32 v133, v135\n"
"buffer_store_dwordx4 v[132:135], v162, s[48:51], 0 offen offset:0 sc1\n"
"v_mul_f32 v16, v16, v194\n"
"v_mul_f32 v17, v17, v195\n"
"v_mul_f32 v18, v18, v196\n"
"v_mul_f32 v19, v19, v197\n"
"v_mul_f32 v40, v40, v198\n"
"v_mul_f32 v41, v41, v199\n"
"v_mul_f32 v42, v42, v200\n"
"v_mul_f32 v43, v43, v201\n"
"v_cvt_pk_bf16_f32 v136, v16, v17\n"
"v_cvt_pk_bf16_f32 v137, v18, v19\n"
"v_cvt_pk_bf16_f32 v138, v40, v41\n"
"v_cvt_pk_bf16_f32 v139, v42, v43\n"
"v_fmac_f32 v20, v223, v178\n"
"v_fmac_f32 v21, v223, v179\n"
"v_fmac_f32 v22, v223, v180\n"
"v_fmac_f32 v23, v223, v181\n"
"v_fmac_f32 v44, v223, v182\n"
"v_fmac_f32 v45, v223, v183\n"
"v_fmac_f32 v46, v223, v184\n"
"v_fmac_f32 v47, v223, v185\n"
"v_permlane16_swap_b32 v136, v138\n"
"v_permlane16_swap_b32 v137, v139\n"
"buffer_store_dwordx4 v[136:139], v163, s[48:51], 0 offen offset:0 sc1\n"
"v_mul_f32 v20, v20, v194\n"
"v_mul_f32 v21, v21, v195\n"
"v_mul_f32 v22, v22, v196\n"
"v_mul_f32 v23, v23, v197\n"
"v_mul_f32 v44, v44, v198\n"
"v_mul_f32 v45, v45, v199\n"
"v_mul_f32 v46, v46, v200\n"
"v_mul_f32 v47, v47, v201\n"
"v_cvt_pk_bf16_f32 v128, v20, v21\n"
"v_cvt_pk_bf16_f32 v129, v22, v23\n"
"v_cvt_pk_bf16_f32 v130, v44, v45\n"
"v_cvt_pk_bf16_f32 v131, v46, v47\n"
"v_fmac_f32 v56, v220, v186\n"
"v_fmac_f32 v57, v220, v187\n"
"v_fmac_f32 v58, v220, v188\n"
"v_fmac_f32 v59, v220, v189\n"
"v_fmac_f32 v80, v220, v190\n"
"v_fmac_f32 v81, v220, v191\n"
"v_fmac_f32 v82, v220, v192\n"
"v_fmac_f32 v83, v220, v193\n"
"v_permlane16_swap_b32 v128, v130\n"
"v_permlane16_swap_b32 v129, v131\n"
"buffer_store_dwordx4 v[128:131], v164, s[48:51], 0 offen offset:0 sc1\n"
"v_mul_f32 v56, v56, v202\n"
"v_mul_f32 v57, v57, v203\n"
"v_mul_f32 v58, v58, v204\n"
"v_mul_f32 v59, v59, v205\n"
"v_mul_f32 v80, v80, v206\n"
"v_mul_f32 v81, v81, v207\n"
"v_mul_f32 v82, v82, v208\n"
"v_mul_f32 v83, v83, v209\n"
"v_cvt_pk_bf16_f32 v132, v56, v57\n"
"v_cvt_pk_bf16_f32 v133, v58, v59\n"
"v_cvt_pk_bf16_f32 v134, v80, v81\n"
"v_cvt_pk_bf16_f32 v135, v82, v83\n"
"v_fmac_f32 v60, v221, v186\n"
"v_fmac_f32 v61, v221, v187\n"
"v_fmac_f32 v62, v221, v188\n"
"v_fmac_f32 v63, v221, v189\n"
"v_fmac_f32 v84, v221, v190\n"
"v_fmac_f32 v85, v221, v191\n"
"v_fmac_f32 v86, v221, v192\n"
"v_fmac_f32 v87, v221, v193\n"
"v_permlane16_swap_b32 v132, v134\n"
"v_permlane16_swap_b32 v133, v135\n"
"buffer_store_dwordx4 v[132:135], v161, s[48:51], s11 offen offset:0 sc1\n"
"v_mul_f32 v60, v60, v202\n"
"v_mul_f32 v61, v61, v203\n"
"v_mul_f32 v62, v62, v204\n"
"v_mul_f32 v63, v63, v205\n"
"v_mul_f32 v84, v84, v206\n"
"v_mul_f32 v85, v85, v207\n"
"v_mul_f32 v86, v86, v208\n"
"v_mul_f32 v87, v87, v209\n"
"v_cvt_pk_bf16_f32 v136, v60, v61\n"
"v_cvt_pk_bf16_f32 v137, v62, v63\n"
"v_cvt_pk_bf16_f32 v138, v84, v85\n"
"v_cvt_pk_bf16_f32 v139, v86, v87\n"
"s_waitcnt vmcnt(5)\n"
"s_mov_b64 exec, 1\n"
"ds_add_rtn_u32 v244, v251, v252 offset:12\n"
"s_waitcnt lgkmcnt(0)\n"
"v_cmp_ne_u32 vcc, 3, v244\n"
"s_nop 0\n"
"s_cbranch_vccnz Llf67_%=\n"
"v_mov_b32 v244, s71\n"
"buffer_store_dword v244, off, s[48:51], s14 sc1\n"
"Llf67_%=:\n"
"s_mov_b64 exec, -1\n"
"v_fmac_f32 v64, v222, v186\n"
"v_fmac_f32 v65, v222, v187\n"
"v_fmac_f32 v66, v222, v188\n"
"v_fmac_f32 v67, v222, v189\n"
"v_fmac_f32 v88, v222, v190\n"
"v_fmac_f32 v89, v222, v191\n"
"v_fmac_f32 v90, v222, v192\n"
"v_fmac_f32 v91, v222, v193\n"
"v_permlane16_swap_b32 v136, v138\n"
"v_permlane16_swap_b32 v137, v139\n"
"buffer_store_dwordx4 v[136:139], v162, s[48:51], s11 offen offset:0 sc1\n"
"v_mul_f32 v64, v64, v202\n"
"v_mul_f32 v65, v65, v203\n"
"v_mul_f32 v66, v66, v204\n"
"v_mul_f32 v67, v67, v205\n"
"v_mul_f32 v88, v88, v206\n"
"v_mul_f32 v89, v89, v207\n"
"v_mul_f32 v90, v90, v208\n"
"v_mul_f32 v91, v91, v209\n"
"v_cvt_pk_bf16_f32 v128, v64, v65\n"
"v_cvt_pk_bf16_f32 v129, v66, v67\n"
"v_cvt_pk_bf16_f32 v130, v88, v89\n"
"v_cvt_pk_bf16_f32 v131, v90, v91\n"
"v_fmac_f32 v68, v223, v186\n"
"v_fmac_f32 v69, v223, v187\n"
"v_fmac_f32 v70, v223, v188\n"
"v_fmac_f32 v71, v223, v189\n"
"v_fmac_f32 v92, v223, v190\n"
"v_fmac_f32 v93, v223, v191\n"
"v_fmac_f32 v94, v223, v192\n"
"v_fmac_f32 v95, v223, v193\n"
"v_permlane16_swap_b32 v128, v130\n"
"v_permlane16_swap_b32 v129, v131\n"
"buffer_store_dwordx4 v[128:131], v163, s[48:51], s11 offen offset:0 sc1\n"
"v_mul_f32 v68, v68, v202\n"
"v_mul_f32 v69, v69, v203\n"
"v_mul_f32 v70, v70, v204\n"
"v_mul_f32 v71, v71, v205\n"
"v_mul_f32 v92, v92, v206\n"
"v_mul_f32 v93, v93, v207\n"
"v_mul_f32 v94, v94, v208\n"
"v_mul_f32 v95, v95, v209\n"
"v_cvt_pk_bf16_f32 v132, v68, v69\n"
"v_cvt_pk_bf16_f32 v133, v70, v71\n"
"v_cvt_pk_bf16_f32 v134, v92, v93\n"
"v_cvt_pk_bf16_f32 v135, v94, v95\n"
"s_nop 0\n"
"v_permlane16_swap_b32 v132, v134\n"
"v_permlane16_swap_b32 v133, v135\n"
"buffer_store_dwordx4 v[132:135], v164, s[48:51], s11 offen offset:0 sc1\n"
"s_add_u32 s14, s14, 454656\n"
"s_waitcnt vmcnt(0)\n"
"s_mov_b64 exec, 1\n"
"ds_add_rtn_u32 v244, v251, v252 offset:52\n"
"s_waitcnt lgkmcnt(0)\n"
"v_cmp_ne_u32 vcc, 3, v244\n"
"s_nop 0\n"
"s_cbranch_vccnz Llf68_%=\n"
"v_mov_b32 v244, s71\n"
"buffer_store_dword v244, off, s[48:51], s14 sc1\n"
"Llf68_%=:\n"
"s_mov_b64 exec, -1\n"
"LATT53_%=:\n"
"s_setprio 2\n"
"s_mov_b32 s90, 400000\n"
"Lpl69_%=:\n"
"s_mov_b64 exec, 0x3ff\n"
"buffer_load_dword v240, v227, s[48:51], 0 offen sc1\n"
"s_waitcnt vmcnt(0)\n"
"v_cmp_ne_u32 vcc, s71, v240\n"
"s_mov_b64 exec, -1\n"
"s_sub_u32 s90, s90, 1\n"
"s_cbranch_scc1 Lpd70_%=\n"
"s_cbranch_vccz Lpd70_%=\n"
"s_sleep 1\n"
"s_branch Lpl69_%=\n"
"Lpd70_%=:\n"
"LATU54_%=:\n"
"buffer_load_dwordx4 v[136:139], v253, s[48:51], s19 offen offset:0 sc1\n"
"buffer_load_dwordx4 v[140:143], v253, s[48:51], s19 offen offset:1024 sc1\n"
"s_add_u32 s91, s18, 0\n"
"buffer_load_dwordx4 v[144:147], v249, s[48:51], s91 offen offset:0 sc1\n"
"buffer_load_dwordx4 v[148:151], v249, s[48:51], s91 offen offset:1024 sc1\n"
"s_add_u32 s91, s18, 4096\n"
"buffer_load_dwordx4 v[160:163], v249, s[48:51], s91 offen offset:0 sc1\n"
"buffer_load_dwordx4 v[164:167], v249, s[48:51], s91 offen offset:1024 sc1\n"
"s_add_u32 s91, s18, 8192\n"
"buffer_load_dwordx4 v[176:179], v249, s[48:51], s91 offen offset:0 sc1\n"
"buffer_load_dwordx4 v[180:183], v249, s[48:51], s91 offen offset:1024 sc1\n"
"s_add_u32 s91, s18, 12288\n"
"buffer_load_dwordx4 v[192:195], v249, s[48:51], s91 offen offset:0 sc1\n"
"buffer_load_dwordx4 v[196:199], v249, s[48:51], s91 offen offset:1024 sc1\n"
"v_mov_b32 v254, 0xdddddddd\n"
"ds_read_b32 v243, v251 offset:4\n"
"v_mov_b32 v112, 0\n"
"v_mov_b32 v113, 0\n"
"v_mov_b32 v114, 0\n"
"v_mov_b32 v115, 0\n"
"s_nop 1\n"
"v_mfma_f32_32x32x16_bf16 v[0:15], v[112:115], v[112:115], 0\n"
"v_mfma_f32_32x32x16_bf16 v[16:31], v[112:115], v[112:115], 0\n"
"v_mfma_f32_32x32x16_bf16 v[32:47], v[112:115], v[112:115], 0\n"
"v_mfma_f32_32x32x16_bf16 v[48:63], v[112:115], v[112:115], 0\n"
"s_waitcnt lgkmcnt(0)\n"
"v_cmp_gt_u32 vcc, 4, v243\n"
"s_nop 0\n"
"s_cbranch_vccnz Lcs71_%=\n"
"Lcb72_%=:\n"
"s_waitcnt vmcnt(6)\n"
"v_mfma_f32_32x32x16_bf16 v[64:79], v[144:147], v[136:139], 0\n"
"v_mfma_f32_32x32x16_bf16 v[64:79], v[148:151], v[140:143], v[64:79]\n"
"s_waitcnt vmcnt(4)\n"
"v_mfma_f32_32x32x16_bf16 v[80:95], v[160:163], v[136:139], 0\n"
"v_mfma_f32_32x32x16_bf16 v[80:95], v[164:167], v[140:143], v[80:95]\n"
"s_waitcnt vmcnt(2)\n"
"v_mfma_f32_32x32x16_bf16 v[96:111], v[176:179], v[136:139], 0\n"
"v_mfma_f32_32x32x16_bf16 v[96:111], v[180:183], v[140:143], v[96:111]\n"
"s_mov_b64 exec, 0xfff\n"
"buffer_load_dword v242, v228, s[48:51], 0 offen sc1\n"
"s_mov_b64 exec, -1\n"
"s_nop 2\n"
"v_max_f32 v240, v64, v65\n"
"v_max3_f32 v241, v66, v67, v68\n"
"v_max3_f32 v240, v240, v69, v70\n"
"v_max3_f32 v241, v241, v71, v72\n"
"v_max3_f32 v240, v240, v73, v74\n"
"v_max3_f32 v241, v241, v75, v76\n"
"v_max3_f32 v240, v240, v77, v78\n"
"v_max3_f32 v240, v240, v241, v79\n"
"v_max3_f32 v240, v80, v81, v240\n"
"v_max3_f32 v241, v82, v83, v84\n"
"v_max3_f32 v240, v240, v85, v86\n"
"v_max3_f32 v241, v241, v87, v88\n"
"v_max3_f32 v240, v240, v89, v90\n"
"v_max3_f32 v241, v241, v91, v92\n"
"v_max3_f32 v240, v240, v93, v94\n"
"v_max3_f32 v240, v240, v241, v95\n"
"v_max3_f32 v240, v96, v97, v240\n"
"v_max3_f32 v241, v98, v99, v100\n"
"v_max3_f32 v240, v240, v101, v102\n"
"v_max3_f32 v241, v241, v103, v104\n"
"v_max3_f32 v240, v240, v105, v106\n"
"v_max3_f32 v241, v241, v107, v108\n"
"v_max3_f32 v240, v240, v109, v110\n"
"v_max3_f32 v240, v240, v241, v111\n"
"v_mov_b32 v241, v240\n"
"ds_write_b128 v238, v[136:139] offset:0\n"
"s_nop 0\n"
"v_permlane32_swap_b32 v240, v241\n"
"v_max_f32 v240, v240, v241\n"
"v_sub_f32 v240, 0xc2c00000, v240\n"
"s_mov_b32 exec_lo, 0\n"
"v_cvt_pk_bf16_f32 v142, v240, 0\n"
"s_mov_b32 exec_lo, -1\n"
"ds_write_b128 v238, v[140:143] offset:1024\n"
"s_mov_b64 exec, 1\n"
"ds_add_u32 v251, v252 offset:36\n"
"s_mov_b64 exec, -1\n"
"s_waitcnt vmcnt(0)\n"
"s_mov_b64 exec, 0xfff\n"
"v_cmp_ne_u32 vcc, s71, v242\n"
"s_mov_b64 exec, -1\n"
"s_cbranch_vccnz Lns74_%=\n"
"Lnb75_%=:\n"
"s_add_u32 s91, s18, 0\n"
"buffer_load_dwordx4 v[152:155], v249, s[48:51], s91 offen offset:2048 sc1\n"
"buffer_load_dwordx4 v[156:159], v249, s[48:51], s91 offen offset:3072 sc1\n"
"s_add_u32 s91, s18, 4096\n"
"buffer_load_dwordx4 v[168:171], v249, s[48:51], s91 offen offset:2048 sc1\n"
"buffer_load_dwordx4 v[172:175], v249, s[48:51], s91 offen offset:3072 sc1\n"
"s_add_u32 s91, s18, 8192\n"
"buffer_load_dwordx4 v[184:187], v249, s[48:51], s91 offen offset:2048 sc1\n"
"buffer_load_dwordx4 v[188:191], v249, s[48:51], s91 offen offset:3072 sc1\n"
"s_add_u32 s91, s18, 12288\n"
"buffer_load_dwordx4 v[200:203], v249, s[48:51], s91 offen offset:2048 sc1\n"
"buffer_load_dwordx4 v[204:207], v249, s[48:51], s91 offen offset:3072 sc1\n"
"s_add_u32 s18, s18, 12288\n"
"ds_read_b32 v243, v251 offset:36\n"
"s_waitcnt lgkmcnt(0)\n"
"v_cmp_gt_u32 vcc, 4, v243\n"
"s_nop 0\n"
"s_cbranch_vccnz Lcs78_%=\n"
"Lcb79_%=:\n"
"ds_read_b128 v[112:115], v229 offset:0\n"
"ds_read_b128 v[116:119], v229 offset:1024\n"
"ds_read_b128 v[120:123], v230 offset:0\n"
"ds_read_b128 v[124:127], v230 offset:1024\n"
"ds_read_b128 v[128:131], v237 offset:0\n"
"ds_read_b128 v[132:135], v237 offset:1024\n"
"s_waitcnt lgkmcnt(0)\n"
"s_mov_b64 exec, 1\n"
"ds_add_u32 v251, v252 offset:56\n"
"s_mov_b64 exec, -1\n"
"s_setprio 0\n"
"v_mfma_f32_32x32x16_bf16 v[64:79], v[144:147], v[112:115], 0\n"
"v_mfma_f32_32x32x16_bf16 v[64:79], v[148:151], v[116:119], v[64:79]\n"
"v_mfma_f32_32x32x16_bf16 v[80:95], v[144:147], v[120:123], 0\n"
"s_nop 10\n"
"v_exp_f32 v64, v64\n"
"v_exp_f32 v65, v65\n"
"v_exp_f32 v66, v66\n"
"v_exp_f32 v67, v67\n"
"v_mfma_f32_32x32x16_bf16 v[80:95], v[148:151], v[124:127], v[80:95]\n"
"v_exp_f32 v68, v68\n"
"v_exp_f32 v69, v69\n"
"v_exp_f32 v70, v70\n"
"v_exp_f32 v71, v71\n"
"v_exp_f32 v72, v72\n"
"v_exp_f32 v73, v73\n"
"v_exp_f32 v74, v74\n"
"v_exp_f32 v75, v75\n"
"s_waitcnt vmcnt(6)\n"
"v_smfmac_f32_32x32x32_bf16 v[0:15], v[152:155], v[64:71], v254\n"
"v_exp_f32 v76, v76\n"
"v_exp_f32 v77, v77\n"
"v_exp_f32 v78, v78\n"
"v_exp_f32 v79, v79\n"
"v_mfma_f32_32x32x16_bf16 v[96:111], v[144:147], v[128:131], 0\n"
"v_exp_f32 v80, v80\n"
"v_exp_f32 v81, v81\n"
"v_exp_f32 v82, v82\n"
"v_exp_f32 v83, v83\n"
"v_mfma_f32_32x32x16_bf16 v[96:111], v[148:151], v[132:135], v[96:111]\n"
"v_exp_f32 v84, v84\n"
"v_exp_f32 v85, v85\n"
"v_exp_f32 v86, v86\n"
"v_exp_f32 v87, v87\n"
"v_smfmac_f32_32x32x32_bf16 v[0:15], v[156:159], v[72:79], v254\n"
"s_add_u32 s18, s18, 4096\n"
"s_and_b32 s18, s18, 0xffff\n"
"buffer_load_dwordx4 v[208:211], v249, s[48:51], s18 offen offset:0 sc1\n"
"buffer_load_dwordx4 v[212:215], v249, s[48:51], s18 offen offset:1024 sc1\n"
"buffer_load_dwordx4 v[216:219], v249, s[48:51], s18 offen offset:2048 sc1\n"
"buffer_load_dwordx4 v[220:223], v249, s[48:51], s18 offen offset:3072 sc1\n"
"v_exp_f32 v88, v88\n"
"v_exp_f32 v89, v89\n"
"v_exp_f32 v90, v90\n"
"v_exp_f32 v91, v91\n"
"v_smfmac_f32_32x32x32_bf16 v[16:31], v[152:155], v[80:87], v254\n"
"v_exp_f32 v92, v92\n"
"v_exp_f32 v93, v93\n"
"v_exp_f32 v94, v94\n"
"v_exp_f32 v95, v95\n"
"v_mfma_f32_32x32x16_bf16 v[64:79], v[144:147], v[136:139], 0\n"
"v_exp_f32 v96, v96\n"
"v_exp_f32 v97, v97\n"
"v_exp_f32 v98, v98\n"
"v_exp_f32 v99, v99\n"
"v_mfma_f32_32x32x16_bf16 v[64:79], v[148:151], v[140:143], v[64:79]\n"
"v_exp_f32 v100, v100\n"
"v_exp_f32 v101, v101\n"
"v_exp_f32 v102, v102\n"
"v_exp_f32 v103, v103\n"
"v_smfmac_f32_32x32x32_bf16 v[16:31], v[156:159], v[88:95], v254\n"
"s_add_u32 s18, s18, 4096\n"
"buffer_load_dwordx4 v[224:227], v249, s[48:51], s18 offen offset:0 sc1\n"
"buffer_load_dwordx4 v[228:231], v249, s[48:51], s18 offen offset:1024 sc1\n"
"buffer_load_dwordx4 v[232:235], v249, s[48:51], s18 offen offset:2048 sc1\n"
"buffer_load_dwordx4 v[236:239], v249, s[48:51], s18 offen offset:3072 sc1\n"
"v_exp_f32 v104, v104\n"
"v_exp_f32 v105, v105\n"
"v_exp_f32 v106, v106\n"
"v_exp_f32 v107, v107\n"
"v_smfmac_f32_32x32x32_bf16 v[32:47], v[152:155], v[96:103], v254\n"
"s_waitcnt vmcnt(12)\n"
"v_exp_f32 v108, v108\n"
"v_exp_f32 v109, v109\n"
"v_exp_f32 v110, v110\n"
"v_exp_f32 v111, v111\n"
"v_mfma_f32_32x32x16_bf16 v[80:95], v[160:163], v[112:115], 0\n"
"v_exp_f32 v64, v64\n"
"v_exp_f32 v65, v65\n"
"v_exp_f32 v66, v66\n"
"v_exp_f32 v67, v67\n"
"v_mfma_f32_32x32x16_bf16 v[80:95], v[164:167], v[116:119], v[80:95]\n"
"v_exp_f32 v68, v68\n"
"v_exp_f32 v69, v69\n"
"v_exp_f32 v70, v70\n"
"v_exp_f32 v71, v71\n"
"v_smfmac_f32_32x32x32_bf16 v[32:47], v[156:159], v[104:111], v254\n"
"v_exp_f32 v72, v72\n"
"v_exp_f32 v73, v73\n"
"v_exp_f32 v74, v74\n"
"v_exp_f32 v75, v75\n"
"v_smfmac_f32_32x32x32_bf16 v[48:63], v[152:155], v[64:71], v254\n"
"v_exp_f32 v76, v76\n"
"v_exp_f32 v77, v77\n"
"v_exp_f32 v78, v78\n"
"v_exp_f32 v79, v79\n"
"v_mfma_f32_32x32x16_bf16 v[96:111], v[160:163], v[120:123], 0\n"
"v_exp_f32 v80, v80\n"
"v_exp_f32 v81, v81\n"
"v_exp_f32 v82, v82\n"
"v_exp_f32 v83, v83\n"
"v_mfma_f32_32x32x16_bf16 v[96:111], v[164:167], v[124:127], v[96:111]\n"
"v_exp_f32 v84, v84\n"
"v_exp_f32 v85, v85\n"
"v_exp_f32 v86, v86\n"
"v_exp_f32 v87, v87\n"
"v_smfmac_f32_32x32x32_bf16 v[48:63], v[156:159], v[72:79], v254\n"
"s_add_u32 s18, s18, 4096\n"
"buffer_load_dwordx4 v[144:147], v249, s[48:51], s18 offen offset:0 sc1\n"
"buffer_load_dwordx4 v[148:151], v249, s[48:51], s18 offen offset:1024 sc1\n"
"buffer_load_dwordx4 v[152:155], v249, s[48:51], s18 offen offset:2048 sc1\n"
"buffer_load_dwordx4 v[156:159], v249, s[48:51], s18 offen offset:3072 sc1\n"
"v_exp_f32 v88, v88\n"
"v_exp_f32 v89, v89\n"
"v_exp_f32 v90, v90\n"
"v_exp_f32 v91, v91\n"
"v_smfmac_f32_32x32x32_bf16 v[0:15], v[168:171], v[80:87], v254\n"
"v_exp_f32 v92, v92\n"
"v_exp_f32 v93, v93\n"
"v_exp_f32 v94, v94\n"
"v_exp_f32 v95, v95\n"
"v_mfma_f32_32x32x16_bf16 v[64:79], v[160:163], v[128:131], 0\n"
"v_exp_f32 v96, v96\n"
"v_exp_f32 v97, v97\n"
"v_exp_f32 v98, v98\n"
"v_exp_f32 v99, v99\n"
"v_mfma_f32_32x32x16_bf16 v[64:79], v[164:167], v[132:135], v[64:79]\n"
"v_exp_f32 v100, v100\n"
"v_exp_f32 v101, v101\n"
"v_exp_f32 v102, v102\n"
"v_exp_f32 v103, v103\n"
"v_smfmac_f32_32x32x32_bf16 v[0:15], v[172:175], v[88:95], v254\n"
"v_exp_f32 v104, v104\n"
"v_exp_f32 v105, v105\n"
"v_exp_f32 v106, v106\n"
"v_exp_f32 v107, v107\n"
"v_smfmac_f32_32x32x32_bf16 v[16:31], v[168:171], v[96:103], v254\n"
"v_exp_f32 v108, v108\n"
"v_exp_f32 v109, v109\n"
"v_exp_f32 v110, v110\n"
"v_exp_f32 v111, v111\n"
"v_mfma_f32_32x32x16_bf16 v[80:95], v[160:163], v[136:139], 0\n"
"v_exp_f32 v64, v64\n"
"v_exp_f32 v65, v65\n"
"v_exp_f32 v66, v66\n"
"v_exp_f32 v67, v67\n"
"v_mfma_f32_32x32x16_bf16 v[80:95], v[164:167], v[140:143], v[80:95]\n"
"v_exp_f32 v68, v68\n"
"v_exp_f32 v69, v69\n"
"v_exp_f32 v70, v70\n"
"v_exp_f32 v71, v71\n"
"v_smfmac_f32_32x32x32_bf16 v[16:31], v[172:175], v[104:111], v254\n"
"v_exp_f32 v72, v72\n"
"v_exp_f32 v73, v73\n"
"v_exp_f32 v74, v74\n"
"v_exp_f32 v75, v75\n"
"v_smfmac_f32_32x32x32_bf16 v[32:47], v[168:171], v[64:71], v254\n"
"s_waitcnt vmcnt(14)\n"
"v_exp_f32 v76, v76\n"
"v_exp_f32 v77, v77\n"
"v_exp_f32 v78, v78\n"
"v_exp_f32 v79, v79\n"
"v_mfma_f32_32x32x16_bf16 v[96:111], v[176:179], v[112:115], 0\n"
"v_exp_f32 v80, v80\n"
"v_exp_f32 v81, v81\n"
"v_exp_f32 v82, v82\n"
"v_exp_f32 v83, v83\n"
"v_mfma_f32_32x32x16_bf16 v[96:111], v[180:183], v[116:119], v[96:111]\n"
"v_exp_f32 v84, v84\n"
"v_exp_f32 v85, v85\n"
"v_exp_f32 v86, v86\n"
"v_exp_f32 v87, v87\n"
"v_smfmac_f32_32x32x32_bf16 v[32:47], v[172:175], v[72:79], v254\n"
"v_exp_f32 v88, v88\n"
"v_exp_f32 v89, v89\n"
"v_exp_f32 v90, v90\n"
"v_exp_f32 v91, v91\n"
"v_smfmac_f32_32x32x32_bf16 v[48:63], v[168:171], v[80:87], v254\n"
"v_exp_f32 v92, v92\n"
"v_exp_f32 v93, v93\n"
"v_exp_f32 v94, v94\n"
"v_exp_f32 v95, v95\n"
"v_mfma_f32_32x32x16_bf16 v[64:79], v[176:179], v[120:123], 0\n"
"v_exp_f32 v96, v96\n"
"v_exp_f32 v97, v97\n"
"v_exp_f32 v98, v98\n"
"v_exp_f32 v99, v99\n"
"v_mfma_f32_32x32x16_bf16 v[64:79], v[180:183], v[124:127], v[64:79]\n"
"v_exp_f32 v100, v100\n"
"v_exp_f32 v101, v101\n"
"v_exp_f32 v102, v102\n"
"v_exp_f32 v103, v103\n"
"v_smfmac_f32_32x32x32_bf16 v[48:63], v[172:175], v[88:95], v254\n"
"s_add_u32 s18, s18, 4096\n"
"buffer_load_dwordx4 v[160:163], v249, s[48:51], s18 offen offset:0 sc1\n"
"buffer_load_dwordx4 v[164:167], v249, s[48:51], s18 offen offset:1024 sc1\n"
"buffer_load_dwordx4 v[168:171], v249, s[48:51], s18 offen offset:2048 sc1\n"
"buffer_load_dwordx4 v[172:175], v249, s[48:51], s18 offen offset:3072 sc1\n"
"v_exp_f32 v104, v104\n"
"v_exp_f32 v105, v105\n"
"v_exp_f32 v106, v106\n"
"v_exp_f32 v107, v107\n"
"v_smfmac_f32_32x32x32_bf16 v[0:15], v[184:187], v[96:103], v254\n"
"v_exp_f32 v108, v108\n"
"v_exp_f32 v109, v109\n"
"v_exp_f32 v110, v110\n"
"v_exp_f32 v111, v111\n"
"v_mfma_f32_32x32x16_bf16 v[80:95], v[176:179], v[128:131], 0\n"
"v_exp_f32 v64, v64\n"
"v_exp_f32 v65, v65\n"
"v_exp_f32 v66, v66\n"
"v_exp_f32 v67, v67\n"
"v_mfma_f32_32x32x16_bf16 v[80:95], v[180:183], v[132:135], v[80:95]\n"
"v_exp_f32 v68, v68\n"
"v_exp_f32 v69, v69\n"
"v_exp_f32 v70, v70\n"
"v_exp_f32 v71, v71\n"
"v_smfmac_f32_32x32x32_bf16 v[0:15], v[188:191], v[104:111], v254\n"
"v_exp_f32 v72, v72\n"
"v_exp_f32 v73, v73\n"
"v_exp_f32 v74, v74\n"
"v_exp_f32 v75, v75\n"
"v_smfmac_f32_32x32x32_bf16 v[16:31], v[184:187], v[64:71], v254\n"
"v_exp_f32 v76, v76\n"
"v_exp_f32 v77, v77\n"
"v_exp_f32 v78, v78\n"
"v_exp_f32 v79, v79\n"
"v_mfma_f32_32x32x16_bf16 v[96:111], v[176:179], v[136:139], 0\n"
"v_exp_f32 v80, v80\n"
"v_exp_f32 v81, v81\n"
"v_exp_f32 v82, v82\n"
"v_exp_f32 v83, v83\n"
"v_mfma_f32_32x32x16_bf16 v[96:111], v[180:183], v[140:143], v[96:111]\n"
"v_exp_f32 v84, v84\n"
"v_exp_f32 v85, v85\n"
"v_exp_f32 v86, v86\n"
"v_exp_f32 v87, v87\n"
"v_smfmac_f32_32x32x32_bf16 v[16:31], v[188:191], v[72:79], v254\n"
"v_exp_f32 v88, v88\n"
"v_exp_f32 v89, v89\n"
"v_exp_f32 v90, v90\n"
"v_exp_f32 v91, v91\n"
"v_smfmac_f32_32x32x32_bf16 v[32:47], v[184:187], v[80:87], v254\n"
"s_waitcnt vmcnt(16)\n"
"v_exp_f32 v92, v92\n"
"v_exp_f32 v93, v93\n"
"v_exp_f32 v94, v94\n"
"v_exp_f32 v95, v95\n"
"v_mfma_f32_32x32x16_bf16 v[64:79], v[192:195], v[112:115], 0\n"
"v_exp_f32 v96, v96\n"
"v_exp_f32 v97, v97\n"
"v_exp_f32 v98, v98\n"
"v_exp_f32 v99, v99\n"
"v_mfma_f32_32x32x16_bf16 v[64:79], v[196:199], v[116:119], v[64:79]\n"
"v_exp_f32 v100, v100\n"
"v_exp_f32 v101, v101\n"
"v_exp_f32 v102, v102\n"
"v_exp_f32 v103, v103\n"
"v_smfmac_f32_32x32x32_bf16 v[32:47], v[188:191], v[88:95], v254\n"
"v_exp_f32 v104, v104\n"
"v_exp_f32 v105, v105\n"
"v_exp_f32 v106, v106\n"
"v_exp_f32 v107, v107\n"
"v_smfmac_f32_32x32x32_bf16 v[48:63], v[184:187], v[96:103], v254\n"
"v_exp_f32 v108, v108\n"
"v_exp_f32 v109, v109\n"
"v_exp_f32 v110, v110\n"
"v_exp_f32 v111, v111\n"
"v_mfma_f32_32x32x16_bf16 v[80:95], v[192:195], v[120:123], 0\n"
"v_exp_f32 v64, v64\n"
"v_exp_f32 v65, v65\n"
"v_exp_f32 v66, v66\n"
"v_exp_f32 v67, v67\n"
"v_mfma_f32_32x32x16_bf16 v[80:95], v[196:199], v[124:127], v[80:95]\n"
"v_exp_f32 v68, v68\n"
"v_exp_f32 v69, v69\n"
"v_exp_f32 v70, v70\n"
"v_exp_f32 v71, v71\n"
"v_smfmac_f32_32x32x32_bf16 v[48:63], v[188:191], v[104:111], v254\n"
"s_add_u32 s18, s18, 4096\n"
"s_and_b32 s18, s18, 0xffff\n"
"buffer_load_dwordx4 v[176:179], v249, s[48:51], s18 offen offset:0 sc1\n"
"buffer_load_dwordx4 v[180:183], v249, s[48:51], s18 offen offset:1024 sc1\n"
"buffer_load_dwordx4 v[184:187], v249, s[48:51], s18 offen offset:2048 sc1\n"
"buffer_load_dwordx4 v[188:191], v249, s[48:51], s18 offen offset:3072 sc1\n"
"v_exp_f32 v72, v72\n"
"v_exp_f32 v73, v73\n"
"v_exp_f32 v74, v74\n"
"v_exp_f32 v75, v75\n"
"v_smfmac_f32_32x32x32_bf16 v[0:15], v[200:203], v[64:71], v254\n"
"v_exp_f32 v76, v76\n"
"v_exp_f32 v77, v77\n"
"v_exp_f32 v78, v78\n"
"v_exp_f32 v79, v79\n"
"v_mfma_f32_32x32x16_bf16 v[96:111], v[192:195], v[128:131], 0\n"
"v_exp_f32 v80, v80\n"
"v_exp_f32 v81, v81\n"
"v_exp_f32 v82, v82\n"
"v_exp_f32 v83, v83\n"
"v_mfma_f32_32x32x16_bf16 v[96:111], v[196:199], v[132:135], v[96:111]\n"
"v_exp_f32 v84, v84\n"
"v_exp_f32 v85, v85\n"
"v_exp_f32 v86, v86\n"
"v_exp_f32 v87, v87\n"
"v_smfmac_f32_32x32x32_bf16 v[0:15], v[204:207], v[72:79], v254\n"
"v_exp_f32 v88, v88\n"
"v_exp_f32 v89, v89\n"
"v_exp_f32 v90, v90\n"
"v_exp_f32 v91, v91\n"
"v_smfmac_f32_32x32x32_bf16 v[16:31], v[200:203], v[80:87], v254\n"
"v_exp_f32 v92, v92\n"
"v_exp_f32 v93, v93\n"
"v_exp_f32 v94, v94\n"
"v_exp_f32 v95, v95\n"
"v_mfma_f32_32x32x16_bf16 v[64:79], v[192:195], v[136:139], 0\n"
"v_exp_f32 v96, v96\n"
"v_exp_f32 v97, v97\n"
"v_exp_f32 v98, v98\n"
"v_exp_f32 v99, v99\n"
"v_mfma_f32_32x32x16_bf16 v[64:79], v[196:199], v[140:143], v[64:79]\n"
"v_exp_f32 v100, v100\n"
"v_exp_f32 v101, v101\n"
"v_exp_f32 v102, v102\n"
"v_exp_f32 v103, v103\n"
"v_smfmac_f32_32x32x32_bf16 v[16:31], v[204:207], v[88:95], v254\n"
"v_exp_f32 v104, v104\n"
"v_exp_f32 v105, v105\n"
"v_exp_f32 v106, v106\n"
"v_exp_f32 v107, v107\n"
"v_smfmac_f32_32x32x32_bf16 v[32:47], v[200:203], v[96:103], v254\n"
"s_waitcnt vmcnt(16)\n"
"v_exp_f32 v108, v108\n"
"v_exp_f32 v109, v109\n"
"v_exp_f32 v110, v110\n"
"v_exp_f32 v111, v111\n"
"v_mfma_f32_32x32x16_bf16 v[80:95], v[208:211], v[112:115], 0\n"
"v_exp_f32 v64, v64\n"
"v_exp_f32 v65, v65\n"
"v_exp_f32 v66, v66\n"
"v_exp_f32 v67, v67\n"
"v_mfma_f32_32x32x16_bf16 v[80:95], v[212:215], v[116:119], v[80:95]\n"
"v_exp_f32 v68, v68\n"
"v_exp_f32 v69, v69\n"
"v_exp_f32 v70, v70\n"
"v_exp_f32 v71, v71\n"
"v_smfmac_f32_32x32x32_bf16 v[32:47], v[204:207], v[104:111], v254\n"
"v_exp_f32 v72, v72\n"
"v_exp_f32 v73, v73\n"
"v_exp_f32 v74, v74\n"
"v_exp_f32 v75, v75\n"
"v_smfmac_f32_32x32x32_bf16 v[48:63], v[200:203], v[64:71], v254\n"
"v_exp_f32 v76, v76\n"
"v_exp_f32 v77, v77\n"
"v_exp_f32 v78, v78\n"
"v_exp_f32 v79, v79\n"
"v_mfma_f32_32x32x16_bf16 v[96:111], v[208:211], v[120:123], 0\n"
"v_exp_f32 v80, v80\n"
"v_exp_f32 v81, v81\n"
"v_exp_f32 v82, v82\n"
"v_exp_f32 v83, v83\n"
"v_mfma_f32_32x32x16_bf16 v[96:111], v[212:215], v[124:127], v[96:111]\n"
"v_exp_f32 v84, v84\n"
"v_exp_f32 v85, v85\n"
"v_exp_f32 v86, v86\n"
"v_exp_f32 v87, v87\n"
"v_smfmac_f32_32x32x32_bf16 v[48:63], v[204:207], v[72:79], v254\n"
"s_add_u32 s18, s18, 4096\n"
"buffer_load_dwordx4 v[192:195], v249, s[48:51], s18 offen offset:0 sc1\n"
"buffer_load_dwordx4 v[196:199], v249, s[48:51], s18 offen offset:1024 sc1\n"
"buffer_load_dwordx4 v[200:203], v249, s[48:51], s18 offen offset:2048 sc1\n"
"buffer_load_dwordx4 v[204:207], v249, s[48:51], s18 offen offset:3072 sc1\n"
"v_exp_f32 v88, v88\n"
"v_exp_f32 v89, v89\n"
"v_exp_f32 v90, v90\n"
"v_exp_f32 v91, v91\n"
"v_smfmac_f32_32x32x32_bf16 v[0:15], v[216:219], v[80:87], v254\n"
"v_exp_f32 v92, v92\n"
"v_exp_f32 v93, v93\n"
"v_exp_f32 v94, v94\n"
"v_exp_f32 v95, v95\n"
"v_mfma_f32_32x32x16_bf16 v[64:79], v[208:211], v[128:131], 0\n"
"v_exp_f32 v96, v96\n"
"v_exp_f32 v97, v97\n"
"v_exp_f32 v98, v98\n"
"v_exp_f32 v99, v99\n"
"v_mfma_f32_32x32x16_bf16 v[64:79], v[212:215], v[132:135], v[64:79]\n"
"v_exp_f32 v100, v100\n"
"v_exp_f32 v101, v101\n"
"v_exp_f32 v102, v102\n"
"v_exp_f32 v103, v103\n"
"v_smfmac_f32_32x32x32_bf16 v[0:15], v[220:223], v[88:95], v254\n"
"v_exp_f32 v104, v104\n"
"v_exp_f32 v105, v105\n"
"v_exp_f32 v106, v106\n"
"v_exp_f32 v107, v107\n"
"v_smfmac_f32_32x32x32_bf16 v[16:31], v[216:219], v[96:103], v254\n"
"v_exp_f32 v108, v108\n"
"v_exp_f32 v109, v109\n"
"v_exp_f32 v110, v110\n"
"v_exp_f32 v111, v111\n"
"v_mfma_f32_32x32x16_bf16 v[80:95], v[208:211], v[136:139], 0\n"
"v_exp_f32 v64, v64\n"
"v_exp_f32 v65, v65\n"
"v_exp_f32 v66, v66\n"
"v_exp_f32 v67, v67\n"
"v_mfma_f32_32x32x16_bf16 v[80:95], v[212:215], v[140:143], v[80:95]\n"
"v_exp_f32 v68, v68\n"
"v_exp_f32 v69, v69\n"
"v_exp_f32 v70, v70\n"
"v_exp_f32 v71, v71\n"
"v_smfmac_f32_32x32x32_bf16 v[16:31], v[220:223], v[104:111], v254\n"
"v_exp_f32 v72, v72\n"
"v_exp_f32 v73, v73\n"
"v_exp_f32 v74, v74\n"
"v_exp_f32 v75, v75\n"
"v_smfmac_f32_32x32x32_bf16 v[32:47], v[216:219], v[64:71], v254\n"
"s_waitcnt vmcnt(16)\n"
"v_exp_f32 v76, v76\n"
"v_exp_f32 v77, v77\n"
"v_exp_f32 v78, v78\n"
"v_exp_f32 v79, v79\n"
"v_mfma_f32_32x32x16_bf16 v[96:111], v[224:227], v[112:115], 0\n"
"v_exp_f32 v80, v80\n"
"v_exp_f32 v81, v81\n"
"v_exp_f32 v82, v82\n"
"v_exp_f32 v83, v83\n"
"v_mfma_f32_32x32x16_bf16 v[96:111], v[228:231], v[116:119], v[96:111]\n"
"v_exp_f32 v84, v84\n"
"v_exp_f32 v85, v85\n"
"v_exp_f32 v86, v86\n"
"v_exp_f32 v87, v87\n"
"v_smfmac_f32_32x32x32_bf16 v[32:47], v[220:223], v[72:79], v254\n"
"v_exp_f32 v88, v88\n"
"v_exp_f32 v89, v89\n"
"v_exp_f32 v90, v90\n"
"v_exp_f32 v91, v91\n"
"v_smfmac_f32_32x32x32_bf16 v[48:63], v[216:219], v[80:87], v254\n"
"v_exp_f32 v92, v92\n"
"v_exp_f32 v93, v93\n"
"v_exp_f32 v94, v94\n"
"v_exp_f32 v95, v95\n"
"v_mfma_f32_32x32x16_bf16 v[64:79], v[224:227], v[120:123], 0\n"
"v_exp_f32 v96, v96\n"
"v_exp_f32 v97, v97\n"
"v_exp_f32 v98, v98\n"
"v_exp_f32 v99, v99\n"
"v_mfma_f32_32x32x16_bf16 v[64:79], v[228:231], v[124:127], v[64:79]\n"
"v_exp_f32 v100, v100\n"
"v_exp_f32 v101, v101\n"
"v_exp_f32 v102, v102\n"
"v_exp_f32 v103, v103\n"
"v_smfmac_f32_32x32x32_bf16 v[48:63], v[220:223], v[88:95], v254\n"
"s_add_u32 s18, s18, 4096\n"
"buffer_load_dwordx4 v[208:211], v249, s[48:51], s18 offen offset:0 sc1\n"
"buffer_load_dwordx4 v[212:215], v249, s[48:51], s18 offen offset:1024 sc1\n"
"buffer_load_dwordx4 v[216:219], v249, s[48:51], s18 offen offset:2048 sc1\n"
"buffer_load_dwordx4 v[220:223], v249, s[48:51], s18 offen offset:3072 sc1\n"
"v_exp_f32 v104, v104\n"
"v_exp_f32 v105, v105\n"
"v_exp_f32 v106, v106\n"
"v_exp_f32 v107, v107\n"
"v_smfmac_f32_32x32x32_bf16 v[0:15], v[232:235], v[96:103], v254\n"
"v_exp_f32 v108, v108\n"
"v_exp_f32 v109, v109\n"
"v_exp_f32 v110, v110\n"
"v_exp_f32 v111, v111\n"
"v_mfma_f32_32x32x16_bf16 v[80:95], v[224:227], v[128:131], 0\n"
"v_exp_f32 v64, v64\n"
"v_exp_f32 v65, v65\n"
"v_exp_f32 v66, v66\n"
"v_exp_f32 v67, v67\n"
"v_mfma_f32_32x32x16_bf16 v[80:95], v[228:231], v[132:135], v[80:95]\n"
"v_exp_f32 v68, v68\n"
"v_exp_f32 v69, v69\n"
"v_exp_f32 v70, v70\n"
"v_exp_f32 v71, v71\n"
"v_smfmac_f32_32x32x32_bf16 v[0:15], v[236:239], v[104:111], v254\n"
"v_exp_f32 v72, v72\n"
"v_exp_f32 v73, v73\n"
"v_exp_f32 v74, v74\n"
"v_exp_f32 v75, v75\n"
"v_smfmac_f32_32x32x32_bf16 v[16:31], v[232:235], v[64:71], v254\n"
"v_exp_f32 v76, v76\n"
"v_exp_f32 v77, v77\n"
"v_exp_f32 v78, v78\n"
"v_exp_f32 v79, v79\n"
"v_mfma_f32_32x32x16_bf16 v[96:111], v[224:227], v[136:139], 0\n"
"v_exp_f32 v80, v80\n"
"v_exp_f32 v81, v81\n"
"v_exp_f32 v82, v82\n"
"v_exp_f32 v83, v83\n"
"v_mfma_f32_32x32x16_bf16 v[96:111], v[228:231], v[140:143], v[96:111]\n"
"v_exp_f32 v84, v84\n"
"v_exp_f32 v85, v85\n"
"v_exp_f32 v86, v86\n"
"v_exp_f32 v87, v87\n"
"v_smfmac_f32_32x32x32_bf16 v[16:31], v[236:239], v[72:79], v254\n"
"v_exp_f32 v88, v88\n"
"v_exp_f32 v89, v89\n"
"v_exp_f32 v90, v90\n"
"v_exp_f32 v91, v91\n"
"v_smfmac_f32_32x32x32_bf16 v[32:47], v[232:235], v[80:87], v254\n"
"s_waitcnt vmcnt(16)\n"
"v_exp_f32 v92, v92\n"
"v_exp_f32 v93, v93\n"
"v_exp_f32 v94, v94\n"
"v_exp_f32 v95, v95\n"
"v_mfma_f32_32x32x16_bf16 v[64:79], v[144:147], v[112:115], 0\n"
"v_exp_f32 v96, v96\n"
"v_exp_f32 v97, v97\n"
"v_exp_f32 v98, v98\n"
"v_exp_f32 v99, v99\n"
"v_mfma_f32_32x32x16_bf16 v[64:79], v[148:151], v[116:119], v[64:79]\n"
"v_exp_f32 v100, v100\n"
"v_exp_f32 v101, v101\n"
"v_exp_f32 v102, v102\n"
"v_exp_f32 v103, v103\n"
"v_smfmac_f32_32x32x32_bf16 v[32:47], v[236:239], v[88:95], v254\n"
"v_exp_f32 v104, v104\n"
"v_exp_f32 v105, v105\n"
"v_exp_f32 v106, v106\n"
"v_exp_f32 v107, v107\n"
"v_smfmac_f32_32x32x32_bf16 v[48:63], v[232:235], v[96:103], v254\n"
"v_exp_f32 v108, v108\n"
"v_exp_f32 v109, v109\n"
"v_exp_f32 v110, v110\n"
"v_exp_f32 v111, v111\n"
"v_mfma_f32_32x32x16_bf16 v[80:95], v[144:147], v[120:123], 0\n"
"v_exp_f32 v64, v64\n"
"v_exp_f32 v65, v65\n"
"v_exp_f32 v66, v66\n"
"v_exp_f32 v67, v67\n"
"v_mfma_f32_32x32x16_bf16 v[80:95], v[148:151], v[124:127], v[80:95]\n"
"v_exp_f32 v68, v68\n"
"v_exp_f32 v69, v69\n"
"v_exp_f32 v70, v70\n"
"v_exp_f32 v71, v71\n"
"v_smfmac_f32_32x32x32_bf16 v[48:63], v[236:239], v[104:111], v254\n"
"s_add_u32 s18, s18, 4096\n"
"buffer_load_dwordx4 v[224:227], v249, s[48:51], s18 offen offset:0 sc1\n"
"buffer_load_dwordx4 v[228:231], v249, s[48:51], s18 offen offset:1024 sc1\n"
"buffer_load_dwordx4 v[232:235], v249, s[48:51], s18 offen offset:2048 sc1\n"
"buffer_load_dwordx4 v[236:239], v249, s[48:51], s18 offen offset:3072 sc1\n"
"v_exp_f32 v72, v72\n"
"v_exp_f32 v73, v73\n"
"v_exp_f32 v74, v74\n"
"v_exp_f32 v75, v75\n"
"v_smfmac_f32_32x32x32_bf16 v[0:15], v[152:155], v[64:71], v254\n"
"v_exp_f32 v76, v76\n"
"v_exp_f32 v77, v77\n"
"v_exp_f32 v78, v78\n"
"v_exp_f32 v79, v79\n"
"v_mfma_f32_32x32x16_bf16 v[96:111], v[144:147], v[128:131], 0\n"
"v_exp_f32 v80, v80\n"
"v_exp_f32 v81, v81\n"
"v_exp_f32 v82, v82\n"
"v_exp_f32 v83, v83\n"
"v_mfma_f32_32x32x16_bf16 v[96:111], v[148:151], v[132:135], v[96:111]\n"
"v_exp_f32 v84, v84\n"
"v_exp_f32 v85, v85\n"
"v_exp_f32 v86, v86\n"
"v_exp_f32 v87, v87\n"
"v_smfmac_f32_32x32x32_bf16 v[0:15], v[156:159], v[72:79], v254\n"
"v_exp_f32 v88, v88\n"
"v_exp_f32 v89, v89\n"
"v_exp_f32 v90, v90\n"
"v_exp_f32 v91, v91\n"
"v_smfmac_f32_32x32x32_bf16 v[16:31], v[152:155], v[80:87], v254\n"
"v_exp_f32 v92, v92\n"
"v_exp_f32 v93, v93\n"
"v_exp_f32 v94, v94\n"
"v_exp_f32 v95, v95\n"
"v_mfma_f32_32x32x16_bf16 v[64:79], v[144:147], v[136:139], 0\n"
"v_exp_f32 v96, v96\n"
"v_exp_f32 v97, v97\n"
"v_exp_f32 v98, v98\n"
"v_exp_f32 v99, v99\n"
"v_mfma_f32_32x32x16_bf16 v[64:79], v[148:151], v[140:143], v[64:79]\n"
"v_exp_f32 v100, v100\n"
"v_exp_f32 v101, v101\n"
"v_exp_f32 v102, v102\n"
"v_exp_f32 v103, v103\n"
"v_smfmac_f32_32x32x32_bf16 v[16:31], v[156:159], v[88:95], v254\n"
"v_exp_f32 v104, v104\n"
"v_exp_f32 v105, v105\n"
"v_exp_f32 v106, v106\n"
"v_exp_f32 v107, v107\n"
"v_smfmac_f32_32x32x32_bf16 v[32:47], v[152:155], v[96:103], v254\n"
"s_waitcnt vmcnt(16)\n"
"v_exp_f32 v108, v108\n"
"v_exp_f32 v109, v109\n"
"v_exp_f32 v110, v110\n"
"v_exp_f32 v111, v111\n"
"v_mfma_f32_32x32x16_bf16 v[80:95], v[160:163], v[112:115], 0\n"
"v_exp_f32 v64, v64\n"
"v_exp_f32 v65, v65\n"
"v_exp_f32 v66, v66\n"
"v_exp_f32 v67, v67\n"
"v_mfma_f32_32x32x16_bf16 v[80:95], v[164:167], v[116:119], v[80:95]\n"
"v_exp_f32 v68, v68\n"
"v_exp_f32 v69, v69\n"
"v_exp_f32 v70, v70\n"
"v_exp_f32 v71, v71\n"
"v_smfmac_f32_32x32x32_bf16 v[32:47], v[156:159], v[104:111], v254\n"
"v_exp_f32 v72, v72\n"
"v_exp_f32 v73, v73\n"
"v_exp_f32 v74, v74\n"
"v_exp_f32 v75, v75\n"
"v_smfmac_f32_32x32x32_bf16 v[48:63], v[152:155], v[64:71], v254\n"
"v_exp_f32 v76, v76\n"
"v_exp_f32 v77, v77\n"
"v_exp_f32 v78, v78\n"
"v_exp_f32 v79, v79\n"
"v_mfma_f32_32x32x16_bf16 v[96:111], v[160:163], v[120:123], 0\n"
"v_exp_f32 v80, v80\n"
"v_exp_f32 v81, v81\n"
"v_exp_f32 v82, v82\n"
"v_exp_f32 v83, v83\n"
"v_mfma_f32_32x32x16_bf16 v[96:111], v[164:167], v[124:127], v[96:111]\n"
"v_exp_f32 v84, v84\n"
"v_exp_f32 v85, v85\n"
"v_exp_f32 v86, v86\n"
"v_exp_f32 v87, v87\n"
"v_smfmac_f32_32x32x32_bf16 v[48:63], v[156:159], v[72:79], v254\n"
"s_add_u32 s18, s18, 4096\n"
"s_and_b32 s18, s18, 0xffff\n"
"buffer_load_dwordx4 v[144:147], v249, s[48:51], s18 offen offset:0 sc1\n"
"buffer_load_dwordx4 v[148:151], v249, s[48:51], s18 offen offset:1024 sc1\n"
"buffer_load_dwordx4 v[152:155], v249, s[48:51], s18 offen offset:2048 sc1\n"
"buffer_load_dwordx4 v[156:159], v249, s[48:51], s18 offen offset:3072 sc1\n"
"v_exp_f32 v88, v88\n"
"v_exp_f32 v89, v89\n"
"v_exp_f32 v90, v90\n"
"v_exp_f32 v91, v91\n"
"v_smfmac_f32_32x32x32_bf16 v[0:15], v[168:171], v[80:87], v254\n"
"v_exp_f32 v92, v92\n"
"v_exp_f32 v93, v93\n"
"v_exp_f32 v94, v94\n"
"v_exp_f32 v95, v95\n"
"v_mfma_f32_32x32x16_bf16 v[64:79], v[160:163], v[128:131], 0\n"
"v_exp_f32 v96, v96\n"
"v_exp_f32 v97, v97\n"
"v_exp_f32 v98, v98\n"
"v_exp_f32 v99, v99\n"
"v_mfma_f32_32x32x16_bf16 v[64:79], v[164:167], v[132:135], v[64:79]\n"
"v_exp_f32 v100, v100\n"
"v_exp_f32 v101, v101\n"
"v_exp_f32 v102, v102\n"
"v_exp_f32 v103, v103\n"
"v_smfmac_f32_32x32x32_bf16 v[0:15], v[172:175], v[88:95], v254\n"
"v_exp_f32 v104, v104\n"
"v_exp_f32 v105, v105\n"
"v_exp_f32 v106, v106\n"
"v_exp_f32 v107, v107\n"
"v_smfmac_f32_32x32x32_bf16 v[16:31], v[168:171], v[96:103], v254\n"
"v_exp_f32 v108, v108\n"
"v_exp_f32 v109, v109\n"
"v_exp_f32 v110, v110\n"
"v_exp_f32 v111, v111\n"
"v_mfma_f32_32x32x16_bf16 v[80:95], v[160:163], v[136:139], 0\n"
"v_exp_f32 v64, v64\n"
"v_exp_f32 v65, v65\n"
"v_exp_f32 v66, v66\n"
"v_exp_f32 v67, v67\n"
"v_mfma_f32_32x32x16_bf16 v[80:95], v[164:167], v[140:143], v[80:95]\n"
"v_exp_f32 v68, v68\n"
"v_exp_f32 v69, v69\n"
"v_exp_f32 v70, v70\n"
"v_exp_f32 v71, v71\n"
"v_smfmac_f32_32x32x32_bf16 v[16:31], v[172:175], v[104:111], v254\n"
"v_exp_f32 v72, v72\n"
"v_exp_f32 v73, v73\n"
"v_exp_f32 v74, v74\n"
"v_exp_f32 v75, v75\n"
"v_smfmac_f32_32x32x32_bf16 v[32:47], v[168:171], v[64:71], v254\n"
"s_waitcnt vmcnt(16)\n"
"v_exp_f32 v76, v76\n"
"v_exp_f32 v77, v77\n"
"v_exp_f32 v78, v78\n"
"v_exp_f32 v79, v79\n"
"v_mfma_f32_32x32x16_bf16 v[96:111], v[176:179], v[112:115], 0\n"
"v_exp_f32 v80, v80\n"
"v_exp_f32 v81, v81\n"
"v_exp_f32 v82, v82\n"
"v_exp_f32 v83, v83\n"
"v_mfma_f32_32x32x16_bf16 v[96:111], v[180:183], v[116:119], v[96:111]\n"
"v_exp_f32 v84, v84\n"
"v_exp_f32 v85, v85\n"
"v_exp_f32 v86, v86\n"
"v_exp_f32 v87, v87\n"
"v_smfmac_f32_32x32x32_bf16 v[32:47], v[172:175], v[72:79], v254\n"
"v_exp_f32 v88, v88\n"
"v_exp_f32 v89, v89\n"
"v_exp_f32 v90, v90\n"
"v_exp_f32 v91, v91\n"
"v_smfmac_f32_32x32x32_bf16 v[48:63], v[168:171], v[80:87], v254\n"
"v_exp_f32 v92, v92\n"
"v_exp_f32 v93, v93\n"
"v_exp_f32 v94, v94\n"
"v_exp_f32 v95, v95\n"
"v_mfma_f32_32x32x16_bf16 v[64:79], v[176:179], v[120:123], 0\n"
"v_exp_f32 v96, v96\n"
"v_exp_f32 v97, v97\n"
"v_exp_f32 v98, v98\n"
"v_exp_f32 v99, v99\n"
"v_mfma_f32_32x32x16_bf16 v[64:79], v[180:183], v[124:127], v[64:79]\n"
"v_exp_f32 v100, v100\n"
"v_exp_f32 v101, v101\n"
"v_exp_f32 v102, v102\n"
"v_exp_f32 v103, v103\n"
"v_smfmac_f32_32x32x32_bf16 v[48:63], v[172:175], v[88:95], v254\n"
"s_add_u32 s18, s18, 4096\n"
"buffer_load_dwordx4 v[160:163], v249, s[48:51], s18 offen offset:0 sc1\n"
"buffer_load_dwordx4 v[164:167], v249, s[48:51], s18 offen offset:1024 sc1\n"
"buffer_load_dwordx4 v[168:171], v249, s[48:51], s18 offen offset:2048 sc1\n"
"buffer_load_dwordx4 v[172:175], v249, s[48:51], s18 offen offset:3072 sc1\n"
"v_exp_f32 v104, v104\n"
"v_exp_f32 v105, v105\n"
"v_exp_f32 v106, v106\n"
"v_exp_f32 v107, v107\n"
"v_smfmac_f32_32x32x32_bf16 v[0:15], v[184:187], v[96:103], v254\n"
"v_exp_f32 v108, v108\n"
"v_exp_f32 v109, v109\n"
"v_exp_f32 v110, v110\n"
"v_exp_f32 v111, v111\n"
"v_mfma_f32_32x32x16_bf16 v[80:95], v[176:179], v[128:131], 0\n"
"v_exp_f32 v64, v64\n"
"v_exp_f32 v65, v65\n"
"v_exp_f32 v66, v66\n"
"v_exp_f32 v67, v67\n"
"v_mfma_f32_32x32x16_bf16 v[80:95], v[180:183], v[132:135], v[80:95]\n"
"v_exp_f32 v68, v68\n"
"v_exp_f32 v69, v69\n"
"v_exp_f32 v70, v70\n"
"v_exp_f32 v71, v71\n"
"v_smfmac_f32_32x32x32_bf16 v[0:15], v[188:191], v[104:111], v254\n"
"v_exp_f32 v72, v72\n"
"v_exp_f32 v73, v73\n"
"v_exp_f32 v74, v74\n"
"v_exp_f32 v75, v75\n"
"v_smfmac_f32_32x32x32_bf16 v[16:31], v[184:187], v[64:71], v254\n"
"v_exp_f32 v76, v76\n"
"v_exp_f32 v77, v77\n"
"v_exp_f32 v78, v78\n"
"v_exp_f32 v79, v79\n"
"v_mfma_f32_32x32x16_bf16 v[96:111], v[176:179], v[136:139], 0\n"
"v_exp_f32 v80, v80\n"
"v_exp_f32 v81, v81\n"
"v_exp_f32 v82, v82\n"
"v_exp_f32 v83, v83\n"
"v_mfma_f32_32x32x16_bf16 v[96:111], v[180:183], v[140:143], v[96:111]\n"
"v_exp_f32 v84, v84\n"
"v_exp_f32 v85, v85\n"
"v_exp_f32 v86, v86\n"
"v_exp_f32 v87, v87\n"
"v_smfmac_f32_32x32x32_bf16 v[16:31], v[188:191], v[72:79], v254\n"
"v_exp_f32 v88, v88\n"
"v_exp_f32 v89, v89\n"
"v_exp_f32 v90, v90\n"
"v_exp_f32 v91, v91\n"
"v_smfmac_f32_32x32x32_bf16 v[32:47], v[184:187], v[80:87], v254\n"
"s_waitcnt vmcnt(16)\n"
"v_exp_f32 v92, v92\n"
"v_exp_f32 v93, v93\n"
"v_exp_f32 v94, v94\n"
"v_exp_f32 v95, v95\n"
"v_mfma_f32_32x32x16_bf16 v[64:79], v[192:195], v[112:115], 0\n"
"v_exp_f32 v96, v96\n"
"v_exp_f32 v97, v97\n"
"v_exp_f32 v98, v98\n"
"v_exp_f32 v99, v99\n"
"v_mfma_f32_32x32x16_bf16 v[64:79], v[196:199], v[116:119], v[64:79]\n"
"v_exp_f32 v100, v100\n"
"v_exp_f32 v101, v101\n"
"v_exp_f32 v102, v102\n"
"v_exp_f32 v103, v103\n"
"v_smfmac_f32_32x32x32_bf16 v[32:47], v[188:191], v[88:95], v254\n"
"v_exp_f32 v104, v104\n"
"v_exp_f32 v105, v105\n"
"v_exp_f32 v106, v106\n"
"v_exp_f32 v107, v107\n"
"v_smfmac_f32_32x32x32_bf16 v[48:63], v[184:187], v[96:103], v254\n"
"v_exp_f32 v108, v108\n"
"v_exp_f32 v109, v109\n"
"v_exp_f32 v110, v110\n"
"v_exp_f32 v111, v111\n"
"v_mfma_f32_32x32x16_bf16 v[80:95], v[192:195], v[120:123], 0\n"
"v_exp_f32 v64, v64\n"
"v_exp_f32 v65, v65\n"
"v_exp_f32 v66, v66\n"
"v_exp_f32 v67, v67\n"
"v_mfma_f32_32x32x16_bf16 v[80:95], v[196:199], v[124:127], v[80:95]\n"
"v_exp_f32 v68, v68\n"
"v_exp_f32 v69, v69\n"
"v_exp_f32 v70, v70\n"
"v_exp_f32 v71, v71\n"
"v_smfmac_f32_32x32x32_bf16 v[48:63], v[188:191], v[104:111], v254\n"
"s_add_u32 s18, s18, 4096\n"
"buffer_load_dwordx4 v[176:179], v249, s[48:51], s18 offen offset:0 sc1\n"
"buffer_load_dwordx4 v[180:183], v249, s[48:51], s18 offen offset:1024 sc1\n"
"buffer_load_dwordx4 v[184:187], v249, s[48:51], s18 offen offset:2048 sc1\n"
"buffer_load_dwordx4 v[188:191], v249, s[48:51], s18 offen offset:3072 sc1\n"
"v_exp_f32 v72, v72\n"
"v_exp_f32 v73, v73\n"
"v_exp_f32 v74, v74\n"
"v_exp_f32 v75, v75\n"
"v_smfmac_f32_32x32x32_bf16 v[0:15], v[200:203], v[64:71], v254\n"
"v_exp_f32 v76, v76\n"
"v_exp_f32 v77, v77\n"
"v_exp_f32 v78, v78\n"
"v_exp_f32 v79, v79\n"
"v_mfma_f32_32x32x16_bf16 v[96:111], v[192:195], v[128:131], 0\n"
"v_exp_f32 v80, v80\n"
"v_exp_f32 v81, v81\n"
"v_exp_f32 v82, v82\n"
"v_exp_f32 v83, v83\n"
"v_mfma_f32_32x32x16_bf16 v[96:111], v[196:199], v[132:135], v[96:111]\n"
"v_exp_f32 v84, v84\n"
"v_exp_f32 v85, v85\n"
"v_exp_f32 v86, v86\n"
"v_exp_f32 v87, v87\n"
"v_smfmac_f32_32x32x32_bf16 v[0:15], v[204:207], v[72:79], v254\n"
"v_exp_f32 v88, v88\n"
"v_exp_f32 v89, v89\n"
"v_exp_f32 v90, v90\n"
"v_exp_f32 v91, v91\n"
"v_smfmac_f32_32x32x32_bf16 v[16:31], v[200:203], v[80:87], v254\n"
"v_exp_f32 v92, v92\n"
"v_exp_f32 v93, v93\n"
"v_exp_f32 v94, v94\n"
"v_exp_f32 v95, v95\n"
"v_mfma_f32_32x32x16_bf16 v[64:79], v[192:195], v[136:139], 0\n"
"v_exp_f32 v96, v96\n"
"v_exp_f32 v97, v97\n"
"v_exp_f32 v98, v98\n"
"v_exp_f32 v99, v99\n"
"v_mfma_f32_32x32x16_bf16 v[64:79], v[196:199], v[140:143], v[64:79]\n"
"v_exp_f32 v100, v100\n"
"v_exp_f32 v101, v101\n"
"v_exp_f32 v102, v102\n"
"v_exp_f32 v103, v103\n"
"v_smfmac_f32_32x32x32_bf16 v[16:31], v[204:207], v[88:95], v254\n"
"v_exp_f32 v104, v104\n"
"v_exp_f32 v105, v105\n"
"v_exp_f32 v106, v106\n"
"v_exp_f32 v107, v107\n"
"v_smfmac_f32_32x32x32_bf16 v[32:47], v[200:203], v[96:103], v254\n"
"s_waitcnt vmcnt(16)\n"
"v_exp_f32 v108, v108\n"
"v_exp_f32 v109, v109\n"
"v_exp_f32 v110, v110\n"
"v_exp_f32 v111, v111\n"
"v_mfma_f32_32x32x16_bf16 v[80:95], v[208:211], v[112:115], 0\n"
"v_exp_f32 v64, v64\n"
"v_exp_f32 v65, v65\n"
"v_exp_f32 v66, v66\n"
"v_exp_f32 v67, v67\n"
"v_mfma_f32_32x32x16_bf16 v[80:95], v[212:215], v[116:119], v[80:95]\n"
"v_exp_f32 v68, v68\n"
"v_exp_f32 v69, v69\n"
"v_exp_f32 v70, v70\n"
"v_exp_f32 v71, v71\n"
"v_smfmac_f32_32x32x32_bf16 v[32:47], v[204:207], v[104:111], v254\n"
"v_exp_f32 v72, v72\n"
"v_exp_f32 v73, v73\n"
"v_exp_f32 v74, v74\n"
"v_exp_f32 v75, v75\n"
"v_smfmac_f32_32x32x32_bf16 v[48:63], v[200:203], v[64:71], v254\n"
"v_exp_f32 v76, v76\n"
"v_exp_f32 v77, v77\n"
"v_exp_f32 v78, v78\n"
"v_exp_f32 v79, v79\n"
"v_mfma_f32_32x32x16_bf16 v[96:111], v[208:211], v[120:123], 0\n"
"v_exp_f32 v80, v80\n"
"v_exp_f32 v81, v81\n"
"v_exp_f32 v82, v82\n"
"v_exp_f32 v83, v83\n"
"v_mfma_f32_32x32x16_bf16 v[96:111], v[212:215], v[124:127], v[96:111]\n"
"v_exp_f32 v84, v84\n"
"v_exp_f32 v85, v85\n"
"v_exp_f32 v86, v86\n"
"v_exp_f32 v87, v87\n"
"v_smfmac_f32_32x32x32_bf16 v[48:63], v[204:207], v[72:79], v254\n"
"s_add_u32 s18, s18, 4096\n"
"buffer_load_dwordx4 v[192:195], v249, s[48:51], s18 offen offset:0 sc1\n"
"buffer_load_dwordx4 v[196:199], v249, s[48:51], s18 offen offset:1024 sc1\n"
"buffer_load_dwordx4 v[200:203], v249, s[48:51], s18 offen offset:2048 sc1\n"
"buffer_load_dwordx4 v[204:207], v249, s[48:51], s18 offen offset:3072 sc1\n"
"v_exp_f32 v88, v88\n"
"v_exp_f32 v89, v89\n"
"v_exp_f32 v90, v90\n"
"v_exp_f32 v91, v91\n"
"v_smfmac_f32_32x32x32_bf16 v[0:15], v[216:219], v[80:87], v254\n"
"v_exp_f32 v92, v92\n"
"v_exp_f32 v93, v93\n"
"v_exp_f32 v94, v94\n"
"v_exp_f32 v95, v95\n"
"v_mfma_f32_32x32x16_bf16 v[64:79], v[208:211], v[128:131], 0\n"
"v_exp_f32 v96, v96\n"
"v_exp_f32 v97, v97\n"
"v_exp_f32 v98, v98\n"
"v_exp_f32 v99, v99\n"
"v_mfma_f32_32x32x16_bf16 v[64:79], v[212:215], v[132:135], v[64:79]\n"
"v_exp_f32 v100, v100\n"
"v_exp_f32 v101, v101\n"
"v_exp_f32 v102, v102\n"
"v_exp_f32 v103, v103\n"
"v_smfmac_f32_32x32x32_bf16 v[0:15], v[220:223], v[88:95], v254\n"
"v_exp_f32 v104, v104\n"
"v_exp_f32 v105, v105\n"
"v_exp_f32 v106, v106\n"
"v_exp_f32 v107, v107\n"
"v_smfmac_f32_32x32x32_bf16 v[16:31], v[216:219], v[96:103], v254\n"
"v_exp_f32 v108, v108\n"
"v_exp_f32 v109, v109\n"
"v_exp_f32 v110, v110\n"
"v_exp_f32 v111, v111\n"
"v_mfma_f32_32x32x16_bf16 v[80:95], v[208:211], v[136:139], 0\n"
"v_exp_f32 v64, v64\n"
"v_exp_f32 v65, v65\n"
"v_exp_f32 v66, v66\n"
"v_exp_f32 v67, v67\n"
"v_mfma_f32_32x32x16_bf16 v[80:95], v[212:215], v[140:143], v[80:95]\n"
"v_exp_f32 v68, v68\n"
"v_exp_f32 v69, v69\n"
"v_exp_f32 v70, v70\n"
"v_exp_f32 v71, v71\n"
"v_smfmac_f32_32x32x32_bf16 v[16:31], v[220:223], v[104:111], v254\n"
"v_exp_f32 v72, v72\n"
"v_exp_f32 v73, v73\n"
"v_exp_f32 v74, v74\n"
"v_exp_f32 v75, v75\n"
"v_smfmac_f32_32x32x32_bf16 v[32:47], v[216:219], v[64:71], v254\n"
"s_waitcnt vmcnt(16)\n"
"v_exp_f32 v76, v76\n"
"v_exp_f32 v77, v77\n"
"v_exp_f32 v78, v78\n"
"v_exp_f32 v79, v79\n"
"v_mfma_f32_32x32x16_bf16 v[96:111], v[224:227], v[112:115], 0\n"
"v_exp_f32 v80, v80\n"
"v_exp_f32 v81, v81\n"
"v_exp_f32 v82, v82\n"
"v_exp_f32 v83, v83\n"
"v_mfma_f32_32x32x16_bf16 v[96:111], v[228:231], v[116:119], v[96:111]\n"
"v_exp_f32 v84, v84\n"
"v_exp_f32 v85, v85\n"
"v_exp_f32 v86, v86\n"
"v_exp_f32 v87, v87\n"
"v_smfmac_f32_32x32x32_bf16 v[32:47], v[220:223], v[72:79], v254\n"
"v_exp_f32 v88, v88\n"
"v_exp_f32 v89, v89\n"
"v_exp_f32 v90, v90\n"
"v_exp_f32 v91, v91\n"
"v_smfmac_f32_32x32x32_bf16 v[48:63], v[216:219], v[80:87], v254\n"
"v_exp_f32 v92, v92\n"
"v_exp_f32 v93, v93\n"
"v_exp_f32 v94, v94\n"
"v_exp_f32 v95, v95\n"
"v_mfma_f32_32x32x16_bf16 v[64:79], v[224:227], v[120:123], 0\n"
"v_exp_f32 v96, v96\n"
"v_exp_f32 v97, v97\n"
"v_exp_f32 v98, v98\n"
"v_exp_f32 v99, v99\n"
"v_mfma_f32_32x32x16_bf16 v[64:79], v[228:231], v[124:127], v[64:79]\n"
"v_exp_f32 v100, v100\n"
"v_exp_f32 v101, v101\n"
"v_exp_f32 v102, v102\n"
"v_exp_f32 v103, v103\n"
"v_smfmac_f32_32x32x32_bf16 v[48:63], v[220:223], v[88:95], v254\n"
"v_exp_f32 v104, v104\n"
"v_exp_f32 v105, v105\n"
"v_exp_f32 v106, v106\n"
"v_exp_f32 v107, v107\n"
"v_smfmac_f32_32x32x32_bf16 v[0:15], v[232:235], v[96:103], v254\n"
"v_exp_f32 v108, v108\n"
"v_exp_f32 v109, v109\n"
"v_exp_f32 v110, v110\n"
"v_exp_f32 v111, v111\n"
"v_mfma_f32_32x32x16_bf16 v[80:95], v[224:227], v[128:131], 0\n"
"v_exp_f32 v64, v64\n"
"v_exp_f32 v65, v65\n"
"v_exp_f32 v66, v66\n"
"v_exp_f32 v67, v67\n"
"v_mfma_f32_32x32x16_bf16 v[80:95], v[228:231], v[132:135], v[80:95]\n"
"v_exp_f32 v68, v68\n"
"v_exp_f32 v69, v69\n"
"v_exp_f32 v70, v70\n"
"v_exp_f32 v71, v71\n"
"v_smfmac_f32_32x32x32_bf16 v[0:15], v[236:239], v[104:111], v254\n"
"v_exp_f32 v72, v72\n"
"v_exp_f32 v73, v73\n"
"v_exp_f32 v74, v74\n"
"v_exp_f32 v75, v75\n"
"v_smfmac_f32_32x32x32_bf16 v[16:31], v[232:235], v[64:71], v254\n"
"v_exp_f32 v76, v76\n"
"v_exp_f32 v77, v77\n"
"v_exp_f32 v78, v78\n"
"v_exp_f32 v79, v79\n"
"v_mfma_f32_32x32x16_bf16 v[96:111], v[224:227], v[136:139], 0\n"
"v_exp_f32 v80, v80\n"
"v_exp_f32 v81, v81\n"
"v_exp_f32 v82, v82\n"
"v_exp_f32 v83, v83\n"
"v_mfma_f32_32x32x16_bf16 v[96:111], v[228:231], v[140:143], v[96:111]\n"
"v_exp_f32 v84, v84\n"
"v_exp_f32 v85, v85\n"
"v_exp_f32 v86, v86\n"
"v_exp_f32 v87, v87\n"
"v_smfmac_f32_32x32x32_bf16 v[16:31], v[236:239], v[72:79], v254\n"
"v_exp_f32 v88, v88\n"
"v_exp_f32 v89, v89\n"
"v_exp_f32 v90, v90\n"
"v_exp_f32 v91, v91\n"
"v_smfmac_f32_32x32x32_bf16 v[32:47], v[232:235], v[80:87], v254\n"
"s_waitcnt vmcnt(12)\n"
"v_exp_f32 v92, v92\n"
"v_exp_f32 v93, v93\n"
"v_exp_f32 v94, v94\n"
"v_exp_f32 v95, v95\n"
"v_mfma_f32_32x32x16_bf16 v[64:79], v[144:147], v[112:115], 0\n"
"v_exp_f32 v96, v96\n"
"v_exp_f32 v97, v97\n"
"v_exp_f32 v98, v98\n"
"v_exp_f32 v99, v99\n"
"v_mfma_f32_32x32x16_bf16 v[64:79], v[148:151], v[116:119], v[64:79]\n"
"v_exp_f32 v100, v100\n"
"v_exp_f32 v101, v101\n"
"v_exp_f32 v102, v102\n"
"v_exp_f32 v103, v103\n"
"v_smfmac_f32_32x32x32_bf16 v[32:47], v[236:239], v[88:95], v254\n"
"v_exp_f32 v104, v104\n"
"v_exp_f32 v105, v105\n"
"v_exp_f32 v106, v106\n"
"v_exp_f32 v107, v107\n"
"v_smfmac_f32_32x32x32_bf16 v[48:63], v[232:235], v[96:103], v254\n"
"v_exp_f32 v108, v108\n"
"v_exp_f32 v109, v109\n"
"v_exp_f32 v110, v110\n"
"v_exp_f32 v111, v111\n"
"v_mfma_f32_32x32x16_bf16 v[80:95], v[144:147], v[120:123], 0\n"
"v_exp_f32 v64, v64\n"
"v_exp_f32 v65, v65\n"
"v_exp_f32 v66, v66\n"
"v_exp_f32 v67, v67\n"
"v_mfma_f32_32x32x16_bf16 v[80:95], v[148:151], v[124:127], v[80:95]\n"
"v_exp_f32 v68, v68\n"
"v_exp_f32 v69, v69\n"
"v_exp_f32 v70, v70\n"
"v_exp_f32 v71, v71\n"
"v_smfmac_f32_32x32x32_bf16 v[48:63], v[236:239], v[104:111], v254\n"
"buffer_load_dwordx4 v[224:227], v248, s[20:23], 0 offen offset:0\n"
"buffer_load_dwordx4 v[228:231], v248, s[20:23], 0 offen offset:64\n"
"v_exp_f32 v72, v72\n"
"v_exp_f32 v73, v73\n"
"v_exp_f32 v74, v74\n"
"v_exp_f32 v75, v75\n"
"v_smfmac_f32_32x32x32_bf16 v[0:15], v[152:155], v[64:71], v254\n"
"v_exp_f32 v76, v76\n"
"v_exp_f32 v77, v77\n"
"v_exp_f32 v78, v78\n"
"v_exp_f32 v79, v79\n"
"v_mfma_f32_32x32x16_bf16 v[96:111], v[144:147], v[128:131], 0\n"
"v_exp_f32 v80, v80\n"
"v_exp_f32 v81, v81\n"
"v_exp_f32 v82, v82\n"
"v_exp_f32 v83, v83\n"
"v_mfma_f32_32x32x16_bf16 v[96:111], v[148:151], v[132:135], v[96:111]\n"
"v_exp_f32 v84, v84\n"
"v_exp_f32 v85, v85\n"
"v_exp_f32 v86, v86\n"
"v_exp_f32 v87, v87\n"
"v_smfmac_f32_32x32x32_bf16 v[0:15], v[156:159], v[72:79], v254\n"
"v_exp_f32 v88, v88\n"
"v_exp_f32 v89, v89\n"
"v_exp_f32 v90, v90\n"
"v_exp_f32 v91, v91\n"
"v_smfmac_f32_32x32x32_bf16 v[16:31], v[152:155], v[80:87], v254\n"
"v_exp_f32 v92, v92\n"
"v_exp_f32 v93, v93\n"
"v_exp_f32 v94, v94\n"
"v_exp_f32 v95, v95\n"
"v_mfma_f32_32x32x16_bf16 v[64:79], v[144:147], v[136:139], 0\n"
"v_exp_f32 v96, v96\n"
"v_exp_f32 v97, v97\n"
"v_exp_f32 v98, v98\n"
"v_exp_f32 v99, v99\n"
"v_mfma_f32_32x32x16_bf16 v[64:79], v[148:151], v[140:143], v[64:79]\n"
"v_exp_f32 v100, v100\n"
"v_exp_f32 v101, v101\n"
"v_exp_f32 v102, v102\n"
"v_exp_f32 v103, v103\n"
"v_smfmac_f32_32x32x32_bf16 v[16:31], v[156:159], v[88:95], v254\n"
"v_exp_f32 v104, v104\n"
"v_exp_f32 v105, v105\n"
"v_exp_f32 v106, v106\n"
"v_exp_f32 v107, v107\n"
"v_smfmac_f32_32x32x32_bf16 v[32:47], v[152:155], v[96:103], v254\n"
"s_waitcnt vmcnt(10)\n"
"v_exp_f32 v108, v108\n"
"v_exp_f32 v109, v109\n"
"v_exp_f32 v110, v110\n"
"v_exp_f32 v111, v111\n"
"v_mfma_f32_32x32x16_bf16 v[80:95], v[160:163], v[112:115], 0\n"
"v_exp_f32 v64, v64\n"
"v_exp_f32 v65, v65\n"
"v_exp_f32 v66, v66\n"
"v_exp_f32 v67, v67\n"
"v_mfma_f32_32x32x16_bf16 v[80:95], v[164:167], v[116:119], v[80:95]\n"
"v_exp_f32 v68, v68\n"
"v_exp_f32 v69, v69\n"
"v_exp_f32 v70, v70\n"
"v_exp_f32 v71, v71\n"
"v_smfmac_f32_32x32x32_bf16 v[32:47], v[156:159], v[104:111], v254\n"
"v_exp_f32 v72, v72\n"
"v_exp_f32 v73, v73\n"
"v_exp_f32 v74, v74\n"
"v_exp_f32 v75, v75\n"
"v_smfmac_f32_32x32x32_bf16 v[48:63], v[152:155], v[64:71], v254\n"
"v_exp_f32 v76, v76\n"
"v_exp_f32 v77, v77\n"
"v_exp_f32 v78, v78\n"
"v_exp_f32 v79, v79\n"
"v_mfma_f32_32x32x16_bf16 v[96:111], v[160:163], v[120:123], 0\n"
"v_exp_f32 v80, v80\n"
"v_exp_f32 v81, v81\n"
"v_exp_f32 v82, v82\n"
"v_exp_f32 v83, v83\n"
"v_mfma_f32_32x32x16_bf16 v[96:111], v[164:167], v[124:127], v[96:111]\n"
"v_exp_f32 v84, v84\n"
"v_exp_f32 v85, v85\n"
"v_exp_f32 v86, v86\n"
"v_exp_f32 v87, v87\n"
"v_smfmac_f32_32x32x32_bf16 v[48:63], v[156:159], v[72:79], v254\n"
"v_exp_f32 v88, v88\n"
"v_exp_f32 v89, v89\n"
"v_exp_f32 v90, v90\n"
"v_exp_f32 v91, v91\n"
"v_smfmac_f32_32x32x32_bf16 v[0:15], v[168:171], v[80:87], v254\n"
"v_exp_f32 v92, v92\n"
"v_exp_f32 v93, v93\n"
"v_exp_f32 v94, v94\n"
"v_exp_f32 v95, v95\n"
"v_mfma_f32_32x32x16_bf16 v[64:79], v[160:163], v[128:131], 0\n"
"v_exp_f32 v96, v96\n"
"v_exp_f32 v97, v97\n"
"v_exp_f32 v98, v98\n"
"v_exp_f32 v99, v99\n"
"v_mfma_f32_32x32x16_bf16 v[64:79], v[164:167], v[132:135], v[64:79]\n"
"v_exp_f32 v100, v100\n"
"v_exp_f32 v101, v101\n"
"v_exp_f32 v102, v102\n"
"v_exp_f32 v103, v103\n"
"v_smfmac_f32_32x32x32_bf16 v[0:15], v[172:175], v[88:95], v254\n"
"v_exp_f32 v104, v104\n"
"v_exp_f32 v105, v105\n"
"v_exp_f32 v106, v106\n"
"v_exp_f32 v107, v107\n"
"v_smfmac_f32_32x32x32_bf16 v[16:31], v[168:171], v[96:103], v254\n"
"v_exp_f32 v108, v108\n"
"v_exp_f32 v109, v109\n"
"v_exp_f32 v110, v110\n"
"v_exp_f32 v111, v111\n"
"v_mfma_f32_32x32x16_bf16 v[80:95], v[160:163], v[136:139], 0\n"
"v_exp_f32 v64, v64\n"
"v_exp_f32 v65, v65\n"
"v_exp_f32 v66, v66\n"
"v_exp_f32 v67, v67\n"
"v_mfma_f32_32x32x16_bf16 v[80:95], v[164:167], v[140:143], v[80:95]\n"
"v_exp_f32 v68, v68\n"
"v_exp_f32 v69, v69\n"
"v_exp_f32 v70, v70\n"
"v_exp_f32 v71, v71\n"
"v_smfmac_f32_32x32x32_bf16 v[16:31], v[172:175], v[104:111], v254\n"
"v_exp_f32 v72, v72\n"
"v_exp_f32 v73, v73\n"
"v_exp_f32 v74, v74\n"
"v_exp_f32 v75, v75\n"
"v_smfmac_f32_32x32x32_bf16 v[32:47], v[168:171], v[64:71], v254\n"
"s_waitcnt vmcnt(6)\n"
"v_exp_f32 v76, v76\n"
"v_exp_f32 v77, v77\n"
"v_exp_f32 v78, v78\n"
"v_exp_f32 v79, v79\n"
"v_mfma_f32_32x32x16_bf16 v[96:111], v[176:179], v[112:115], 0\n"
"v_exp_f32 v80, v80\n"
"v_exp_f32 v81, v81\n"
"v_exp_f32 v82, v82\n"
"v_exp_f32 v83, v83\n"
"v_mfma_f32_32x32x16_bf16 v[96:111], v[180:183], v[116:119], v[96:111]\n"
"v_exp_f32 v84, v84\n"
"v_exp_f32 v85, v85\n"
"v_exp_f32 v86, v86\n"
"v_exp_f32 v87, v87\n"
"v_smfmac_f32_32x32x32_bf16 v[32:47], v[172:175], v[72:79], v254\n"
"v_exp_f32 v88, v88\n"
"v_exp_f32 v89, v89\n"
"v_exp_f32 v90, v90\n"
"v_exp_f32 v91, v91\n"
"v_smfmac_f32_32x32x32_bf16 v[48:63], v[168:171], v[80:87], v254\n"
"v_exp_f32 v92, v92\n"
"v_exp_f32 v93, v93\n"
"v_exp_f32 v94, v94\n"
"v_exp_f32 v95, v95\n"
"v_mfma_f32_32x32x16_bf16 v[64:79], v[176:179], v[120:123], 0\n"
"v_exp_f32 v96, v96\n"
"v_exp_f32 v97, v97\n"
"v_exp_f32 v98, v98\n"
"v_exp_f32 v99, v99\n"
"v_mfma_f32_32x32x16_bf16 v[64:79], v[180:183], v[124:127], v[64:79]\n"
"v_exp_f32 v100, v100\n"
"v_exp_f32 v101, v101\n"
"v_exp_f32 v102, v102\n"
"v_exp_f32 v103, v103\n"
"v_smfmac_f32_32x32x32_bf16 v[48:63], v[172:175], v[88:95], v254\n"
"v_exp_f32 v104, v104\n"
"v_exp_f32 v105, v105\n"
"v_exp_f32 v106, v106\n"
"v_exp_f32 v107, v107\n"
"v_smfmac_f32_32x32x32_bf16 v[0:15], v[184:187], v[96:103], v254\n"
"v_exp_f32 v108, v108\n"
"v_exp_f32 v109, v109\n"
"v_exp_f32 v110, v110\n"
"v_exp_f32 v111, v111\n"
"v_mfma_f32_32x32x16_bf16 v[80:95], v[176:179], v[128:131], 0\n"
"v_exp_f32 v64, v64\n"
"v_exp_f32 v65, v65\n"
"v_exp_f32 v66, v66\n"
"v_exp_f32 v67, v67\n"
"v_mfma_f32_32x32x16_bf16 v[80:95], v[180:183], v[132:135], v[80:95]\n"
"v_exp_f32 v68, v68\n"
"v_exp_f32 v69, v69\n"
"v_exp_f32 v70, v70\n"
"v_exp_f32 v71, v71\n"
"v_smfmac_f32_32x32x32_bf16 v[0:15], v[188:191], v[104:111], v254\n"
"v_exp_f32 v72, v72\n"
"v_exp_f32 v73, v73\n"
"v_exp_f32 v74, v74\n"
"v_exp_f32 v75, v75\n"
"v_smfmac_f32_32x32x32_bf16 v[16:31], v[184:187], v[64:71], v254\n"
"v_exp_f32 v76, v76\n"
"v_exp_f32 v77, v77\n"
"v_exp_f32 v78, v78\n"
"v_exp_f32 v79, v79\n"
"v_mfma_f32_32x32x16_bf16 v[96:111], v[176:179], v[136:139], 0\n"
"v_exp_f32 v80, v80\n"
"v_exp_f32 v81, v81\n"
"v_exp_f32 v82, v82\n"
"v_exp_f32 v83, v83\n"
"v_mfma_f32_32x32x16_bf16 v[96:111], v[180:183], v[140:143], v[96:111]\n"
"v_exp_f32 v84, v84\n"
"v_exp_f32 v85, v85\n"
"v_exp_f32 v86, v86\n"
"v_exp_f32 v87, v87\n"
"v_smfmac_f32_32x32x32_bf16 v[16:31], v[188:191], v[72:79], v254\n"
"v_exp_f32 v88, v88\n"
"v_exp_f32 v89, v89\n"
"v_exp_f32 v90, v90\n"
"v_exp_f32 v91, v91\n"
"v_smfmac_f32_32x32x32_bf16 v[32:47], v[184:187], v[80:87], v254\n"
"s_waitcnt vmcnt(2)\n"
"ds_read_b32 v243, v251 offset:40\n"
"v_exp_f32 v92, v92\n"
"v_exp_f32 v93, v93\n"
"v_exp_f32 v94, v94\n"
"v_exp_f32 v95, v95\n"
"v_mfma_f32_32x32x16_bf16 v[64:79], v[192:195], v[112:115], 0\n"
"v_exp_f32 v96, v96\n"
"v_exp_f32 v97, v97\n"
"v_exp_f32 v98, v98\n"
"v_exp_f32 v99, v99\n"
"v_mfma_f32_32x32x16_bf16 v[64:79], v[196:199], v[116:119], v[64:79]\n"
"v_exp_f32 v100, v100\n"
"v_exp_f32 v101, v101\n"
"v_exp_f32 v102, v102\n"
"v_exp_f32 v103, v103\n"
"v_smfmac_f32_32x32x32_bf16 v[32:47], v[188:191], v[88:95], v254\n"
"v_exp_f32 v104, v104\n"
"v_exp_f32 v105, v105\n"
"v_exp_f32 v106, v106\n"
"v_exp_f32 v107, v107\n"
"v_smfmac_f32_32x32x32_bf16 v[48:63], v[184:187], v[96:103], v254\n"
"v_exp_f32 v108, v108\n"
"v_exp_f32 v109, v109\n"
"v_exp_f32 v110, v110\n"
"v_exp_f32 v111, v111\n"
"v_mfma_f32_32x32x16_bf16 v[80:95], v[192:195], v[120:123], 0\n"
"v_exp_f32 v64, v64\n"
"v_exp_f32 v65, v65\n"
"v_exp_f32 v66, v66\n"
"v_exp_f32 v67, v67\n"
"v_mfma_f32_32x32x16_bf16 v[80:95], v[196:199], v[124:127], v[80:95]\n"
"v_exp_f32 v68, v68\n"
"v_exp_f32 v69, v69\n"
"v_exp_f32 v70, v70\n"
"v_exp_f32 v71, v71\n"
"v_smfmac_f32_32x32x32_bf16 v[48:63], v[188:191], v[104:111], v254\n"
"v_exp_f32 v72, v72\n"
"v_exp_f32 v73, v73\n"
"v_exp_f32 v74, v74\n"
"v_exp_f32 v75, v75\n"
"v_smfmac_f32_32x32x32_bf16 v[0:15], v[200:203], v[64:71], v254\n"
"s_waitcnt lgkmcnt(0)\n"
"v_cmp_gt_u32 vcc, 4, v243\n"
"s_nop 0\n"
"s_cbranch_vccnz Lcs81_%=\n"
"Lcb82_%=:\n"
"v_exp_f32 v76, v76\n"
"v_exp_f32 v77, v77\n"
"v_exp_f32 v78, v78\n"
"v_exp_f32 v79, v79\n"
"v_mfma_f32_32x32x16_bf16 v[96:111], v[192:195], v[128:131], 0\n"
"v_exp_f32 v80, v80\n"
"v_exp_f32 v81, v81\n"
"v_exp_f32 v82, v82\n"
"v_exp_f32 v83, v83\n"
"v_mfma_f32_32x32x16_bf16 v[96:111], v[196:199], v[132:135], v[96:111]\n"
"v_exp_f32 v84, v84\n"
"v_exp_f32 v85, v85\n"
"v_exp_f32 v86, v86\n"
"v_exp_f32 v87, v87\n"
"v_smfmac_f32_32x32x32_bf16 v[0:15], v[204:207], v[72:79], v254\n"
"v_exp_f32 v88, v88\n"
"v_exp_f32 v89, v89\n"
"v_exp_f32 v90, v90\n"
"v_exp_f32 v91, v91\n"
"v_smfmac_f32_32x32x32_bf16 v[16:31], v[200:203], v[80:87], v254\n"
"v_exp_f32 v92, v92\n"
"v_exp_f32 v93, v93\n"
"v_exp_f32 v94, v94\n"
"v_exp_f32 v95, v95\n"
"v_mfma_f32_32x32x16_bf16 v[64:79], v[192:195], v[136:139], 0\n"
"v_exp_f32 v96, v96\n"
"v_exp_f32 v97, v97\n"
"v_exp_f32 v98, v98\n"
"v_exp_f32 v99, v99\n"
"v_mfma_f32_32x32x16_bf16 v[64:79], v[196:199], v[140:143], v[64:79]\n"
"ds_write_b128 v245, v[0:3] offset:0\n"
"ds_write_b128 v245, v[4:7] offset:1024\n"
"v_exp_f32 v100, v100\n"
"v_exp_f32 v101, v101\n"
"v_exp_f32 v102, v102\n"
"v_exp_f32 v103, v103\n"
"v_smfmac_f32_32x32x32_bf16 v[16:31], v[204:207], v[88:95], v254\n"
"ds_write_b128 v245, v[8:11] offset:2048\n"
"ds_write_b128 v245, v[12:15] offset:3072\n"
"v_exp_f32 v104, v104\n"
"v_exp_f32 v105, v105\n"
"v_exp_f32 v106, v106\n"
"v_exp_f32 v107, v107\n"
"v_smfmac_f32_32x32x32_bf16 v[32:47], v[200:203], v[96:103], v254\n"
"v_exp_f32 v108, v108\n"
"v_exp_f32 v109, v109\n"
"v_exp_f32 v110, v110\n"
"v_exp_f32 v111, v111\n"
"v_exp_f32 v64, v64\n"
"v_exp_f32 v65, v65\n"
"v_exp_f32 v66, v66\n"
"v_exp_f32 v67, v67\n"
"ds_write_b128 v246, v[16:19] offset:0\n"
"ds_write_b128 v246, v[20:23] offset:1024\n"
"v_exp_f32 v68, v68\n"
"v_exp_f32 v69, v69\n"
"v_exp_f32 v70, v70\n"
"v_exp_f32 v71, v71\n"
"v_smfmac_f32_32x32x32_bf16 v[32:47], v[204:207], v[104:111], v254\n"
"ds_write_b128 v246, v[24:27] offset:2048\n"
"ds_write_b128 v246, v[28:31] offset:3072\n"
"v_exp_f32 v72, v72\n"
"v_exp_f32 v73, v73\n"
"v_exp_f32 v74, v74\n"
"v_exp_f32 v75, v75\n"
"v_smfmac_f32_32x32x32_bf16 v[48:63], v[200:203], v[64:71], v254\n"
"v_exp_f32 v76, v76\n"
"v_exp_f32 v77, v77\n"
"v_exp_f32 v78, v78\n"
"v_exp_f32 v79, v79\n"
"s_nop 1\n"
"v_smfmac_f32_32x32x32_bf16 v[48:63], v[204:207], v[72:79], v254\n"
"ds_write_b128 v247, v[32:35] offset:0\n"
"ds_write_b128 v247, v[36:39] offset:1024\n"
"ds_write_b128 v247, v[40:43] offset:2048\n"
"ds_write_b128 v247, v[44:47] offset:3072\n"
"s_setprio 2\n"
"s_waitcnt lgkmcnt(0)\n"
"s_barrier\n"
"ds_read_b128 v[0:3], v250 offset:0\n"
"ds_read_b128 v[4:7], v250 offset:1024\n"
"ds_read_b128 v[8:11], v250 offset:2048\n"
"ds_read_b128 v[12:15], v250 offset:3072\n"
"ds_read_b128 v[16:19], v250 offset:4096\n"
"ds_read_b128 v[20:23], v250 offset:5120\n"
"ds_read_b128 v[24:27], v250 offset:6144\n"
"ds_read_b128 v[28:31], v250 offset:7168\n"
"ds_read_b128 v[32:35], v250 offset:8192\n"
"ds_read_b128 v[36:39], v250 offset:9216\n"
"ds_read_b128 v[40:43], v250 offset:10240\n"
"ds_read_b128 v[44:47], v250 offset:11264\n"
"s_waitcnt lgkmcnt(8)\n"
"v_add_f32 v48, v48, v0\n"
"v_add_f32 v49, v49, v1\n"
"v_add_f32 v50, v50, v2\n"
"v_add_f32 v51, v51, v3\n"
"v_add_f32 v52, v52, v4\n"
"v_add_f32 v53, v53, v5\n"
"v_add_f32 v54, v54, v6\n"
"v_add_f32 v55, v55, v7\n"
"v_add_f32 v56, v56, v8\n"
"v_add_f32 v57, v57, v9\n"
"v_add_f32 v58, v58, v10\n"
"v_add_f32 v59, v59, v11\n"
"v_add_f32 v60, v60, v12\n"
"v_add_f32 v61, v61, v13\n"
"v_add_f32 v62, v62, v14\n"
"v_add_f32 v63, v63, v15\n"
"s_waitcnt lgkmcnt(4)\n"
"v_add_f32 v48, v48, v16\n"
"v_add_f32 v49, v49, v17\n"
"v_add_f32 v50, v50, v18\n"
"v_add_f32 v51, v51, v19\n"
"v_add_f32 v52, v52, v20\n"
"v_add_f32 v53, v53, v21\n"
"v_add_f32 v54, v54, v22\n"
"v_add_f32 v55, v55, v23\n"
"v_add_f32 v56, v56, v24\n"
"v_add_f32 v57, v57, v25\n"
"v_add_f32 v58, v58, v26\n"
"v_add_f32 v59, v59, v27\n"
"v_add_f32 v60, v60, v28\n"
"v_add_f32 v61, v61, v29\n"
"v_add_f32 v62, v62, v30\n"
"v_add_f32 v63, v63, v31\n"
"s_waitcnt lgkmcnt(0)\n"
"v_add_f32 v48, v48, v32\n"
"v_add_f32 v49, v49, v33\n"
"v_add_f32 v50, v50, v34\n"
"v_add_f32 v51, v51, v35\n"
"v_add_f32 v52, v52, v36\n"
"v_add_f32 v53, v53, v37\n"
"v_add_f32 v54, v54, v38\n"
"v_add_f32 v55, v55, v39\n"
"v_add_f32 v56, v56, v40\n"
"v_add_f32 v57, v57, v41\n"
"v_add_f32 v58, v58, v42\n"
"v_add_f32 v59, v59, v43\n"
"v_add_f32 v60, v60, v44\n"
"v_add_f32 v61, v61, v45\n"
"v_add_f32 v62, v62, v46\n"
"v_add_f32 v63, v63, v47\n"
"v_mov_b32 v240, v60\n"
"v_mov_b32 v241, v60\n"
"v_add_f32_e64 v242, |v48|, |v49|\n"
"v_add_f32_e64 v243, |v60|, |v61|\n"
"v_permlane32_swap_b32 v240, v241\n"
"v_rcp_f32 v240, v241\n"
"v_add_f32_e64 v244, |v50|, |v51|\n"
"v_add_f32_e64 v242, v242, |v52|\n"
"v_add_f32_e64 v244, v244, |v53|\n"
"v_add_f32_e64 v242, v242, |v54|\n"
"v_add_f32_e64 v244, v244, |v55|\n"
"v_add_f32_e64 v242, v242, |v56|\n"
"v_add_f32_e64 v244, v244, |v57|\n"
"v_add_f32_e64 v242, v242, |v58|\n"
"v_add_f32_e64 v244, v244, |v59|\n"
"v_add_f32_e64 v243, v243, |v62|\n"
"v_add_f32 v242, v242, v244\n"
"v_add_f32_e64 v243, v243, |v63|\n"
"v_add_f32 v243, v243, v242\n"
"v_mul_f32 v244, 0x3a000000, v241\n"
"v_min_f32 v242, v242, v244\n"
"s_mov_b32 s86, 0x07800000\n"
"v_cmp_nle_f32 s[84:85], s86, v242\n"
"v_cmp_nge_f32 vcc, 0x7b800000, v243\n"
"v_mul_f32 v0, v240, v48\n"
"v_mul_f32 v1, v240, v49\n"
"v_mul_f32 v2, v240, v50\n"
"v_mul_f32 v3, v240, v51\n"
"s_or_b64 vcc, vcc, s[84:85]\n"
"s_cbranch_vccnz Lfb84_%=\n"
"v_mul_f32 v4, v240, v52\n"
"v_mul_f32 v5, v240, v53\n"
"v_mul_f32 v6, v240, v54\n"
"v_mul_f32 v7, v240, v55\n"
"v_mul_f32 v8, v240, v56\n"
"v_mul_f32 v9, v240, v57\n"
"v_mul_f32 v10, v240, v58\n"
"v_mul_f32 v11, v240, v59\n"
"v_mul_f32 v12, v240, v60\n"
"v_mul_f32 v13, v240, v61\n"
"v_mul_f32 v14, v240, v62\n"
"v_mul_f32 v15, v240, v63\n"
"v_cvt_pk_bf16_f32 v16, v0, v1\n"
"v_cvt_pk_bf16_f32 v17, v2, v3\n"
"v_cvt_pk_bf16_f32 v18, v4, v5\n"
"v_cvt_pk_bf16_f32 v19, v6, v7\n"
"v_cvt_pk_bf16_f32 v20, v8, v9\n"
"v_cvt_pk_bf16_f32 v21, v10, v11\n"
"v_cvt_pk_bf16_f32 v22, v12, v13\n"
"v_cvt_pk_bf16_f32 v23, v14, v15\n"
"Lfk85_%=:\n"
"buffer_store_dwordx4 v[16:19], v253, s[48:51], s17 offen offset:0 sc1\n"
"s_mov_b32 exec_lo, 0\n"
"v_mov_b32 v22, 0\n"
"v_mov_b32 v23, 0\n"
"s_mov_b32 exec_lo, -1\n"
"buffer_store_dwordx4 v[20:23], v253, s[48:51], s17 offen offset:1024 sc1\n"
"s_cmp_lt_u32 s66, s83\n"
"s_cbranch_scc1 LAE88_%=\n"
"s_endpgm\n"
"LAE88_%=:\n"
"s_mov_b64 exec, 1\n"
"ds_add_u32 v251, v252 offset:28\n"
"s_mov_b64 exec, -1\n"
"s_waitcnt vmcnt(2)\n"
"v_permlane16_swap_b32 v224, v226\n"
"v_permlane16_swap_b32 v225, v227\n"
"v_permlane16_swap_b32 v228, v230\n"
"v_permlane16_swap_b32 v229, v231\n"
"v_lshlrev_b32 v30, 16, v231\n"
"v_and_b32 v31, 0xffff0000, v231\n"
"v_lshlrev_b32 v28, 16, v230\n"
"v_and_b32 v29, 0xffff0000, v230\n"
"v_lshlrev_b32 v26, 16, v229\n"
"v_and_b32 v27, 0xffff0000, v229\n"
"v_lshlrev_b32 v24, 16, v228\n"
"v_and_b32 v25, 0xffff0000, v228\n"
"v_lshlrev_b32 v22, 16, v227\n"
"v_and_b32 v23, 0xffff0000, v227\n"
"v_lshlrev_b32 v20, 16, v226\n"
"v_and_b32 v21, 0xffff0000, v226\n"
"v_lshlrev_b32 v18, 16, v225\n"
"v_and_b32 v19, 0xffff0000, v225\n"
"v_lshlrev_b32 v16, 16, v224\n"
"v_and_b32 v17, 0xffff0000, v224\n"
"v_lshrrev_b32 v224, 4, v253\n"
"v_lshrrev_b32 v232, 5, v224\n"
"v_bfe_u32 v233, v224, 4, 1\n"
"v_bfe_u32 v234, v224, 2, 2\n"
"v_and_b32 v235, 3, v224\n"
"v_and_b32 v236, 2, v234\n"
"v_or_b32 v236, v236, v233\n"
"s_lshl_b32 s85, s75, 7\n"
"v_lshrrev_b32 v241, 1, v234\n"
"v_lshl_or_b32 v241, v233, 1, v241\n"
"v_lshlrev_b32 v241, 5, v241\n"
"v_lshl_or_b32 v241, v235, 3, v241\n"
"v_lshl_or_b32 v241, v234, 7, v241\n"
"v_lshl_or_b32 v241, v233, 9, v241\n"
"v_lshl_or_b32 v241, v232, 11, v241\n"
"v_add_u32 v241, 49152, v241\n"
"v_xor_b32 v242, 32, v241\n"
"v_xor_b32 v243, 64, v241\n"
"v_xor_b32 v244, 96, v241\n"
"s_lshl1_add_u32 s84, s74, s8\n"
"s_lshl_b32 s84, s84, 15\n"
"s_lshl_b32 s87, s9, 8\n"
"s_add_u32 s84, s84, s87\n"
"s_add_u32 s84, s84, 16777216\n"
"v_and_b32 v240, 15, v224\n"
"v_lshl_add_u32 v245, v240, 4, s84\n"
"v_lshrrev_b32 v247, 4, v224\n"
"v_lshl_add_u32 v245, v247, 9, v245\n"
"s_add_u32 s84, s84, 1024\n"
"v_lshl_add_u32 v246, v224, 11, s84\n"
"s_mul_i32 s84, s70, 4352\n"
"s_movk_i32 s87, 272\n"
"v_lshl_add_u32 v247, v247, 4, s84\n"
"v_mad_u32_u24 v247, v240, s87, v247\n"
"s_mov_b32 s18, 400000\n"
"ds_read_b64_tr_b16 v[96:97], v241 offset:0\n"
"ds_read_b64_tr_b16 v[98:99], v241 offset:1024\n"
"ds_read_b64_tr_b16 v[100:101], v242 offset:0\n"
"ds_read_b64_tr_b16 v[102:103], v242 offset:1024\n"
"ds_read_b64_tr_b16 v[104:105], v243 offset:0\n"
"ds_read_b64_tr_b16 v[106:107], v243 offset:1024\n"
"ds_read_b64_tr_b16 v[108:109], v244 offset:0\n"
"ds_read_b64_tr_b16 v[110:111], v244 offset:1024\n"
"ds_read_b64_tr_b16 v[112:113], v243 offset:3584\n"
"ds_read_b64_tr_b16 v[114:115], v243 offset:4608\n"
"ds_read_b64_tr_b16 v[116:117], v244 offset:3584\n"
"ds_read_b64_tr_b16 v[118:119], v244 offset:4608\n"
"ds_read_b64_tr_b16 v[120:121], v241 offset:3584\n"
"ds_read_b64_tr_b16 v[122:123], v241 offset:4608\n"
"ds_read_b64_tr_b16 v[124:125], v242 offset:3584\n"
"ds_read_b64_tr_b16 v[126:127], v242 offset:4608\n"
"ds_read_b64_tr_b16 v[128:129], v241 offset:7168\n"
"ds_read_b64_tr_b16 v[130:131], v241 offset:8192\n"
"ds_read_b64_tr_b16 v[132:133], v242 offset:7168\n"
"ds_read_b64_tr_b16 v[134:135], v242 offset:8192\n"
"ds_read_b64_tr_b16 v[136:137], v243 offset:7168\n"
"ds_read_b64_tr_b16 v[138:139], v243 offset:8192\n"
"ds_read_b64_tr_b16 v[140:141], v244 offset:7168\n"
"ds_read_b64_tr_b16 v[142:143], v244 offset:8192\n"
"ds_read_b64_tr_b16 v[144:145], v243 offset:10752\n"
"ds_read_b64_tr_b16 v[146:147], v243 offset:11776\n"
"ds_read_b64_tr_b16 v[148:149], v244 offset:10752\n"
"ds_read_b64_tr_b16 v[150:151], v244 offset:11776\n"
"ds_read_b64_tr_b16 v[152:153], v241 offset:10752\n"
"ds_read_b64_tr_b16 v[154:155], v241 offset:11776\n"
"ds_read_b64_tr_b16 v[156:157], v242 offset:10752\n"
"ds_read_b64_tr_b16 v[158:159], v242 offset:11776\n"
"s_mov_b64 exec, 0xffff\n"
"buffer_load_dword v249, v246, s[48:51], 0 offen sc1\n"
"s_mov_b64 exec, -1\n"
"ds_read_b64_tr_b16 v[160:161], v241 offset:14336\n"
"ds_read_b64_tr_b16 v[162:163], v241 offset:15360\n"
"ds_read_b64_tr_b16 v[164:165], v242 offset:14336\n"
"ds_read_b64_tr_b16 v[166:167], v242 offset:15360\n"
"ds_read_b64_tr_b16 v[168:169], v243 offset:14336\n"
"ds_read_b64_tr_b16 v[170:171], v243 offset:15360\n"
"ds_read_b64_tr_b16 v[172:173], v244 offset:14336\n"
"ds_read_b64_tr_b16 v[174:175], v244 offset:15360\n"
"ds_read_b64_tr_b16 v[176:177], v243 offset:17920\n"
"ds_read_b64_tr_b16 v[178:179], v243 offset:18944\n"
"ds_read_b64_tr_b16 v[180:181], v244 offset:17920\n"
"ds_read_b64_tr_b16 v[182:183], v244 offset:18944\n"
"ds_read_b64_tr_b16 v[184:185], v241 offset:17920\n"
"ds_read_b64_tr_b16 v[186:187], v241 offset:18944\n"
"ds_read_b64_tr_b16 v[188:189], v242 offset:17920\n"
"ds_read_b64_tr_b16 v[190:191], v242 offset:18944\n"
"ds_read_b64_tr_b16 v[192:193], v241 offset:21504\n"
"ds_read_b64_tr_b16 v[194:195], v241 offset:22528\n"
"ds_read_b64_tr_b16 v[196:197], v242 offset:21504\n"
"ds_read_b64_tr_b16 v[198:199], v242 offset:22528\n"
"ds_read_b64_tr_b16 v[200:201], v243 offset:21504\n"
"ds_read_b64_tr_b16 v[202:203], v243 offset:22528\n"
"ds_read_b64_tr_b16 v[204:205], v244 offset:21504\n"
"ds_read_b64_tr_b16 v[206:207], v244 offset:22528\n"
"ds_read_b64_tr_b16 v[208:209], v243 offset:25088\n"
"ds_read_b64_tr_b16 v[210:211], v243 offset:26112\n"
"ds_read_b64_tr_b16 v[212:213], v244 offset:25088\n"
"ds_read_b64_tr_b16 v[214:215], v244 offset:26112\n"
"ds_read_b64_tr_b16 v[216:217], v241 offset:25088\n"
"ds_read_b64_tr_b16 v[218:219], v241 offset:26112\n"
"ds_read_b64_tr_b16 v[220:221], v242 offset:25088\n"
"ds_read_b64_tr_b16 v[222:223], v242 offset:26112\n"
"s_waitcnt vmcnt(0)\n"
"s_mov_b64 exec, 0xffff\n"
"v_cmp_eq_u32 vcc, -1, v249\n"
"s_mov_b64 exec, -1\n"
"s_cbranch_vccz Lgo89_%=\n"
"LRE90_%=:\n"
"Lpl91_%=:\n"
"s_mov_b64 exec, 0xffff\n"
"buffer_load_dword v249, v246, s[48:51], 0 offen sc1\n"
"s_waitcnt vmcnt(0)\n"
"v_cmp_eq_u32 vcc, -1, v249\n"
"s_mov_b64 exec, -1\n"
"s_sub_u32 s18, s18, 1\n"
"s_cbranch_scc1 Lgo89_%=\n"
"s_cbranch_vccz Lpd92_%=\n"
"s_sleep 0\n"
"s_branch Lpl91_%=\n"
"Lpd92_%=:\n"
"Lgo89_%=:\n"
"buffer_load_dwordx4 v[32:35], v245, s[48:51], 0 offen offset:0 sc1\n"
"buffer_load_dwordx4 v[36:39], v245, s[48:51], 0 offen offset:2048 sc1\n"
"s_movk_i32 s91, 4096\n"
"buffer_load_dwordx4 v[40:43], v245, s[48:51], s91 offen offset:0 sc1\n"
"buffer_load_dwordx4 v[44:47], v245, s[48:51], s91 offen offset:2048 sc1\n"
"s_movk_i32 s91, 8192\n"
"buffer_load_dwordx4 v[48:51], v245, s[48:51], s91 offen offset:0 sc1\n"
"buffer_load_dwordx4 v[52:55], v245, s[48:51], s91 offen offset:2048 sc1\n"
"s_movk_i32 s91, 12288\n"
"buffer_load_dwordx4 v[56:59], v245, s[48:51], s91 offen offset:0 sc1\n"
"buffer_load_dwordx4 v[60:63], v245, s[48:51], s91 offen offset:2048 sc1\n"
"s_movk_i32 s91, 16384\n"
"buffer_load_dwordx4 v[64:67], v245, s[48:51], s91 offen offset:0 sc1\n"
"buffer_load_dwordx4 v[68:71], v245, s[48:51], s91 offen offset:2048 sc1\n"
"v_mov_b32 v249, 0\n"
"s_waitcnt vmcnt(9) lgkmcnt(0)\n"
"s_movk_i32 s91, 20480\n"
"buffer_load_dwordx4 v[72:75], v245, s[48:51], s91 offen offset:0 sc1\n"
"v_mfma_f32_16x16x32_bf16 v[0:3], v[96:99], v[32:35], 0\n"
"v_max3_u32 v249, v249, v32, v33\n"
"ds_read_b64_tr_b16 v[96:97], v241 offset:28672\n"
"v_mfma_f32_16x16x32_bf16 v[4:7], v[100:103], v[32:35], 0\n"
"v_max3_u32 v249, v249, v34, v35\n"
"ds_read_b64_tr_b16 v[98:99], v241 offset:29696\n"
"v_mfma_f32_16x16x32_bf16 v[8:11], v[104:107], v[32:35], 0\n"
"ds_read_b64_tr_b16 v[100:101], v242 offset:28672\n"
"ds_read_b64_tr_b16 v[102:103], v242 offset:29696\n"
"v_mfma_f32_16x16x32_bf16 v[12:15], v[108:111], v[32:35], 0\n"
"ds_read_b64_tr_b16 v[104:105], v243 offset:28672\n"
"ds_read_b64_tr_b16 v[106:107], v243 offset:29696\n"
"s_waitcnt vmcnt(9)\n"
"buffer_load_dwordx4 v[76:79], v245, s[48:51], s91 offen offset:2048 sc1\n"
"v_mfma_f32_16x16x32_bf16 v[0:3], v[112:115], v[36:39], v[0:3]\n"
"v_max3_u32 v249, v249, v36, v37\n"
"ds_read_b64_tr_b16 v[108:109], v244 offset:28672\n"
"v_mfma_f32_16x16x32_bf16 v[4:7], v[116:119], v[36:39], v[4:7]\n"
"v_max3_u32 v249, v249, v38, v39\n"
"ds_read_b64_tr_b16 v[110:111], v244 offset:29696\n"
"v_mfma_f32_16x16x32_bf16 v[8:11], v[120:123], v[36:39], v[8:11]\n"
"ds_read_b64_tr_b16 v[112:113], v243 offset:32256\n"
"ds_read_b64_tr_b16 v[114:115], v243 offset:33280\n"
"v_mfma_f32_16x16x32_bf16 v[12:15], v[124:127], v[36:39], v[12:15]\n"
"ds_read_b64_tr_b16 v[116:117], v244 offset:32256\n"
"ds_read_b64_tr_b16 v[118:119], v244 offset:33280\n"
"s_waitcnt vmcnt(9)\n"
"s_movk_i32 s91, 24576\n"
"buffer_load_dwordx4 v[80:83], v245, s[48:51], s91 offen offset:0 sc1\n"
"v_mfma_f32_16x16x32_bf16 v[0:3], v[128:131], v[40:43], v[0:3]\n"
"v_max3_u32 v249, v249, v40, v41\n"
"ds_read_b64_tr_b16 v[120:121], v241 offset:32256\n"
"v_mfma_f32_16x16x32_bf16 v[4:7], v[132:135], v[40:43], v[4:7]\n"
"v_max3_u32 v249, v249, v42, v43\n"
"ds_read_b64_tr_b16 v[122:123], v241 offset:33280\n"
"v_mfma_f32_16x16x32_bf16 v[8:11], v[136:139], v[40:43], v[8:11]\n"
"ds_read_b64_tr_b16 v[124:125], v242 offset:32256\n"
"ds_read_b64_tr_b16 v[126:127], v242 offset:33280\n"
"v_mfma_f32_16x16x32_bf16 v[12:15], v[140:143], v[40:43], v[12:15]\n"
"ds_read_b64_tr_b16 v[128:129], v241 offset:35840\n"
"ds_read_b64_tr_b16 v[130:131], v241 offset:36864\n"
"s_waitcnt vmcnt(9)\n"
"buffer_load_dwordx4 v[84:87], v245, s[48:51], s91 offen offset:2048 sc1\n"
"v_mfma_f32_16x16x32_bf16 v[0:3], v[144:147], v[44:47], v[0:3]\n"
"v_max3_u32 v249, v249, v44, v45\n"
"ds_read_b64_tr_b16 v[132:133], v242 offset:35840\n"
"v_mfma_f32_16x16x32_bf16 v[4:7], v[148:151], v[44:47], v[4:7]\n"
"v_max3_u32 v249, v249, v46, v47\n"
"ds_read_b64_tr_b16 v[134:135], v242 offset:36864\n"
"v_mfma_f32_16x16x32_bf16 v[8:11], v[152:155], v[44:47], v[8:11]\n"
"ds_read_b64_tr_b16 v[136:137], v243 offset:35840\n"
"ds_read_b64_tr_b16 v[138:139], v243 offset:36864\n"
"v_mfma_f32_16x16x32_bf16 v[12:15], v[156:159], v[44:47], v[12:15]\n"
"ds_read_b64_tr_b16 v[140:141], v244 offset:35840\n"
"ds_read_b64_tr_b16 v[142:143], v244 offset:36864\n"
"s_waitcnt vmcnt(9)\n"
"s_movk_i32 s91, 28672\n"
"buffer_load_dwordx4 v[88:91], v245, s[48:51], s91 offen offset:0 sc1\n"
"v_mfma_f32_16x16x32_bf16 v[0:3], v[160:163], v[48:51], v[0:3]\n"
"v_max3_u32 v249, v249, v48, v49\n"
"ds_read_b64_tr_b16 v[144:145], v243 offset:39424\n"
"v_mfma_f32_16x16x32_bf16 v[4:7], v[164:167], v[48:51], v[4:7]\n"
"v_max3_u32 v249, v249, v50, v51\n"
"ds_read_b64_tr_b16 v[146:147], v243 offset:40448\n"
"v_mfma_f32_16x16x32_bf16 v[8:11], v[168:171], v[48:51], v[8:11]\n"
"ds_read_b64_tr_b16 v[148:149], v244 offset:39424\n"
"ds_read_b64_tr_b16 v[150:151], v244 offset:40448\n"
"v_mfma_f32_16x16x32_bf16 v[12:15], v[172:175], v[48:51], v[12:15]\n"
"ds_read_b64_tr_b16 v[152:153], v241 offset:39424\n"
"ds_read_b64_tr_b16 v[154:155], v241 offset:40448\n"
"s_waitcnt vmcnt(9)\n"
"buffer_load_dwordx4 v[92:95], v245, s[48:51], s91 offen offset:2048 sc1\n"
"v_mfma_f32_16x16x32_bf16 v[0:3], v[176:179], v[52:55], v[0:3]\n"
"v_max3_u32 v249, v249, v52, v53\n"
"ds_read_b64_tr_b16 v[156:157], v242 offset:39424\n"
"v_mfma_f32_16x16x32_bf16 v[4:7], v[180:183], v[52:55], v[4:7]\n"
"v_max3_u32 v249, v249, v54, v55\n"
"ds_read_b64_tr_b16 v[158:159], v242 offset:40448\n"
"v_mfma_f32_16x16x32_bf16 v[8:11], v[184:187], v[52:55], v[8:11]\n"
"ds_read_b64_tr_b16 v[160:161], v241 offset:43008\n"
"ds_read_b64_tr_b16 v[162:163], v241 offset:44032\n"
"v_mfma_f32_16x16x32_bf16 v[12:15], v[188:191], v[52:55], v[12:15]\n"
"ds_read_b64_tr_b16 v[164:165], v242 offset:43008\n"
"ds_read_b64_tr_b16 v[166:167], v242 offset:44032\n"
"s_waitcnt vmcnt(9)\n"
"v_mfma_f32_16x16x32_bf16 v[0:3], v[192:195], v[56:59], v[0:3]\n"
"v_max3_u32 v249, v249, v56, v57\n"
"ds_read_b64_tr_b16 v[168:169], v243 offset:43008\n"
"v_mfma_f32_16x16x32_bf16 v[4:7], v[196:199], v[56:59], v[4:7]\n"
"v_max3_u32 v249, v249, v58, v59\n"
"ds_read_b64_tr_b16 v[170:171], v243 offset:44032\n"
"v_mfma_f32_16x16x32_bf16 v[8:11], v[200:203], v[56:59], v[8:11]\n"
"ds_read_b64_tr_b16 v[172:173], v244 offset:43008\n"
"ds_read_b64_tr_b16 v[174:175], v244 offset:44032\n"
"v_mfma_f32_16x16x32_bf16 v[12:15], v[204:207], v[56:59], v[12:15]\n"
"ds_read_b64_tr_b16 v[176:177], v243 offset:46592\n"
"ds_read_b64_tr_b16 v[178:179], v243 offset:47616\n"
"s_waitcnt vmcnt(8)\n"
"v_mfma_f32_16x16x32_bf16 v[0:3], v[208:211], v[60:63], v[0:3]\n"
"v_max3_u32 v249, v249, v60, v61\n"
"ds_read_b64_tr_b16 v[180:181], v244 offset:46592\n"
"v_mfma_f32_16x16x32_bf16 v[4:7], v[212:215], v[60:63], v[4:7]\n"
"v_max3_u32 v249, v249, v62, v63\n"
"ds_read_b64_tr_b16 v[182:183], v244 offset:47616\n"
"v_mfma_f32_16x16x32_bf16 v[8:11], v[216:219], v[60:63], v[8:11]\n"
"ds_read_b64_tr_b16 v[184:185], v241 offset:46592\n"
"ds_read_b64_tr_b16 v[186:187], v241 offset:47616\n"
"v_mfma_f32_16x16x32_bf16 v[12:15], v[220:223], v[60:63], v[12:15]\n"
"ds_read_b64_tr_b16 v[188:189], v242 offset:46592\n"
"ds_read_b64_tr_b16 v[190:191], v242 offset:47616\n"
"s_waitcnt vmcnt(7) lgkmcnt(15)\n"
"v_mfma_f32_16x16x32_bf16 v[0:3], v[96:99], v[64:67], v[0:3]\n"
"v_max3_u32 v249, v249, v64, v65\n"
"ds_read_b64_tr_b16 v[192:193], v241 offset:50176\n"
"v_mfma_f32_16x16x32_bf16 v[4:7], v[100:103], v[64:67], v[4:7]\n"
"v_max3_u32 v249, v249, v66, v67\n"
"ds_read_b64_tr_b16 v[194:195], v241 offset:51200\n"
"v_mfma_f32_16x16x32_bf16 v[8:11], v[104:107], v[64:67], v[8:11]\n"
"ds_read_b64_tr_b16 v[196:197], v242 offset:50176\n"
"ds_read_b64_tr_b16 v[198:199], v242 offset:51200\n"
"v_mfma_f32_16x16x32_bf16 v[12:15], v[108:111], v[64:67], v[12:15]\n"
"ds_read_b64_tr_b16 v[200:201], v243 offset:50176\n"
"ds_read_b64_tr_b16 v[202:203], v243 offset:51200\n"
"s_waitcnt vmcnt(6) lgkmcnt(15)\n"
"v_mfma_f32_16x16x32_bf16 v[0:3], v[112:115], v[68:71], v[0:3]\n"
"v_max3_u32 v249, v249, v68, v69\n"
"ds_read_b64_tr_b16 v[204:205], v244 offset:50176\n"
"v_mfma_f32_16x16x32_bf16 v[4:7], v[116:119], v[68:71], v[4:7]\n"
"v_max3_u32 v249, v249, v70, v71\n"
"ds_read_b64_tr_b16 v[206:207], v244 offset:51200\n"
"v_mfma_f32_16x16x32_bf16 v[8:11], v[120:123], v[68:71], v[8:11]\n"
"ds_read_b64_tr_b16 v[208:209], v243 offset:53760\n"
"ds_read_b64_tr_b16 v[210:211], v243 offset:54784\n"
"v_mfma_f32_16x16x32_bf16 v[12:15], v[124:127], v[68:71], v[12:15]\n"
"ds_read_b64_tr_b16 v[212:213], v244 offset:53760\n"
"ds_read_b64_tr_b16 v[214:215], v244 offset:54784\n"
"s_waitcnt vmcnt(5) lgkmcnt(15)\n"
"v_mfma_f32_16x16x32_bf16 v[0:3], v[128:131], v[72:75], v[0:3]\n"
"v_max3_u32 v249, v249, v72, v73\n"
"ds_read_b64_tr_b16 v[216:217], v241 offset:53760\n"
"v_mfma_f32_16x16x32_bf16 v[4:7], v[132:135], v[72:75], v[4:7]\n"
"v_max3_u32 v249, v249, v74, v75\n"
"ds_read_b64_tr_b16 v[218:219], v241 offset:54784\n"
"v_mfma_f32_16x16x32_bf16 v[8:11], v[136:139], v[72:75], v[8:11]\n"
"ds_read_b64_tr_b16 v[220:221], v242 offset:53760\n"
"ds_read_b64_tr_b16 v[222:223], v242 offset:54784\n"
"v_mfma_f32_16x16x32_bf16 v[12:15], v[140:143], v[72:75], v[12:15]\n"
"s_waitcnt vmcnt(4) lgkmcnt(15)\n"
"v_mfma_f32_16x16x32_bf16 v[0:3], v[144:147], v[76:79], v[0:3]\n"
"v_max3_u32 v249, v249, v76, v77\n"
"v_mfma_f32_16x16x32_bf16 v[4:7], v[148:151], v[76:79], v[4:7]\n"
"v_max3_u32 v249, v249, v78, v79\n"
"v_mfma_f32_16x16x32_bf16 v[8:11], v[152:155], v[76:79], v[8:11]\n"
"v_mfma_f32_16x16x32_bf16 v[12:15], v[156:159], v[76:79], v[12:15]\n"
"s_waitcnt vmcnt(3) lgkmcnt(15)\n"
"v_mfma_f32_16x16x32_bf16 v[0:3], v[160:163], v[80:83], v[0:3]\n"
"v_max3_u32 v249, v249, v80, v81\n"
"v_mfma_f32_16x16x32_bf16 v[4:7], v[164:167], v[80:83], v[4:7]\n"
"v_max3_u32 v249, v249, v82, v83\n"
"v_mfma_f32_16x16x32_bf16 v[8:11], v[168:171], v[80:83], v[8:11]\n"
"v_mfma_f32_16x16x32_bf16 v[12:15], v[172:175], v[80:83], v[12:15]\n"
"s_waitcnt vmcnt(2) lgkmcnt(15)\n"
"v_mfma_f32_16x16x32_bf16 v[0:3], v[176:179], v[84:87], v[0:3]\n"
"v_max3_u32 v249, v249, v84, v85\n"
"v_mfma_f32_16x16x32_bf16 v[4:7], v[180:183], v[84:87], v[4:7]\n"
"v_max3_u32 v249, v249, v86, v87\n"
"v_mfma_f32_16x16x32_bf16 v[8:11], v[184:187], v[84:87], v[8:11]\n"
"v_mfma_f32_16x16x32_bf16 v[12:15], v[188:191], v[84:87], v[12:15]\n"
"ds_read_b32 v250, v251 offset:32\n"
"s_waitcnt vmcnt(1) lgkmcnt(9)\n"
"v_mfma_f32_16x16x32_bf16 v[0:3], v[192:195], v[88:91], v[0:3]\n"
"v_max3_u32 v249, v249, v88, v89\n"
"v_mfma_f32_16x16x32_bf16 v[4:7], v[196:199], v[88:91], v[4:7]\n"
"v_max3_u32 v249, v249, v90, v91\n"
"v_mfma_f32_16x16x32_bf16 v[8:11], v[200:203], v[88:91], v[8:11]\n"
"v_mfma_f32_16x16x32_bf16 v[12:15], v[204:207], v[88:91], v[12:15]\n"
"s_waitcnt vmcnt(0) lgkmcnt(1)\n"
"v_mfma_f32_16x16x32_bf16 v[0:3], v[208:211], v[92:95], v[0:3]\n"
"v_max3_u32 v249, v249, v92, v93\n"
"v_mfma_f32_16x16x32_bf16 v[4:7], v[212:215], v[92:95], v[4:7]\n"
"v_max3_u32 v249, v249, v94, v95\n"
"v_mfma_f32_16x16x32_bf16 v[8:11], v[216:219], v[92:95], v[8:11]\n"
"v_mfma_f32_16x16x32_bf16 v[12:15], v[220:223], v[92:95], v[12:15]\n"
"s_waitcnt lgkmcnt(0)\n"
"v_cmp_gt_u32 vcc, 4, v250\n"
"s_nop 0\n"
"s_cbranch_vccnz Lcs93_%=\n"
"Lcb94_%=:\n"
"ds_read_b128 v[96:99], v247 offset:0\n"
"ds_read_b128 v[100:103], v247 offset:64\n"
"ds_read_b128 v[104:107], v247 offset:128\n"
"ds_read_b128 v[108:111], v247 offset:192\n"
"v_cmp_eq_u32 vcc, -1, v249\n"
"s_nop 0\n"
"s_cbranch_vccnz Lrs96_%=\n"
"Lrb97_%=:\n"
"s_waitcnt lgkmcnt(0)\n"
"v_fmac_f32 v16, v0, v96\n"
"v_fmac_f32 v17, v1, v97\n"
"v_fmac_f32 v18, v2, v98\n"
"v_fmac_f32 v19, v3, v99\n"
"v_fmac_f32 v20, v4, v100\n"
"v_fmac_f32 v21, v5, v101\n"
"v_fmac_f32 v22, v6, v102\n"
"v_fmac_f32 v23, v7, v103\n"
"v_cvt_pk_bf16_f32 v32, v16, v17\n"
"v_cvt_pk_bf16_f32 v33, v18, v19\n"
"v_cvt_pk_bf16_f32 v34, v20, v21\n"
"v_cvt_pk_bf16_f32 v35, v22, v23\n"
"s_nop 0\n"
"v_permlane16_swap_b32 v32, v34\n"
"v_permlane16_swap_b32 v33, v35\n"
"buffer_store_dwordx4 v[32:35], v248, s[44:47], 0 offen offset:0 sc1\n"
"v_fmac_f32 v24, v8, v104\n"
"v_fmac_f32 v25, v9, v105\n"
"v_fmac_f32 v26, v10, v106\n"
"v_fmac_f32 v27, v11, v107\n"
"v_fmac_f32 v28, v12, v108\n"
"v_fmac_f32 v29, v13, v109\n"
"v_fmac_f32 v30, v14, v110\n"
"v_fmac_f32 v31, v15, v111\n"
"v_cvt_pk_bf16_f32 v36, v24, v25\n"
"v_cvt_pk_bf16_f32 v37, v26, v27\n"
"v_cvt_pk_bf16_f32 v38, v28, v29\n"
"v_cvt_pk_bf16_f32 v39, v30, v31\n"
"s_nop 0\n"
"v_permlane16_swap_b32 v36, v38\n"
"v_permlane16_swap_b32 v37, v39\n"
"buffer_store_dwordx4 v[36:39], v248, s[44:47], 0 offen offset:64 sc1\n"
"s_lshl2_add_u32 s84, s64, s70\n"
"s_cmp_lg_u32 s84, 0\n"
"s_cbranch_scc1 LE98_%=\n"
"s_mov_b64 exec, 1\n"
"v_mov_b32 v80, s71\n"
"buffer_store_dword v80, off, s[48:51], 0 sc1\n"
"s_mov_b64 exec, -1\n"
"LE98_%=:\n"
"s_endpgm\n"
"Lfs5_%=:\n"
"s_mov_b32 s90, 400000\n"
"Lfl7_%=:\n"
"s_sleep 4\n"
"buffer_load_dwordx4 v[192:195], v196, s[48:51], 0 offen offset:0 sc1\n"
"buffer_load_dwordx4 v[176:179], v197, s[48:51], 0 offen offset:0 sc1\n"
"buffer_load_dwordx4 v[180:183], v197, s[48:51], 0 offen offset:1024 sc1\n"
"buffer_load_dwordx4 v[184:187], v197, s[48:51], 0 offen offset:2048 sc1\n"
"s_mov_b32 exec_hi, 0\n"
"buffer_load_dwordx4 v[188:191], v197, s[48:51], 0 offen offset:3072 sc1\n"
"s_mov_b32 exec_hi, -1\n"
"s_waitcnt vmcnt(0)\n"
"s_sub_u32 s90, s90, 1\n"
"s_cbranch_scc1 Lfb6_%=\n"
"v_min3_u32 v164, v177, v179, s71\n"
"v_max3_u32 v165, v177, v179, s71\n"
"v_min3_u32 v164, v181, v183, v164\n"
"v_max3_u32 v165, v181, v183, v165\n"
"v_min3_u32 v164, v185, v187, v164\n"
"v_max3_u32 v165, v185, v187, v165\n"
"v_min3_u32 v164, v189, v191, v164\n"
"v_max3_u32 v165, v189, v191, v165\n"
"v_min3_u32 v164, v193, v195, v164\n"
"v_max3_u32 v165, v193, v195, v165\n"
"v_cmp_ne_u32 vcc, v164, v165\n"
"s_nop 0\n"
"s_cbranch_vccnz Lfl7_%=\n"
"s_branch Lfb6_%=\n"
"Lcs8_%=:\n"
"s_mov_b32 s90, 400000\n"
"Lcl10_%=:\n"
"s_sleep 1\n"
"ds_read_b32 v222, v226 offset:48\n"
"s_waitcnt lgkmcnt(0)\n"
"s_sub_u32 s90, s90, 1\n"
"s_cbranch_scc1 Lcb9_%=\n"
"v_cmp_gt_u32 vcc, s87, v222\n"
"s_nop 0\n"
"s_cbranch_vccnz Lcl10_%=\n"
"s_branch Lcb9_%=\n"
"Lcs11_%=:\n"
"s_mov_b32 s90, 400000\n"
"Lcl13_%=:\n"
"s_sleep 1\n"
"ds_read_b32 v222, v226 offset:0\n"
"s_waitcnt lgkmcnt(0)\n"
"s_sub_u32 s90, s90, 1\n"
"s_cbranch_scc1 Lcb12_%=\n"
"v_cmp_gt_u32 vcc, 4, v222\n"
"s_nop 0\n"
"s_cbranch_vccnz Lcl13_%=\n"
"s_branch Lcb12_%=\n"
"Lcs14_%=:\n"
"s_mov_b32 s90, 400000\n"
"Lcl16_%=:\n"
"s_sleep 1\n"
"ds_read_b32 v222, v226 offset:4\n"
"s_waitcnt lgkmcnt(0)\n"
"s_sub_u32 s90, s90, 1\n"
"s_cbranch_scc1 Lcb15_%=\n"
"v_cmp_gt_u32 vcc, 4, v222\n"
"s_nop 0\n"
"s_cbranch_vccnz Lcl16_%=\n"
"s_branch Lcb15_%=\n"
"Lcs17_%=:\n"
"s_mov_b32 s90, 400000\n"
"Lcl19_%=:\n"
"s_sleep 1\n"
"ds_read_b32 v222, v226 offset:8\n"
"s_waitcnt lgkmcnt(0)\n"
"s_sub_u32 s90, s90, 1\n"
"s_cbranch_scc1 Lcb18_%=\n"
"v_cmp_gt_u32 vcc, 4, v222\n"
"s_nop 0\n"
"s_cbranch_vccnz Lcl19_%=\n"
"s_branch Lcb18_%=\n"
"Lcs20_%=:\n"
"s_mov_b32 s90, 400000\n"
"Lcl22_%=:\n"
"s_sleep 1\n"
"ds_read_b32 v222, v226 offset:72\n"
"s_waitcnt lgkmcnt(0)\n"
"s_sub_u32 s90, s90, 1\n"
"s_cbranch_scc1 Lcb21_%=\n"
"v_cmp_gt_u32 vcc, 4, v222\n"
"s_nop 0\n"
"s_cbranch_vccnz Lcl22_%=\n"
"s_branch Lcb21_%=\n"
"Lcs23_%=:\n"
"s_mov_b32 s90, 400000\n"
"Lcl25_%=:\n"
"s_sleep 1\n"
"ds_read_b32 v222, v226 offset:44\n"
"s_waitcnt lgkmcnt(0)\n"
"s_sub_u32 s90, s90, 1\n"
"s_cbranch_scc1 Lcb24_%=\n"
"v_cmp_gt_u32 vcc, 4, v222\n"
"s_nop 0\n"
"s_cbranch_vccnz Lcl25_%=\n"
"s_branch Lcb24_%=\n"
"Lcs26_%=:\n"
"s_mov_b32 s90, 400000\n"
"Lcl28_%=:\n"
"s_sleep 1\n"
"ds_read_b32 v252, v243 offset:20\n"
"s_waitcnt lgkmcnt(0)\n"
"s_sub_u32 s90, s90, 1\n"
"s_cbranch_scc1 Lcb27_%=\n"
"v_cmp_gt_u32 vcc, 4, v252\n"
"s_nop 0\n"
"s_cbranch_vccnz Lcl28_%=\n"
"s_branch Lcb27_%=\n"
"Lcs29_%=:\n"
"s_mov_b32 s90, 400000\n"
"Lcl31_%=:\n"
"s_sleep 2\n"
"ds_read_b32 v252, v243 offset:56\n"
"s_waitcnt lgkmcnt(0)\n"
"s_sub_u32 s90, s90, 1\n"
"s_cbranch_scc1 Lcb30_%=\n"
"v_cmp_gt_u32 vcc, 4, v252\n"
"s_nop 0\n"
"s_cbranch_vccnz Lcl31_%=\n"
"s_branch Lcb30_%=\n"
"Lns36_%=:\n"
"s_mov_b32 s90, 400000\n"
"Lpl38_%=:\n"
"s_mov_b64 exec, 0x7\n"
"buffer_load_dword v252, v251, s[48:51], 0 offen sc1\n"
"s_waitcnt vmcnt(0)\n"
"v_cmp_ne_u32 vcc, s71, v252\n"
"s_mov_b64 exec, -1\n"
"s_sub_u32 s90, s90, 1\n"
"s_cbranch_scc1 Lpd39_%=\n"
"s_cbranch_vccz Lpd39_%=\n"
"s_sleep 4\n"
"s_branch Lpl38_%=\n"
"Lpd39_%=:\n"
"s_branch Lnb37_%=\n"
"Lns40_%=:\n"
"s_mov_b32 s90, 400000\n"
"Lpl42_%=:\n"
"s_mov_b64 exec, 0x1f\n"
"buffer_load_dword v252, v251, s[48:51], 0 offen sc1\n"
"s_waitcnt vmcnt(0)\n"
"v_cmp_ne_u32 vcc, s71, v252\n"
"s_mov_b64 exec, -1\n"
"s_sub_u32 s90, s90, 1\n"
"s_cbranch_scc1 Lpd43_%=\n"
"s_cbranch_vccz Lpd43_%=\n"
"s_sleep 4\n"
"s_branch Lpl42_%=\n"
"Lpd43_%=:\n"
"s_branch Lnb41_%=\n"
"Lcs44_%=:\n"
"s_mov_b32 s90, 400000\n"
"Lcl46_%=:\n"
"s_sleep 1\n"
"ds_read_b32 v252, v243 offset:28\n"
"s_waitcnt lgkmcnt(0)\n"
"s_sub_u32 s90, s90, 1\n"
"s_cbranch_scc1 Lcb45_%=\n"
"v_cmp_gt_u32 vcc, 4, v252\n"
"s_nop 0\n"
"s_cbranch_vccnz Lcl46_%=\n"
"s_branch Lcb45_%=\n"
"Lcs47_%=:\n"
"s_mov_b32 s90, 400000\n"
"Lcl49_%=:\n"
"s_sleep 1\n"
"ds_read_b32 v252, v243 offset:24\n"
"s_waitcnt lgkmcnt(0)\n"
"s_sub_u32 s90, s90, 1\n"
"s_cbranch_scc1 Lcb48_%=\n"
"v_cmp_gt_u32 vcc, 4, v252\n"
"s_nop 0\n"
"s_cbranch_vccnz Lcl49_%=\n"
"s_branch Lcb48_%=\n"
"Lcs55_%=:\n"
"s_mov_b32 s90, 400000\n"
"Lcl57_%=:\n"
"s_sleep 1\n"
"ds_read_b32 v240, v251 offset:60\n"
"s_waitcnt lgkmcnt(0)\n"
"s_sub_u32 s90, s90, 1\n"
"s_cbranch_scc1 Lcb56_%=\n"
"v_cmp_gt_u32 vcc, 4, v240\n"
"s_nop 0\n"
"s_cbranch_vccnz Lcl57_%=\n"
"s_branch Lcb56_%=\n"
"Lcs59_%=:\n"
"s_mov_b32 s90, 400000\n"
"Lcl61_%=:\n"
"s_sleep 1\n"
"ds_read_b32 v244, v251 offset:72\n"
"s_waitcnt lgkmcnt(0)\n"
"s_sub_u32 s90, s90, 1\n"
"s_cbranch_scc1 Lcb60_%=\n"
"v_cmp_gt_u32 vcc, 4, v244\n"
"s_nop 0\n"
"s_cbranch_vccnz Lcl61_%=\n"
"s_branch Lcb60_%=\n"
"Lcs63_%=:\n"
"s_mov_b32 s90, 400000\n"
"Lcl65_%=:\n"
"s_sleep 1\n"
"ds_read_b32 v240, v251 offset:60\n"
"s_waitcnt lgkmcnt(0)\n"
"s_sub_u32 s90, s90, 1\n"
"s_cbranch_scc1 Lcb64_%=\n"
"v_cmp_gt_u32 vcc, 4, v240\n"
"s_nop 0\n"
"s_cbranch_vccnz Lcl65_%=\n"
"s_branch Lcb64_%=\n"
"Lcs71_%=:\n"
"s_mov_b32 s90, 400000\n"
"Lcl73_%=:\n"
"s_sleep 1\n"
"ds_read_b32 v243, v251 offset:4\n"
"s_waitcnt lgkmcnt(0)\n"
"s_sub_u32 s90, s90, 1\n"
"s_cbranch_scc1 Lcb72_%=\n"
"v_cmp_gt_u32 vcc, 4, v243\n"
"s_nop 0\n"
"s_cbranch_vccnz Lcl73_%=\n"
"s_branch Lcb72_%=\n"
"Lns74_%=:\n"
"s_mov_b32 s90, 400000\n"
"Lpl76_%=:\n"
"s_mov_b64 exec, 0xfff\n"
"buffer_load_dword v242, v228, s[48:51], 0 offen sc1\n"
"s_waitcnt vmcnt(0)\n"
"v_cmp_ne_u32 vcc, s71, v242\n"
"s_mov_b64 exec, -1\n"
"s_sub_u32 s90, s90, 1\n"
"s_cbranch_scc1 Lpd77_%=\n"
"s_cbranch_vccz Lpd77_%=\n"
"s_sleep 1\n"
"s_branch Lpl76_%=\n"
"Lpd77_%=:\n"
"s_branch Lnb75_%=\n"
"Lcs78_%=:\n"
"s_mov_b32 s90, 400000\n"
"Lcl80_%=:\n"
"s_sleep 0\n"
"ds_read_b32 v243, v251 offset:36\n"
"s_waitcnt lgkmcnt(0)\n"
"s_sub_u32 s90, s90, 1\n"
"s_cbranch_scc1 Lcb79_%=\n"
"v_cmp_gt_u32 vcc, 4, v243\n"
"s_nop 0\n"
"s_cbranch_vccnz Lcl80_%=\n"
"s_branch Lcb79_%=\n"
"Lcs81_%=:\n"
"s_mov_b32 s90, 400000\n"
"Lcl83_%=:\n"
"s_sleep 1\n"
"ds_read_b32 v243, v251 offset:40\n"
"s_waitcnt lgkmcnt(0)\n"
"s_sub_u32 s90, s90, 1\n"
"s_cbranch_scc1 Lcb82_%=\n"
"v_cmp_gt_u32 vcc, 4, v243\n"
"s_nop 0\n"
"s_cbranch_vccnz Lcl83_%=\n"
"s_branch Lcb82_%=\n"
"Lfb84_%=:\n"
"s_mov_b32 exec_lo, 0\n"
"v_mov_b32 v142, 0\n"
"s_mov_b32 exec_lo, -1\n"
"v_mov_b32 v244, 0xff800000\n"
"s_add_u32 s84, s16, 1048576\n"
"s_add_u32 s85, s84, 262144\n"
"s_mov_b32 s86, s84\n"
"Lf186_%=:\n"
"buffer_load_dwordx4 v[144:147], v253, s[48:51], s86 offen offset:0 sc1\n"
"buffer_load_dwordx4 v[148:151], v253, s[48:51], s86 offen offset:1024 sc1\n"
"s_waitcnt vmcnt(0)\n"
"v_mfma_f32_32x32x16_bf16 v[64:79], v[144:147], v[136:139], 0\n"
"v_mfma_f32_32x32x16_bf16 v[64:79], v[148:151], v[140:143], v[64:79]\n"
"s_nop 11\n"
"v_max3_f32 v244, v64, v65, v244\n"
"v_max3_f32 v245, v66, v67, v68\n"
"v_max3_f32 v244, v244, v69, v70\n"
"v_max3_f32 v245, v245, v71, v72\n"
"v_max3_f32 v244, v244, v73, v74\n"
"v_max3_f32 v245, v245, v75, v76\n"
"v_max3_f32 v244, v244, v77, v78\n"
"v_max3_f32 v244, v244, v245, v79\n"
"s_add_u32 s86, s86, 4096\n"
"s_cmp_lt_u32 s86, s85\n"
"s_cbranch_scc1 Lf186_%=\n"
"v_mov_b32 v245, v244\n"
"s_nop 1\n"
"v_permlane32_swap_b32 v244, v245\n"
"v_max_f32 v244, v244, v245\n"
"v_mov_b32 v48, 0\n"
"v_mov_b32 v49, 0\n"
"v_mov_b32 v50, 0\n"
"v_mov_b32 v51, 0\n"
"v_mov_b32 v52, 0\n"
"v_mov_b32 v53, 0\n"
"v_mov_b32 v54, 0\n"
"v_mov_b32 v55, 0\n"
"v_mov_b32 v56, 0\n"
"v_mov_b32 v57, 0\n"
"v_mov_b32 v58, 0\n"
"v_mov_b32 v59, 0\n"
"v_mov_b32 v60, 0\n"
"v_mov_b32 v61, 0\n"
"v_mov_b32 v62, 0\n"
"v_mov_b32 v63, 0\n"
"Lf287_%=:\n"
"buffer_load_dwordx4 v[144:147], v253, s[48:51], s84 offen offset:0 sc1\n"
"buffer_load_dwordx4 v[148:151], v253, s[48:51], s84 offen offset:1024 sc1\n"
"buffer_load_dwordx4 v[152:155], v253, s[48:51], s84 offen offset:2048 sc1\n"
"buffer_load_dwordx4 v[156:159], v253, s[48:51], s84 offen offset:3072 sc1\n"
"s_waitcnt vmcnt(0)\n"
"v_mfma_f32_32x32x16_bf16 v[64:79], v[144:147], v[136:139], 0\n"
"v_mfma_f32_32x32x16_bf16 v[64:79], v[148:151], v[140:143], v[64:79]\n"
"s_nop 11\n"
"v_sub_f32 v64, v64, v244\n"
"v_sub_f32 v65, v65, v244\n"
"v_sub_f32 v66, v66, v244\n"
"v_sub_f32 v67, v67, v244\n"
"v_sub_f32 v68, v68, v244\n"
"v_sub_f32 v69, v69, v244\n"
"v_sub_f32 v70, v70, v244\n"
"v_sub_f32 v71, v71, v244\n"
"v_sub_f32 v72, v72, v244\n"
"v_sub_f32 v73, v73, v244\n"
"v_sub_f32 v74, v74, v244\n"
"v_sub_f32 v75, v75, v244\n"
"v_sub_f32 v76, v76, v244\n"
"v_sub_f32 v77, v77, v244\n"
"v_sub_f32 v78, v78, v244\n"
"v_sub_f32 v79, v79, v244\n"
"v_exp_f32 v64, v64\n"
"v_exp_f32 v65, v65\n"
"v_exp_f32 v66, v66\n"
"v_exp_f32 v67, v67\n"
"v_exp_f32 v68, v68\n"
"v_exp_f32 v69, v69\n"
"v_exp_f32 v70, v70\n"
"v_exp_f32 v71, v71\n"
"v_exp_f32 v72, v72\n"
"v_exp_f32 v73, v73\n"
"v_exp_f32 v74, v74\n"
"v_exp_f32 v75, v75\n"
"v_exp_f32 v76, v76\n"
"v_exp_f32 v77, v77\n"
"v_exp_f32 v78, v78\n"
"v_exp_f32 v79, v79\n"
"v_smfmac_f32_32x32x32_bf16 v[48:63], v[152:155], v[64:71], v254\n"
"s_nop 0\n"
"v_smfmac_f32_32x32x32_bf16 v[48:63], v[156:159], v[72:79], v254\n"
"s_add_u32 s84, s84, 4096\n"
"s_cmp_lt_u32 s84, s85\n"
"s_cbranch_scc1 Lf287_%=\n"
"s_nop 8\n"
"v_mov_b32 v240, v60\n"
"v_mov_b32 v241, v60\n"
"v_add_f32_e64 v242, |v48|, |v49|\n"
"v_add_f32_e64 v243, |v60|, |v61|\n"
"v_permlane32_swap_b32 v240, v241\n"
"v_rcp_f32 v240, v241\n"
"s_nop 0\n"
"v_mul_f32 v0, v240, v48\n"
"v_mul_f32 v1, v240, v49\n"
"v_mul_f32 v2, v240, v50\n"
"v_mul_f32 v3, v240, v51\n"
"v_mul_f32 v4, v240, v52\n"
"v_mul_f32 v5, v240, v53\n"
"v_mul_f32 v6, v240, v54\n"
"v_mul_f32 v7, v240, v55\n"
"v_mul_f32 v8, v240, v56\n"
"v_mul_f32 v9, v240, v57\n"
"v_mul_f32 v10, v240, v58\n"
"v_mul_f32 v11, v240, v59\n"
"v_mul_f32 v12, v240, v60\n"
"v_mul_f32 v13, v240, v61\n"
"v_mul_f32 v14, v240, v62\n"
"v_mul_f32 v15, v240, v63\n"
"v_cvt_pk_bf16_f32 v16, v0, v1\n"
"v_cvt_pk_bf16_f32 v17, v2, v3\n"
"v_cvt_pk_bf16_f32 v18, v4, v5\n"
"v_cvt_pk_bf16_f32 v19, v6, v7\n"
"v_cvt_pk_bf16_f32 v20, v8, v9\n"
"v_cvt_pk_bf16_f32 v21, v10, v11\n"
"v_cvt_pk_bf16_f32 v22, v12, v13\n"
"v_cvt_pk_bf16_f32 v23, v14, v15\n"
"v_min_u32 v16, -2, v16\n"
"v_min_u32 v17, -2, v17\n"
"v_min_u32 v18, -2, v18\n"
"v_min_u32 v19, -2, v19\n"
"v_min_u32 v20, -2, v20\n"
"v_min_u32 v21, -2, v21\n"
"v_min_u32 v22, -2, v22\n"
"v_min_u32 v23, -2, v23\n"
"s_branch Lfk85_%=\n"
"Lcs93_%=:\n"
"s_mov_b32 s90, 400000\n"
"Lcl95_%=:\n"
"s_sleep 1\n"
"ds_read_b32 v250, v251 offset:32\n"
"s_waitcnt lgkmcnt(0)\n"
"s_sub_u32 s90, s90, 1\n"
"s_cbranch_scc1 Lcb94_%=\n"
"v_cmp_gt_u32 vcc, 4, v250\n"
"s_nop 0\n"
"s_cbranch_vccnz Lcl95_%=\n"
"s_branch Lcb94_%=\n"
"Lrs96_%=:\n"
"s_waitcnt vmcnt(0) lgkmcnt(0)\n"
"s_cmp_gt_u32 s18, 400000\n"
"s_cbranch_scc1 Lrb97_%=\n"
"ds_read_b64_tr_b16 v[96:97], v241 offset:0\n"
"ds_read_b64_tr_b16 v[98:99], v241 offset:1024\n"
"ds_read_b64_tr_b16 v[100:101], v242 offset:0\n"
"ds_read_b64_tr_b16 v[102:103], v242 offset:1024\n"
"ds_read_b64_tr_b16 v[104:105], v243 offset:0\n"
"ds_read_b64_tr_b16 v[106:107], v243 offset:1024\n"
"ds_read_b64_tr_b16 v[108:109], v244 offset:0\n"
"ds_read_b64_tr_b16 v[110:111], v244 offset:1024\n"
"ds_read_b64_tr_b16 v[112:113], v243 offset:3584\n"
"ds_read_b64_tr_b16 v[114:115], v243 offset:4608\n"
"ds_read_b64_tr_b16 v[116:117], v244 offset:3584\n"
"ds_read_b64_tr_b16 v[118:119], v244 offset:4608\n"
"ds_read_b64_tr_b16 v[120:121], v241 offset:3584\n"
"ds_read_b64_tr_b16 v[122:123], v241 offset:4608\n"
"ds_read_b64_tr_b16 v[124:125], v242 offset:3584\n"
"ds_read_b64_tr_b16 v[126:127], v242 offset:4608\n"
"ds_read_b64_tr_b16 v[128:129], v241 offset:7168\n"
"ds_read_b64_tr_b16 v[130:131], v241 offset:8192\n"
"ds_read_b64_tr_b16 v[132:133], v242 offset:7168\n"
"ds_read_b64_tr_b16 v[134:135], v242 offset:8192\n"
"ds_read_b64_tr_b16 v[136:137], v243 offset:7168\n"
"ds_read_b64_tr_b16 v[138:139], v243 offset:8192\n"
"ds_read_b64_tr_b16 v[140:141], v244 offset:7168\n"
"ds_read_b64_tr_b16 v[142:143], v244 offset:8192\n"
"ds_read_b64_tr_b16 v[144:145], v243 offset:10752\n"
"ds_read_b64_tr_b16 v[146:147], v243 offset:11776\n"
"ds_read_b64_tr_b16 v[148:149], v244 offset:10752\n"
"ds_read_b64_tr_b16 v[150:151], v244 offset:11776\n"
"ds_read_b64_tr_b16 v[152:153], v241 offset:10752\n"
"ds_read_b64_tr_b16 v[154:155], v241 offset:11776\n"
"ds_read_b64_tr_b16 v[156:157], v242 offset:10752\n"
"ds_read_b64_tr_b16 v[158:159], v242 offset:11776\n"
"ds_read_b64_tr_b16 v[160:161], v241 offset:14336\n"
"ds_read_b64_tr_b16 v[162:163], v241 offset:15360\n"
"ds_read_b64_tr_b16 v[164:165], v242 offset:14336\n"
"ds_read_b64_tr_b16 v[166:167], v242 offset:15360\n"
"ds_read_b64_tr_b16 v[168:169], v243 offset:14336\n"
"ds_read_b64_tr_b16 v[170:171], v243 offset:15360\n"
"ds_read_b64_tr_b16 v[172:173], v244 offset:14336\n"
"ds_read_b64_tr_b16 v[174:175], v244 offset:15360\n"
"ds_read_b64_tr_b16 v[176:177], v243 offset:17920\n"
"ds_read_b64_tr_b16 v[178:179], v243 offset:18944\n"
"ds_read_b64_tr_b16 v[180:181], v244 offset:17920\n"
"ds_read_b64_tr_b16 v[182:183], v244 offset:18944\n"
"ds_read_b64_tr_b16 v[184:185], v241 offset:17920\n"
"ds_read_b64_tr_b16 v[186:187], v241 offset:18944\n"
"ds_read_b64_tr_b16 v[188:189], v242 offset:17920\n"
"ds_read_b64_tr_b16 v[190:191], v242 offset:18944\n"
"ds_read_b64_tr_b16 v[192:193], v241 offset:21504\n"
"ds_read_b64_tr_b16 v[194:195], v241 offset:22528\n"
"ds_read_b64_tr_b16 v[196:197], v242 offset:21504\n"
"ds_read_b64_tr_b16 v[198:199], v242 offset:22528\n"
"ds_read_b64_tr_b16 v[200:201], v243 offset:21504\n"
"ds_read_b64_tr_b16 v[202:203], v243 offset:22528\n"
"ds_read_b64_tr_b16 v[204:205], v244 offset:21504\n"
"ds_read_b64_tr_b16 v[206:207], v244 offset:22528\n"
"ds_read_b64_tr_b16 v[208:209], v243 offset:25088\n"
"ds_read_b64_tr_b16 v[210:211], v243 offset:26112\n"
"ds_read_b64_tr_b16 v[212:213], v244 offset:25088\n"
"ds_read_b64_tr_b16 v[214:215], v244 offset:26112\n"
"ds_read_b64_tr_b16 v[216:217], v241 offset:25088\n"
"ds_read_b64_tr_b16 v[218:219], v241 offset:26112\n"
"ds_read_b64_tr_b16 v[220:221], v242 offset:25088\n"
"ds_read_b64_tr_b16 v[222:223], v242 offset:26112\n"
"s_waitcnt vmcnt(0) lgkmcnt(0)\n"
"s_branch LRE90_%=\n"
"LEND50_%=:\n"
        :
        : "s"(__builtin_amdgcn_kernarg_segment_ptr()), "s"(blockIdx.x), "{v0}"(tid),
          "s"((unsigned)(size_t)(__attribute__((address_space(3))) char*)lds)
        : "memory", "vcc", "scc", "v1","v2","v3","v4","v5","v6","v7","v8","v9","v10","v11","v12","v13","v14","v15","v16","v17","v18","v19","v20","v21","v22","v23","v24","v25","v26","v27","v28","v29","v30","v31","v32","v33","v34","v35","v36","v37","v38","v39","v40","v41","v42","v43","v44","v45","v46","v47","v48","v49","v50","v51","v52","v53","v54","v55","v56","v57","v58","v59","v60","v61","v62","v63","v64","v65","v66","v67","v68","v69","v70","v71","v72","v73","v74","v75","v76","v77","v78","v79","v80","v81","v82","v83","v84","v85","v86","v87","v88","v89","v90","v91","v92","v93","v94","v95","v96","v97","v98","v99","v100","v101","v102","v103","v104","v105","v106","v107","v108","v109","v110","v111","v112","v113","v114","v115","v116","v117","v118","v119","v120","v121","v122","v123","v124","v125","v126","v127","v128","v129","v130","v131","v132","v133","v134","v135","v136","v137","v138","v139","v140","v141","v142","v143","v144","v145","v146","v147","v148","v149","v150","v151","v152","v153","v154","v155","v156","v157","v158","v159","v160","v161","v162","v163","v164","v165","v166","v167","v168","v169","v170","v171","v172","v173","v174","v175","v176","v177","v178","v179","v180","v181","v182","v183","v184","v185","v186","v187","v188","v189","v190","v191","v192","v193","v194","v195","v196","v197","v198","v199","v200","v201","v202","v203","v204","v205","v206","v207","v208","v209","v210","v211","v212","v213","v214","v215","v216","v217","v218","v219","v220","v221","v222","v223","v224","v225","v226","v227","v228","v229","v230","v231","v232","v233","v234","v235","v236","v237","v238","v239","v240","v241","v242","v243","v244","v245","v246","v247","v248","v249","v250","v251","v252","v253","v254","v255","s4","s5","s6","s7","s8","s9","s10","s11","s12","s13","s14","s15","s16","s17","s18","s19","s20","s21","s22","s23","s24","s25","s26","s27","s28","s29","s30","s31","s33","s34","s35","s36","s37","s38","s39","s40","s41","s42","s43","s44","s45","s46","s47","s48","s49","s50","s51","s52","s53","s54","s55","s56","s57","s58","s59","s60","s61","s62","s63","s64","s65","s66","s67","s68","s69","s70","s71","s72","s73","s74","s75","s76","s77","s78","s79","s80","s81","s82","s83","s84","s85","s86","s87","s88","s89","s90","s91","s92","s93","s94","s95","s96","s97","s98","s99");
}

extern "C" void kernel_launch(void* const* d_in, const int* in_sizes, int n_in, void* d_out, int out_size, void* d_ws,
                              size_t ws_size, hipStream_t stream) {
    const unsigned F = 0x20000u;
    Args a;
    a.x = Desc{d_in[0], 2048u * 896u, F};
    a.wqkv = Desc{d_in[1], 448u * 2688u, F};
    a.w1 = Desc{d_in[4], 448u * 3584u, F};
    a.w3 = Desc{d_in[5], 448u * 3584u, F};
    a.w2 = Desc{d_in[6], 1792u * 896u, F};
    a.wo = Desc{d_in[2], 448u * 896u, F};
    a.out = Desc{d_out, 2048u * 896u, F};
    a.ws = Desc{d_ws, 0x1200000u, F};
    fused_block<<<256, 512, 0, stream>>>(a);
}
```
